```python
import torch

def _softmax(x, axis=-1):
    return torch.softmax(x, dim=axis)
def _gelu(x):
    return torch.nn.functional.gelu(x, approximate="tanh")
def _silu(x):
    return torch.nn.functional.silu(x)
def _sigmoid(x):
    return torch.sigmoid(x)
def _layernorm(x, axis=-1, eps=1e-5):
    m = x.mean(dim=axis, keepdim=True)
    v = x.var(dim=axis, keepdim=True, unbiased=False)
    return (x - m) / torch.sqrt(v + eps)
def _scatter_add(tgt, idx, src, axis=0):
    return tgt.clone().index_add_(axis, idx, src)
def _segment_sum(src, seg, n):
    out = torch.zeros((n,) + src.shape[1:], dtype=src.dtype)
    return out.index_add_(0, seg, src)
def _pad(x, pw):
    flat = tuple(v for pair in reversed(pw) for v in pair)
    return torch.nn.functional.pad(x, flat)

kernel_name = 'concat_heads__grouped_kv_mqa__compressed_kv_cla__prompt'

def setup_inputs():

    _w = (lambda a: (lambda t: t.to(torch.bfloat16) if t.is_floating_point() else t)(torch.as_tensor(a)))
    def _A_setup():
        B, S, D, H = 1, 2048, 576, 32
        Dh = D // H
        return {
            "x": _w((lambda *s: torch.randn(*s, dtype=torch.bfloat16) * 0.05)(B, S, D)),
            "wq": _w((lambda *s: torch.randn(*s, dtype=torch.bfloat16) * 0.8)(D, H * Dh)),
            "wk": _w((lambda *s: torch.randn(*s, dtype=torch.bfloat16) * 0.8)(D, Dh)),
            "wv": _w((lambda *s: torch.randn(*s, dtype=torch.bfloat16) * 0.05)(D, Dh)),
            "wo": _w((lambda *s: torch.randn(*s, dtype=torch.bfloat16) * 0.05)(H * Dh, D)),
        }
    _A_in = _A_setup()

    def _B_setup():
        B, H, Sq, Sk, D, M = 1, 32, 2048, 2048, 128, 576
        Dc = 4 * D
        return {
            "x": _w((lambda *s: torch.randn(*s, dtype=torch.bfloat16) * 3.2)(B, Sq, M)),
            "c_kv": _w((lambda *s: torch.randn(*s, dtype=torch.bfloat16) * 0.1)(B, Sk, Dc)),
            "w_q": _w((lambda *s: torch.randn(*s, dtype=torch.bfloat16) * 6.4)(M, H, D) * (M ** -0.5)),
            "w_uk": _w((lambda *s: torch.randn(*s, dtype=torch.bfloat16) * 6.4)(Dc, H, D) * (Dc ** -0.5)),
            "w_uv": _w((lambda *s: torch.randn(*s, dtype=torch.bfloat16) * 0.1)(Dc, H, D) * (Dc ** -0.5)),
            "w_o": _w((lambda *s: torch.randn(*s, dtype=torch.bfloat16) * 0.1)(H, D, M) * ((H * D) ** -0.5)),
        }
    _B_in = _B_setup()

    return {
        "A_x": _A_in["x"],
        "A_wq": _A_in["wq"],
        "A_wk": _A_in["wk"],
        "A_wv": _A_in["wv"],
        "A_wo": _A_in["wo"],
        "B_x": _B_in["x"],
        "B_c_kv": _B_in["c_kv"],
        "B_w_q": _B_in["w_q"],
        "B_w_uk": _B_in["w_uk"],
        "B_w_uv": _B_in["w_uv"],
        "B_w_o": _B_in["w_o"],
    }

def reference(A_x, A_wq, A_wk, A_wv, A_wo, B_x, B_c_kv, B_w_q, B_w_uk, B_w_uv, B_w_o):
    def _A_ref(x, wq, wk, wv, wo):
        B, S, D = x.shape
        H = 32
        Dh = D // H
        q = (lambda x, *d: x.permute(*d))((x @ wq).reshape(B, S, H, Dh), 0, 2, 1, 3)
        k = x @ wk
        v = x @ wv
        scores = torch.einsum("bhqd,bkd->bhqk", q, k) * (Dh ** -0.5)
        p = _softmax(scores, axis=-1)
        o = torch.einsum("bhqk,bkd->bhqd", p, v)
        return (lambda x, *d: x.permute(*d))(o, 0, 2, 1, 3).reshape(B, S, H * Dh) @ wo

    def _B_ref(x, c_kv, w_q, w_uk, w_uv, w_o):
        B, Sq, M = x.shape[0], x.shape[1], x.shape[2]
        Sk = c_kv.shape[1]
        H, D = w_q.shape[1], w_q.shape[2]
        q = torch.einsum("bqm,mhd->bhqd", x, w_q)
        k = torch.einsum("bkc,chd->bhkd", c_kv, w_uk)
        v = torch.einsum("bkc,chd->bhkd", c_kv, w_uv)
        scores = torch.einsum("bhqd,bhkd->bhqk", q, k) * (D ** -0.5)
        off = Sk - Sq
        qi = torch.arange(Sq)[:, None]
        kj = torch.arange(Sk)[None, :]
        cmask = kj <= (qi + off)
        scores = torch.where(cmask[None, None], scores, -1e9)
        p = _softmax(scores, axis=-1)
        ctx = torch.einsum("bhqk,bhkd->bhqd", p, v)
        return torch.einsum("bhqd,hdm->bqm", ctx, w_o)

    a = _A_ref(A_x, A_wq, A_wk, A_wv, A_wo)
    b = _B_ref(B_x, B_c_kv, B_w_q, B_w_uk, B_w_uv, B_w_o)
    return torch.concatenate([a, b], axis=-1)
```

```cpp
#include <hip/hip_runtime.h>
#include <hip/hip_bf16.h>
typedef __hip_bfloat16 bf16;
__device__ __forceinline__ float b2f(bf16 x){ return __bfloat162float(x);}
__device__ __forceinline__ bf16 f2b(float x){ return __float2bfloat16(x);}

__global__ void gemm_naive(const bf16* A, int lda, const bf16* B, int ldb, bf16* C, int ldc, int M, int N, int K){
  __shared__ float As[16][17], Bs[16][17];
  int tx=threadIdx.x, ty=threadIdx.y;
  int row=blockIdx.y*16+ty, col=blockIdx.x*16+tx;
  float acc=0;
  for(int k0=0;k0<K;k0+=16){
    As[ty][tx]=(row<M && k0+tx<K)? b2f(A[(size_t)row*lda+k0+tx]):0.f;
    Bs[ty][tx]=(col<N && k0+ty<K)? b2f(B[(size_t)(k0+ty)*ldb+col]):0.f;
    __syncthreads();
    for(int k=0;k<16;k++) acc+=As[ty][k]*Bs[k][tx];
    __syncthreads();
  }
  if(row<M&&col<N) C[(size_t)row*ldc+col]=f2b(acc);
}
__global__ void attn_naive(const bf16* Q, int ldq, const bf16* Kp, int ldk, int khs, const bf16* V, bf16* O, int S, int Dh, float scale, int causal){
  int h=blockIdx.y, q=blockIdx.x, lane=threadIdx.x;
  __shared__ float qs[128]; __shared__ float p[2048]; __shared__ float red[64];
  for(int d=lane; d<Dh; d+=64) qs[d]=b2f(Q[(size_t)q*ldq+h*Dh+d]);
  __syncthreads();
  int nk = causal? q+1 : S;
  float mx=-1e30f;
  for(int k=lane;k<nk;k+=64){ float s=0; const bf16* kp=Kp+(size_t)k*ldk+h*khs; for(int d=0;d<Dh;d++) s+=qs[d]*b2f(kp[d]); s*=scale; p[k]=s; mx=fmaxf(mx,s);}
  red[lane]=mx; __syncthreads();
  mx=-1e30f; for(int i=0;i<64;i++) mx=fmaxf(mx,red[i]); __syncthreads();
  float sum=0; for(int k=lane;k<nk;k+=64){ float e=__expf(p[k]-mx); p[k]=e; sum+=e;}
  red[lane]=sum; __syncthreads();
  sum=0; for(int i=0;i<64;i++) sum+=red[i];
  for(int d=lane; d<Dh; d+=64){ float o=0; for(int k=0;k<nk;k++) o+=p[k]*b2f(V[(size_t)k*ldk+h*khs+d]); O[(size_t)q*ldq+h*Dh+d]=f2b(o/sum);}
}
extern "C" void kernel_launch(void* const* d_in, const int* in_sizes, int n_in, void* d_out, int out_size, void* d_ws, size_t ws_size, hipStream_t stream){
  const bf16* Ax=(const bf16*)d_in[0]; const bf16* Awq=(const bf16*)d_in[1]; const bf16* Awk=(const bf16*)d_in[2]; const bf16* Awv=(const bf16*)d_in[3]; const bf16* Awo=(const bf16*)d_in[4];
  const bf16* Bx=(const bf16*)d_in[5]; const bf16* Bc=(const bf16*)d_in[6]; const bf16* Bwq=(const bf16*)d_in[7]; const bf16* Bwuk=(const bf16*)d_in[8]; const bf16* Bwuv=(const bf16*)d_in[9]; const bf16* Bwo=(const bf16*)d_in[10];
  bf16* out=(bf16*)d_out; bf16* ws=(bf16*)d_ws;
  const int S=2048;
  bf16* qa=ws; bf16* ka=qa+S*576; bf16* va=ka+S*18; bf16* oa=va+S*18+64;
  bf16* qb=oa+S*576; bf16* kb=qb+(size_t)S*4096; bf16* vb=kb+(size_t)S*4096; bf16* cb=vb+(size_t)S*4096;
  dim3 blk(16,16);
  gemm_naive<<<dim3(36,128),blk,0,stream>>>(Ax,576,Awq,576,qa,576,S,576,576);
  gemm_naive<<<dim3(2,128),blk,0,stream>>>(Ax,576,Awk,18,ka,18,S,18,576);
  gemm_naive<<<dim3(2,128),blk,0,stream>>>(Ax,576,Awv,18,va,18,S,18,576);
  attn_naive<<<dim3(S,32),64,0,stream>>>(qa,576,ka,18,0,va,oa,S,18,0.23570226039551584f,0);
  gemm_naive<<<dim3(36,128),blk,0,stream>>>(oa,576,Awo,576,out,1152,S,576,576);
  gemm_naive<<<dim3(256,128),blk,0,stream>>>(Bx,576,Bwq,4096,qb,4096,S,4096,576);
  gemm_naive<<<dim3(256,128),blk,0,stream>>>(Bc,512,Bwuk,4096,kb,4096,S,4096,512);
  gemm_naive<<<dim3(256,128),blk,0,stream>>>(Bc,512,Bwuv,4096,vb,4096,S,4096,512);
  attn_naive<<<dim3(S,32),64,0,stream>>>(qb,4096,kb,4096,128,vb,cb,S,128,0.08838834764831845f,1);
  gemm_naive<<<dim3(36,128),blk,0,stream>>>(cb,4096,Bwo,576,out+576,1152,S,576,4096);
}
```

```cpp
#include <hip/hip_runtime.h>
#include <hip/hip_bf16.h>
#include <cstdio>
#include <cstdint>
#include <utility>

#define ST_AUX 0
#define DI __device__ __forceinline__
typedef short bf16x8 __attribute__((ext_vector_type(8)));
typedef short s16x4 __attribute__((ext_vector_type(4)));
typedef float f32x16 __attribute__((ext_vector_type(16)));
typedef float f32x4 __attribute__((ext_vector_type(4)));
typedef float f32x2 __attribute__((ext_vector_type(2)));
typedef __bf16 bf16x2_t __attribute__((ext_vector_type(2)));
typedef unsigned u32x4 __attribute__((ext_vector_type(4)));
typedef unsigned u32x2 __attribute__((ext_vector_type(2)));
typedef __attribute__((address_space(3))) const char* lds_cptr;
typedef __attribute__((address_space(3))) char* lds_ptr;
#define SBAR() __builtin_amdgcn_sched_barrier(0)
#define PIN(x) asm volatile("" : "+v"(x))
#define MFMA(a, b, c) __builtin_amdgcn_mfma_f32_32x32x16_bf16(a, b, c, 0, 0, 0)
#define SRD_W3 0x00020000

DI unsigned rfl(unsigned x) { return __builtin_amdgcn_readfirstlane(x); }
DI unsigned cvtpk(float lo, float hi) { f32x2 v = {lo, hi}; bf16x2_t b = __builtin_convertvector(v, bf16x2_t); return __builtin_bit_cast(unsigned, b); }
DI void dma16(unsigned m0, unsigned voff, __amdgpu_buffer_rsrc_t srd, unsigned soff) {
  asm volatile("s_mov_b32 m0, %0\n\ts_nop 0\n\tbuffer_load_dwordx4 %1, %2, %3 offen lds" :: "s"(m0), "v"(voff), "s"(srd), "s"(soff) : "m0", "memory");
}
template <int OFF> DI void dma16o(unsigned m0, unsigned voff, __amdgpu_buffer_rsrc_t srd, unsigned soff) {
  asm volatile("s_mov_b32 m0, %0\n\ts_nop 0\n\tbuffer_load_dwordx4 %1, %2, %3 offen offset:%4 lds" :: "s"(m0), "v"(voff), "s"(srd), "s"(soff), "i"(OFF) : "m0", "memory");
}
DI void dma4(unsigned m0, unsigned voff, __amdgpu_buffer_rsrc_t srd, unsigned soff) {
  asm volatile("s_mov_b32 m0, %0\n\ts_nop 0\n\tbuffer_load_dword %1, %2, %3 offen lds" :: "s"(m0), "v"(voff), "s"(srd), "s"(soff) : "m0", "memory");
}
DI __amdgpu_buffer_rsrc_t mksrd(const void* p, unsigned bytes) { return __builtin_amdgcn_make_buffer_rsrc((void*)p, (short)0, (int)bytes, SRD_W3); }
DI bf16x8 bld16(__amdgpu_buffer_rsrc_t srd, unsigned voff, unsigned soff) { return __builtin_bit_cast(bf16x8, __builtin_amdgcn_raw_buffer_load_b128(srd, voff, soff, 0)); }
DI void bst16(bf16x8 v, __amdgpu_buffer_rsrc_t srd, unsigned voff, unsigned soff) { __builtin_amdgcn_raw_buffer_store_b128(__builtin_bit_cast(u32x4, v), srd, voff + soff, 0, ST_AUX); }
template <int N> DI void wait_bar() { asm volatile("s_waitcnt vmcnt(%0)\n\ts_barrier" :: "i"(N) : "memory"); }
DI bf16x8 lds_rd16(lds_cptr p) { return *(const __attribute__((address_space(3))) bf16x8*)p; }
DI s16x4 lds_tr(lds_cptr p) { return __builtin_bit_cast(s16x4, __builtin_amdgcn_ds_read_tr16_b64_v4i16((__attribute__((address_space(3))) s16x4*)p)); }
DI bf16x8 cat(s16x4 a, s16x4 b) { return __builtin_shufflevector(a, b, 0, 1, 2, 3, 4, 5, 6, 7); }
DI bf16x8 pack8(const f32x16& x, int g, float sc) {
  u32x4 w;
#pragma unroll
  for (int e = 0; e < 4; ++e) w[e] = cvtpk(x[8 * g + 2 * e] * sc, x[8 * g + 2 * e + 1] * sc);
  return __builtin_bit_cast(bf16x8, w);
}
DI bf16x8 pack8n(const f32x16& x, int g) {
  u32x4 w;
#pragma unroll
  for (int e = 0; e < 4; ++e) w[e] = cvtpk(x[8 * g + 2 * e], x[8 * g + 2 * e + 1]);
  return __builtin_bit_cast(bf16x8, w);
}

#define DBG_S 1
#define STAMP(ws, k, i)
constexpr int S = 2048, H = 32, DB = 128, MB = 576, DC = 512, DA = 576, DHA = 18;
constexpr float CB = 0.08838834764831845f * 1.4426950408889634f;
constexpr float CA = 0.23570226039551584f * 1.4426950408889634f;
constexpr size_t WS_KF = 0;
constexpr size_t WS_VF = WS_KF + (size_t)H * S * DB * 2;
constexpr size_t WS_QF = WS_VF + (size_t)H * S * DB * 2;
constexpr size_t WS_CF = WS_QF + (size_t)H * S * DB * 2;
constexpr size_t WS_QA = WS_CF + (size_t)H * S * DB * 2;
constexpr size_t WS_KA = WS_QA + (size_t)S * DA * 2 + 256;
constexpr size_t WS_VA = WS_KA + 65536 + 16384;
constexpr size_t WS_OA = WS_VA + 77824;
constexpr size_t WS_PART = WS_OA + (size_t)S * DA * 2;
constexpr size_t WS_END = WS_PART + (size_t)4 * S * 576 * 4;

struct K1P {
  const void *cx, *bx, *wuk, *wuv, *wq;
  const void *ax, *awq, *awk, *awv;
  char* ws;
};
constexpr int XSLOT = 32768, WRING = 98304, WSLOT = 16384;
constexpr int nWp(int s) { return s < 16 ? 2 : (s <= 33 ? 1 : 0); }
constexpr int nXp(int g) { return ((g >> 1) + 2) <= 16 ? 2 : 0; }
constexpr int nD(int g) { return nWp(g + 3) + nXp(g); }
constexpr int waitN(int g) { return g == 0 ? 6 : (g == 16 ? nD(15) + 16 : (g == 17 ? 16 + nD(16) : nD(g - 1))); }

struct K1C {
  unsigned lds0, w;
  unsigned xoffC, xoffX;
  unsigned woff;
  __amdgpu_buffer_rsrc_t sC, sX, sUK, sUV, sWQ;
  lds_cptr xb[4];
  lds_cptr wb;
};
template <int T> DI void k1_issueX(const K1C& c, int half) {
#pragma unroll
  for (int ii = 0; ii < 2; ++ii) {
    const int i = 2 * half + ii;
    const unsigned m0 = c.lds0 + (T % 3) * XSLOT + i * 8192 + c.w * 1024;
    if constexpr (T < 8) dma16(m0, c.xoffC, c.sC, (unsigned)(i * 64 * 1024 + T * 128));
    else                 dma16(m0, c.xoffX, c.sX, (unsigned)(i * 64 * 1152 + (T - 8) * 128));
  }
}
template <int G> DI void k1_issueW(const K1C& c) {
  const unsigned m0 = c.lds0 + WRING + (G & 3) * WSLOT + c.w * 1024;
  if constexpr (G < 16) { dma16(m0, c.woff, c.sUK, (unsigned)(G * 32 * 8192)); dma16(m0 + 8192, c.woff, c.sUV, (unsigned)(G * 32 * 8192)); }
  else dma16(m0, c.woff, c.sWQ, (unsigned)((G - 16) * 32 * 8192));
}
template <int G> DI void k1_top(const K1C& c) {
  wait_bar<waitN(G)>();
  if constexpr (G + 3 <= 33) k1_issueW<G + 3>(c);
  if constexpr (((G >> 1) + 2) <= 16) k1_issueX<(G >> 1) + 2>(c, G & 1);
}
struct FragKV { bf16x8 x[4]; s16x4 wl[2], wh[2]; };
struct FragQ { bf16x8 x[2]; s16x4 wl[2], wh[2]; };
template <int G, int SS, int I> DI void kv_ld(const K1C& c, FragKV& f) {
  constexpr int T = G >> 1;
  lds_cptr xp = c.xb[2 * (G & 1) + SS] + (T % 3) * XSLOT;
  lds_cptr wp = c.wb + (G & 3) * WSLOT + SS * 4096;
  if constexpr (I == 0) f.x[0] = lds_rd16(xp);
  else if constexpr (I == 1) f.wl[0] = lds_tr(wp);
  else if constexpr (I == 2) f.wh[0] = lds_tr(wp + 256);
  else if constexpr (I == 3) f.wl[1] = lds_tr(wp + 512);
  else if constexpr (I == 4) f.wh[1] = lds_tr(wp + 768);
  else f.x[I - 4] = lds_rd16(xp + (I - 4) * 4096);
}
template <int G, int I> DI void k1_dma_piece(const K1C& c) {
  constexpr int GW = G + 3, TX = (G >> 1) + 2;
  if constexpr (I < 2) {
    if constexpr (GW <= 33) {
      const unsigned m0 = c.lds0 + WRING + (GW & 3) * WSLOT + c.w * 1024;
      if constexpr (GW < 16) { if constexpr (I == 0) dma16(m0, c.woff, c.sUK, (unsigned)(GW * 32 * 8192)); else dma16(m0 + 8192, c.woff, c.sUV, (unsigned)(GW * 32 * 8192)); }
      else if constexpr (I == 0) dma16(m0, c.woff, c.sWQ, (unsigned)((GW - 16) * 32 * 8192));
    }
  } else if constexpr (TX <= 16) {
    constexpr int i = 2 * (G & 1) + (I - 2);
    const unsigned m0 = c.lds0 + (TX % 3) * XSLOT + i * 8192 + c.w * 1024;
    if constexpr (TX < 8) dma16(m0, c.xoffC, c.sC, (unsigned)(i * 64 * 1024 + TX * 128));
    else                  dma16(m0, c.xoffX, c.sX, (unsigned)(i * 64 * 1152 + (TX - 8) * 128));
  }
}
template <bool ISV, int M> DI void kv_mfma(f32x16 (&acc)[4][2], const FragKV& f) {
  constexpr int r = M >> 1, ct = M & 1;
  const bf16x8 wf = cat(f.wl[ct], f.wh[ct]);
  acc[r][ct] = ISV ? MFMA(f.x[r], wf, acc[r][ct]) : MFMA(wf, f.x[r], acc[r][ct]);
}
template <bool ISV, int GN, int SN, int DMAG, bool LOAD, int M> DI void kv_gap(const K1C& c, f32x16 (&acc)[4][2], const FragKV& cur, FragKV& nxt) {
  kv_mfma<ISV, M>(acc, cur);
  if constexpr (LOAD) kv_ld<GN, SN, M>(c, nxt);
  if constexpr (DMAG >= 0 && (M & 1) && M < 8) k1_dma_piece<DMAG, (M >> 1)>(c);
  SBAR();
}
template <bool ISV, int GN, int SN, int DMAG, bool LOAD> DI void kv_phase(const K1C& c, f32x16 (&acc)[4][2], const FragKV& cur, FragKV& nxt) {
  kv_gap<ISV, GN, SN, DMAG, LOAD, 0>(c, acc, cur, nxt); kv_gap<ISV, GN, SN, DMAG, LOAD, 1>(c, acc, cur, nxt);
  kv_gap<ISV, GN, SN, DMAG, LOAD, 2>(c, acc, cur, nxt); kv_gap<ISV, GN, SN, DMAG, LOAD, 3>(c, acc, cur, nxt);
  kv_gap<ISV, GN, SN, DMAG, LOAD, 4>(c, acc, cur, nxt); kv_gap<ISV, GN, SN, DMAG, LOAD, 5>(c, acc, cur, nxt);
  kv_gap<ISV, GN, SN, DMAG, LOAD, 6>(c, acc, cur, nxt); kv_gap<ISV, GN, SN, DMAG, LOAD, 7>(c, acc, cur, nxt);
}
template <int G, bool ISV> DI void k1_step_kv(const K1C& c, f32x16 (&acc)[4][2], FragKV& fa, FragKV& fb) {
  wait_bar<waitN(G)>(); SBAR();
  if constexpr (G == 0) {
    kv_ld<0, 0, 0>(c, fa); kv_ld<0, 0, 1>(c, fa); kv_ld<0, 0, 2>(c, fa); kv_ld<0, 0, 3>(c, fa);
    kv_ld<0, 0, 4>(c, fa); kv_ld<0, 0, 5>(c, fa); kv_ld<0, 0, 6>(c, fa); kv_ld<0, 0, 7>(c, fa); SBAR();
  }
  kv_phase<ISV, G, 1, G, true>(c, acc, fa, fb);
  kv_phase<ISV, G + 1, 0, -1, (G < 15)>(c, acc, fb, fa);
}
template <int G, bool ISV> struct KVLoop { static DI void run(const K1C& c, f32x16 (&acc)[4][2], FragKV& fa, FragKV& fb) { k1_step_kv<G, ISV>(c, acc, fa, fb); if constexpr (G < 15) KVLoop<G + 1, ISV>::run(c, acc, fa, fb); } };

template <int G, int SS, int I> DI void q_ld(const K1C& c, FragQ& f) {
  constexpr int T = G >> 1;
  lds_cptr xp = c.xb[2 * (G & 1) + SS] + (T % 3) * XSLOT;
  lds_cptr wp = c.wb + (G & 3) * WSLOT + SS * 4096;
  if constexpr (I == 0) f.x[0] = lds_rd16(xp);
  else if constexpr (I == 1) f.wl[0] = lds_tr(wp);
  else if constexpr (I == 2) f.wh[0] = lds_tr(wp + 256);
  else if constexpr (I == 3) f.x[1] = lds_rd16(xp + 4096);
  else if constexpr (I == 4) f.wl[1] = lds_tr(wp + 512);
  else f.wh[1] = lds_tr(wp + 768);
}
template <int M> DI void q_mfma(f32x16 (&acc)[2][2], const FragQ& f) {
  constexpr int r = M & 1, ct = M >> 1;
  acc[r][ct] = MFMA(cat(f.wl[ct], f.wh[ct]), f.x[r], acc[r][ct]);
}
template <int GN, int SN, int DMAG, bool LOAD> DI void q_phase(const K1C& c, f32x16 (&acc)[2][2], const FragQ& cur, FragQ& nxt) {
  q_mfma<0>(acc, cur); if constexpr (LOAD) { q_ld<GN, SN, 0>(c, nxt); q_ld<GN, SN, 1>(c, nxt); } SBAR();
  q_mfma<1>(acc, cur); if constexpr (LOAD) { q_ld<GN, SN, 2>(c, nxt); q_ld<GN, SN, 3>(c, nxt); } if constexpr (DMAG >= 0) k1_dma_piece<DMAG, 0>(c); SBAR();
  q_mfma<2>(acc, cur); if constexpr (LOAD) { q_ld<GN, SN, 4>(c, nxt); } if constexpr (DMAG >= 0) k1_dma_piece<DMAG, 2>(c); SBAR();
  q_mfma<3>(acc, cur); if constexpr (LOAD) { q_ld<GN, SN, 5>(c, nxt); } if constexpr (DMAG >= 0) k1_dma_piece<DMAG, 3>(c); SBAR();
}
template <int G> DI void k1_step_q(const K1C& c, f32x16 (&acc)[2][2], FragQ& fa, FragQ& fb) {
  wait_bar<waitN(G)>(); SBAR();
  if constexpr (G == 16) {
    q_ld<G, 0, 0>(c, fa); q_ld<G, 0, 1>(c, fa); q_ld<G, 0, 2>(c, fa); q_ld<G, 0, 3>(c, fa); q_ld<G, 0, 4>(c, fa); q_ld<G, 0, 5>(c, fa); SBAR();
  }
  q_phase<G, 1, G, true>(c, acc, fa, fb);
  q_phase<G + 1, 0, -1, (G < 33)>(c, acc, fb, fa);
}
template <int G> struct QLoop { static DI void run(const K1C& c, f32x16 (&acc)[2][2], FragQ& fa, FragQ& fb) { k1_step_q<G>(c, acc, fa, fb); if constexpr (G < 33) QLoop<G + 1>::run(c, acc, fa, fb); } };

template <bool ISV> DI void k1_kv(K1C& c, const K1P& p, int head, int j, int lane) {
  f32x16 acc[4][2];
#pragma unroll
  for (int r = 0; r < 4; ++r) { acc[r][0] = f32x16{}; acc[r][1] = f32x16{}; }
  FragKV fa, fb;
  KVLoop<0, ISV>::run(c, acc, fa, fb);
  STAMP(p.ws, 0, 1);
  const int wr = c.w >> 2, wc1 = c.w & 1;
  const __amdgpu_buffer_rsrc_t sWS = mksrd(p.ws, 128u << 20);
#pragma unroll
  for (int r = 0; r < 4; ++r)
#pragma unroll
    for (int ct = 0; ct < 2; ++ct)
#pragma unroll
      for (int g = 0; g < 2; ++g) {
        const bf16x8 v = pack8n(acc[r][ct], g);
        size_t off;
        if constexpr (!ISV) { const int kt32 = 8 * j + 4 * wr + r, ds = 2 * (2 * wc1 + ct) + g; off = WS_KF + ((size_t)(head * 64 + kt32) * 8 + ds) * 1024; }
        else { const int kt64 = 4 * j + 2 * wr + (r >> 1), ks = 2 * (r & 1) + g, db = 2 * wc1 + ct; off = WS_VF + ((size_t)((head * 32 + kt64) * 4 + ks) * 4 + db) * 1024; }
        __builtin_amdgcn_raw_buffer_store_b128(__builtin_bit_cast(u32x4, v), sWS, (unsigned)(off + lane * 16), 0, ST_AUX);
      }
  SBAR();
}

__global__ __launch_bounds__(512, 2) void k1_proj(K1P p) {
  extern __shared__ __attribute__((aligned(16))) char lds[];
  const int tid = threadIdx.x, lane = tid & 63, l32 = lane & 31, hi = lane >> 5;
  const int b = blockIdx.x, xcd = b & 7, ii = b >> 3, head = xcd * 4 + (ii & 3), j = ii >> 2;
  K1C c;
  c.lds0 = (unsigned)(size_t)(lds_ptr)lds; c.w = rfl(tid >> 6);
  const int w = c.w;
  {
    const int r = lane >> 3, chp = lane & 7, row = 8 * w + r, ch = chp ^ ((4 * (w & 1) + (r >> 1)) & 7);
    c.xoffC = (unsigned)(row * 1024 + ch * 16); c.xoffX = (unsigned)(row * 1152 + ch * 16);
    const int sub = lane >> 5, rr = (lane & 31) >> 2, c4 = lane & 3;
    c.woff = (unsigned)((8 * (w >> 1) + rr) * 8192 + (32 * (2 * (w & 1) + sub) + 8 * c4) * 2);
  }
  c.sC = mksrd((const char*)p.cx + (size_t)j * 256 * 1024, 256 * 1024);
  c.sX = mksrd((const char*)p.bx + (size_t)j * 256 * 1152, 256 * 1152);
  c.sUK = mksrd((const char*)p.wuk + head * 256, 512 * 8192 - head * 256);
  c.sUV = mksrd((const char*)p.wuv + head * 256, 512 * 8192 - head * 256);
  c.sWQ = mksrd((const char*)p.wq + head * 256, 576 * 8192 - head * 256);
  const int blk = (lane >> 4) & 1, q4 = (lane & 15) >> 2, p4 = lane & 3;
  const unsigned wlane = 2048 * hi + 64 * q4 + 32 * blk + 8 * p4;
  {
    const int wr = w >> 2, wc = w & 3;
#pragma unroll
    for (int s = 0; s < 4; ++s) c.xb[s] = (lds_cptr)lds + (wr * 4) * 4096 + 128 * l32 + 16 * ((2 * s + hi) ^ ((l32 >> 1) & 7));
    c.wb = (lds_cptr)lds + WRING + (wc >> 1) * 8192 + (2 * (wc & 1)) * 512 + wlane;
  }
  STAMP(p.ws, 0, 0);
  if (ii < 20) {
    const int rg = 4 * xcd + ii / 5, cg = ii % 5;
    const __amdgpu_buffer_rsrc_t sAX = mksrd((const char*)p.ax + (size_t)rg * 64 * 1152, 64 * 1152);
    const __amdgpu_buffer_rsrc_t sAQ = mksrd((const char*)p.awq + cg * 256, 576 * 1152 - cg * 256);
    constexpr int ASL = 73728, AWO = 24576;
    const unsigned xv = (unsigned)((lane >> 3) * 1152 + (((lane & 7) ^ ((4 * (w & 1) + (lane >> 4)) & 7)) * 16));
    const unsigned wv = (unsigned)(((lane & 31) >> 2) * 1152 + (32 * (lane >> 5) + 8 * (lane & 3)) * 2);
    auto issue = [&](int th) {
      const unsigned base = c.lds0 + (th & 1) * ASL;
#pragma unroll
      for (int kc = 0; kc < 3; ++kc) dma16(rfl(base + (kc * 8 + w) * 1024), xv, sAX, rfl((unsigned)(w * 8 * 1152 + (th * 3 + kc) * 128)));
#pragma unroll
      for (int i = 0; i < 6; ++i) {
        const int g = 3 * w + (i >> 1), pr = i & 1;
        dma16(rfl(base + AWO + (g * 2 + pr) * 1024), wv, sAQ, rfl((unsigned)((th * 192 + g * 8) * 1152 + pr * 128)));
      }
    };
    const int rt = w >> 2, ct = w & 3, tile = 4 * cg + ct;
    f32x16 acc = f32x16{};
    issue(0); issue(1);
    for (int th = 0; th < 3; ++th) {
      if (th < 2) asm volatile("s_waitcnt vmcnt(9) lgkmcnt(0)\n\ts_barrier" ::: "memory");
      else asm volatile("s_waitcnt vmcnt(0) lgkmcnt(0)\n\ts_barrier" ::: "memory");
      lds_cptr xp = (lds_cptr)lds + (th & 1) * ASL + rt * 4096 + 128 * l32;
      lds_cptr wp = (lds_cptr)lds + (th & 1) * ASL + AWO + ct * 512 + 2048 * hi + 64 * q4 + 32 * blk + 8 * p4;
      bf16x8 xf[4]; s16x4 wl[4], wh[4];
#define ALD(k) do { xf[(k) & 3] = lds_rd16(xp + ((k) >> 2) * 8192 + 16 * ((2 * ((k) & 3) + hi) ^ ((l32 >> 1) & 7))); wl[(k) & 3] = lds_tr(wp + (k) * 4096); wh[(k) & 3] = lds_tr(wp + (k) * 4096 + 256); } while (0)
      if (tile < 18) {
        ALD(0); ALD(1); ALD(2);
#pragma unroll
        for (int k = 0; k < 12; ++k) {
          if (k + 3 < 12) ALD(k + 3);
          acc = MFMA(cat(wl[k & 3], wh[k & 3]), xf[k & 3], acc);
          SBAR();
        }
      }
      if (th == 0) { asm volatile("s_waitcnt lgkmcnt(0)\n\ts_barrier" ::: "memory"); issue(2); }
    }
    const int row = (rg * 2 + rt) * 32 + l32;
    if (tile < 18) {
#pragma unroll
      for (int g = 0; g < 4; ++g) {
        u32x2 v = {cvtpk(acc[4 * g] * CA, acc[4 * g + 1] * CA), cvtpk(acc[4 * g + 2] * CA, acc[4 * g + 3] * CA)};
        *(u32x2*)(p.ws + WS_QA + (size_t)row * 1152 + (tile * 32 + 8 * g + 4 * hi) * 2) = v;
      }
    }
  } else if (ii < 28) {
    const int r32 = 8 * xcd + (ii - 20);
    const __amdgpu_buffer_rsrc_t sAX = mksrd((const char*)p.ax + (size_t)r32 * 32 * 1152, 32 * 1152);
    for (int pc = w; pc < 36; pc += 8) {
      const int kc = pc >> 2, rq = pc & 3, r = lane >> 3, row = 8 * rq + r, ch = (lane & 7) ^ ((row >> 1) & 7);
      dma16(rfl(c.lds0 + pc * 1024), (unsigned)(row * 1152 + ch * 16), sAX, rfl((unsigned)(kc * 128)));
    }
    {
      const unsigned* gk = (const unsigned*)p.awk; const unsigned* gv = (const unsigned*)p.awv;
      unsigned vk[11], vv[11];
#pragma unroll
      for (int it = 0; it < 11; ++it) { const int i = tid + it * 512; const int ic = i < 5184 ? i : 0; vk[it] = gk[ic]; vv[it] = gv[ic]; }
#pragma unroll
      for (int it = 0; it < 11; ++it) {
        const int i = tid + it * 512;
        if (i < 5184) { const int r = i / 9, d = i - r * 9; char* dst = lds + 36864 + (r >> 3) * 1024 + (r & 7) * 64 + d * 4; *(unsigned*)dst = vk[it]; *(unsigned*)(dst + 512) = vv[it]; }
      }
    }
    asm volatile("s_waitcnt vmcnt(0) lgkmcnt(0)\n\ts_barrier" ::: "memory");
    const int isv = w >> 2, kq = w & 3;
    f32x16 acc = f32x16{};
    lds_cptr xp = (lds_cptr)lds + 128 * l32;
    lds_cptr wp = (lds_cptr)lds + 36864 + isv * 512 + 1024 * hi + 64 * q4 + 32 * blk + 8 * p4;
#pragma unroll
    for (int k9 = 0; k9 < 9; ++k9) {
      const int k = kq * 9 + k9;
      const bf16x8 xf = lds_rd16(xp + (k >> 2) * 4096 + 16 * ((2 * (k & 3) + hi) ^ ((l32 >> 1) & 7)));
      const bf16x8 wf = cat(lds_tr(wp + k * 2048), lds_tr(wp + k * 2048 + 256));
      if (isv) acc = MFMA(xf, wf, acc); else acc = MFMA(wf, xf, acc);
    }
    asm volatile("s_waitcnt lgkmcnt(0)\n\ts_barrier" ::: "memory");
    float* red = (float*)lds + w * 1024;
#pragma unroll
    for (int q = 0; q < 4; ++q) *(f32x4*)(red + q * 256 + lane * 4) = f32x4{acc[4 * q], acc[4 * q + 1], acc[4 * q + 2], acc[4 * q + 3]};
    __syncthreads();
    if (kq == 0) {
#pragma unroll
      for (int o = 1; o < 4; ++o)
#pragma unroll
        for (int q = 0; q < 4; ++q) { const f32x4 y = *(const f32x4*)(red + o * 1024 + q * 256 + lane * 4); acc[4 * q] += y[0]; acc[4 * q + 1] += y[1]; acc[4 * q + 2] += y[2]; acc[4 * q + 3] += y[3]; }
      const int row = r32 * 32 + l32;
      if (!isv) {
        char* k0 = p.ws + WS_KA + (size_t)r32 * 1024;
        *(u32x2*)(k0 + (l32) * 16 + 8 * hi) = u32x2{cvtpk(acc[0], acc[1]), cvtpk(acc[2], acc[3])};
        *(u32x2*)(k0 + (l32 + 32) * 16 + 8 * hi) = u32x2{cvtpk(acc[4], acc[5]), cvtpk(acc[6], acc[7])};
        if (hi == 0) *(u32x2*)(p.ws + WS_KA + 65536 + (size_t)row * 8) = u32x2{cvtpk(acc[8], acc[9]), 0x00003F80u};
      } else {
#pragma unroll
        for (int g = 0; g < 2; ++g) {
          bf16x8 v = pack8n(acc, g);
          if (l32 == 18) v = bf16x8{0x3F80, 0x3F80, 0x3F80, 0x3F80, 0x3F80, 0x3F80, 0x3F80, 0x3F80};
          if (l32 < 19) *(bf16x8*)(p.ws + WS_VA + ((size_t)((2 * r32 + g) * 2 + hi) * 19 + l32) * 16) = v;
        }
      }
    }
  }
  asm volatile("s_waitcnt lgkmcnt(0)\n\ts_barrier" ::: "memory");
  STAMP(p.ws, 0, 6);
  k1_issueX<0>(c, 0); k1_issueX<0>(c, 1); k1_issueW<0>(c); k1_issueW<1>(c); k1_issueX<1>(c, 0); k1_issueX<1>(c, 1); k1_issueW<2>(c);
  if ((w & 2) == 0) k1_kv<false>(c, p, head, j, lane); else k1_kv<true>(c, p, head, j, lane);
  STAMP(p.ws, 0, 2);
  {
    const int wr4 = w >> 1, wc2 = w & 1;
#pragma unroll
    for (int s = 0; s < 4; ++s) c.xb[s] = (lds_cptr)lds + (wr4 * 2) * 4096 + 128 * l32 + 16 * ((2 * s + hi) ^ ((l32 >> 1) & 7));
    c.wb = (lds_cptr)lds + WRING + (2 * wc2) * 512 + wlane;
    f32x16 acc[2][2];
    acc[0][0] = f32x16{}; acc[0][1] = f32x16{}; acc[1][0] = f32x16{}; acc[1][1] = f32x16{};
    FragQ fa, fb;
    QLoop<16>::run(c, acc, fa, fb);
    STAMP(p.ws, 0, 3);
#pragma unroll
    for (int r = 0; r < 2; ++r)
#pragma unroll
      for (int ct = 0; ct < 2; ++ct)
#pragma unroll
        for (int g = 0; g < 2; ++g) {
          const int qt32 = 8 * j + 2 * wr4 + r, ds = 2 * (2 * wc2 + ct) + g;
          __builtin_amdgcn_raw_buffer_store_b128(__builtin_bit_cast(u32x4, pack8(acc[r][ct], g, CB)), mksrd(p.ws, 128u << 20), (unsigned)(WS_QF + ((size_t)(head * 64 + qt32) * 8 + ds) * 1024 + lane * 16), 0, ST_AUX);
        }
  }
  STAMP(p.ws, 0, 4);
  STAMP(p.ws, 0, 5);
}

constexpr float LTHR = 7.9228162514264338e28f;
#define LTHR_V LTHR
struct AttB {
  f32x16 o[4]; bf16x8 qf[8]; f32x16 negm; float m, l;
  f32x16 s0, s1;
  unsigned pw[16];
  bf16x8 fr[4];
  float ps;
};
DI void cmask1(f32x16& s, int key0, int row) {
  const int d = row - key0;
#pragma unroll
  for (int r = 0; r < 16; ++r) { const int c = (r & 3) + 8 * (r >> 2); s[r] = (c > d) ? -INFINITY : s[r]; }
}
DI float hmax(float m) { auto rr = __builtin_amdgcn_permlane32_swap(__float_as_uint(m), __float_as_uint(m), false, false); return fmaxf(__uint_as_float(rr[0]), __uint_as_float(rr[1])); }
DI float hsum(float m) { auto rr = __builtin_amdgcn_permlane32_swap(__float_as_uint(m), __float_as_uint(m), false, false); return __uint_as_float(rr[0]) + __uint_as_float(rr[1]); }
DI float max16(const f32x16& s) { float a = fmaxf(fmaxf(s[0], s[1]), s[2]);
#pragma unroll
  for (int r = 3; r < 15; r += 2) a = fmaxf(fmaxf(a, s[r]), s[r + 1]);
  return fmaxf(a, s[15]); }
DI f32x16 splat16(float x) { f32x16 v;
#pragma unroll
  for (int r = 0; r < 16; ++r) v[r] = x;
  return v; }
#define PKB(w) __builtin_bit_cast(bf16x8, w)
#define EXP2(x) __builtin_amdgcn_exp2f(x)
DI bf16x8 pfrag(const AttB& a, int ks) { u32x4 w = {a.pw[4 * ks], a.pw[4 * ks + 1], a.pw[4 * ks + 2], a.pw[4 * ks + 3]}; return __builtin_bit_cast(bf16x8, w); }

template <bool HASCUR> DI void rare_fix(AttB& a, __amdgpu_buffer_rsrc_t sK, __amdgpu_buffer_rsrc_t sQ, unsigned voff, unsigned kso, unsigned qso, int key0, int row, bool mask) {
  float tm = -INFINITY;
  f32x16 t;
#pragma unroll 1
  for (int kb = 0; kb < 2; ++kb) {
    t = f32x16{};
#pragma unroll 2
    for (int ds = 0; ds < 8; ++ds) t = MFMA(bld16(sK, voff, kso + kb * 8192 + ds * 1024), bld16(sQ, voff, qso + ds * 1024), t);
    if (mask) cmask1(t, key0 + 32 * kb, row);
    tm = fmaxf(tm, max16(t));
  }
  tm = hmax(tm);
  const float mn = fmaxf(a.m, tm), dl = mn - a.m, f = EXP2(-dl);
#pragma unroll
  for (int db = 0; db < 4; ++db)
#pragma unroll
    for (int r = 0; r < 16; ++r) a.o[db][r] *= f;
  float ps = 0.f;
  {
    t = f32x16{};
#pragma unroll 2
    for (int ds = 0; ds < 8; ++ds) t = MFMA(bld16(sK, voff, kso + ds * 1024), bld16(sQ, voff, qso + ds * 1024), t);
    if (mask) cmask1(t, key0, row);
#pragma unroll
    for (int r = 0; r < 16; ++r) { t[r] = EXP2(t[r] - mn); ps += t[r]; }
#pragma unroll
    for (int k = 0; k < 8; ++k) a.pw[k] = cvtpk(t[2 * k], t[2 * k + 1]);
    t = f32x16{};
#pragma unroll 2
    for (int ds = 0; ds < 8; ++ds) t = MFMA(bld16(sK, voff, kso + 8192 + ds * 1024), bld16(sQ, voff, qso + ds * 1024), t);
    if (mask) cmask1(t, key0 + 32, row);
#pragma unroll
    for (int r = 0; r < 16; ++r) { t[r] = EXP2(t[r] - mn); ps += t[r]; }
#pragma unroll
    for (int k = 0; k < 8; ++k) a.pw[8 + k] = cvtpk(t[2 * k], t[2 * k + 1]);
  }
  a.l *= f; a.ps = ps;
  if (HASCUR) {
#pragma unroll
    for (int r = 0; r < 16; ++r) a.s0[r] -= dl;
  }
  a.m = mn; a.negm = splat16(-mn); PIN(a.negm);
}

struct StepCtx {
  lds_cptr kp;
  lds_cptr kpn;
  lds_cptr vp;
  unsigned m0k, m0v, sok, sov; bool dk, dv;
  unsigned voff;
};
template <int I> DI void gapA(AttB& a, const StepCtx& c, __amdgpu_buffer_rsrc_t sK, __amdgpu_buffer_rsrc_t sV) {
  constexpr int ds = I & 7;
  if constexpr (I < 8) { if constexpr (ds == 0) a.s0 = MFMA(a.fr[I & 3], a.qf[0], a.negm); else a.s0 = MFMA(a.fr[I & 3], a.qf[ds], a.s0); }
  else { if constexpr (ds == 0) a.s1 = MFMA(a.fr[I & 3], a.qf[0], a.negm); else a.s1 = MFMA(a.fr[I & 3], a.qf[ds], a.s1); }
  if constexpr (I + 3 < 16) a.fr[(I + 3) & 3] = lds_rd16(c.kp + (I + 3) * 1024);
  else a.fr[(I + 3) & 3] = lds_rd16(c.vp + (I + 3 - 16) * 1024);
  if constexpr (I < 8) {
    a.s1[8 + I] = EXP2(a.s1[8 + I]);
    a.ps += a.s1[7 + I];
    if constexpr (I < 4) { a.pw[8 + I] = cvtpk(a.s1[2 * I], a.s1[2 * I + 1]); PIN(a.pw[8 + I]); }
    else if constexpr (I != 6) { constexpr int k = I == 7 ? 6 : I; a.pw[8 + k] = cvtpk(a.s1[2 * k], a.s1[2 * k + 1]); PIN(a.pw[8 + k]); }
    if constexpr (I & 1) dma16o<(I >> 1) * 1024>(c.m0v, c.voff, sV, c.sov);
  } else {
    constexpr int i = I - 8;
    a.s0[i] = EXP2(a.s0[i]);
    if constexpr (i >= 1) a.ps += a.s0[i - 1];
    if constexpr (i & 1) dma16o<(i >> 1) * 1024>(c.m0k, c.voff, sK, c.sok);
  }
  PIN(a.ps);
  SBAR();
}
template <int I> DI void gapB(AttB& a, const StepCtx& c) {
  constexpr int ks = I >> 2, db = I & 3;
  a.o[db] = MFMA(a.fr[I & 3], pfrag(a, ks), a.o[db]);
  if constexpr (I + 3 < 16) a.fr[(I + 3) & 3] = lds_rd16(c.vp + (I + 3) * 1024);
  else a.fr[(I + 3) & 3] = lds_rd16(c.kpn + (I + 3 - 16) * 1024);
  if constexpr (I < 8) {
    a.s0[8 + I] = EXP2(a.s0[8 + I]);
    a.ps += a.s0[7 + I];
    if constexpr (I >= 4) { constexpr int k = I - 4; a.pw[k] = cvtpk(a.s0[2 * k], a.s0[2 * k + 1]); PIN(a.pw[k]); }
  } else {
    constexpr int i = I - 8;
    a.s1[i] = EXP2(a.s1[i]);
    if constexpr (i == 0) a.ps += a.s0[15]; else a.ps += a.s1[i - 1];
    if constexpr (I < 12) { constexpr int k = I - 4; a.pw[k] = cvtpk(a.s0[2 * k], a.s0[2 * k + 1]); PIN(a.pw[k]); }
  }
  PIN(a.ps);
  SBAR();
}
template <int O, int... Is> DI void phaseA_(AttB& a, const StepCtx& c, __amdgpu_buffer_rsrc_t sK, __amdgpu_buffer_rsrc_t sV, std::integer_sequence<int, Is...>) { (gapA<O + Is>(a, c, sK, sV), ...); }
template <int... Is> DI void phaseB_(AttB& a, const StepCtx& c, std::integer_sequence<int, Is...>) { (gapB<Is>(a, c), ...); }

DI void pv_plain(AttB& a, lds_cptr vp) {
#pragma unroll
  for (int ks = 0; ks < 4; ++ks)
#pragma unroll
    for (int db = 0; db < 4; ++db) { a.o[db] = MFMA(lds_rd16(vp + (ks * 4 + db) * 1024), pfrag(a, ks), a.o[db]); if (db == 3) SBAR(); }
}
struct K2P { char* ws; };
struct AttEnv {
  int grp, wq, lane, hi, l32, n1, L, head; char* lds; unsigned lds0; char* ws;
  __amdgpu_buffer_rsrc_t sK, sV, sQ, sC;
};
DI int tile_of(const AttEnv& e, int x) { return e.grp == 0 ? x : (x < e.n1 ? x : 17 + (x - e.n1)); }
DI void step_top(const AttEnv& e, int s, StepCtx& c) {
  asm volatile("s_waitcnt vmcnt(0)\n\ts_barrier" ::: "memory");
  SBAR();
  const unsigned gb = e.grp * 81920;
  const int k3 = s % 3, k3n = (s + 1) % 3, k3d = (s + 2) % 3;
  c.kp = (lds_cptr)e.lds + gb + k3 * 16384 + e.lane * 16;
  c.kpn = (lds_cptr)e.lds + gb + k3n * 16384 + e.lane * 16;
  c.vp = (lds_cptr)e.lds + gb + 49152 + ((s + 1) & 1) * 16384 + e.lane * 16;
  c.dk = (s + 2 < 17); c.dv = (s < 17);
  c.m0k = rfl(e.lds0 + gb + k3d * 16384 + e.wq * 4096); c.m0v = rfl(e.lds0 + gb + 49152 + (s & 1) * 16384 + e.wq * 4096);
  c.sok = rfl((unsigned)(tile_of(e, s + 2) * 16384 + e.wq * 4096)); c.sov = rfl((unsigned)(tile_of(e, s) * 16384 + e.wq * 4096));
  c.voff = e.lane * 16;
}
DI void dma_all(const AttEnv& e, const StepCtx& c) {
  if (c.dk) {
#pragma unroll
    for (int i = 0; i < 4; ++i) dma16(c.m0k + i * 1024, c.voff, e.sK, c.sok + i * 1024); }
  if (c.dv) {
#pragma unroll
    for (int i = 0; i < 4; ++i) dma16(c.m0v + i * 1024, c.voff, e.sV, c.sov + i * 1024); }
}
template <bool HASCUR> DI void check_prev(AttB& a, const AttEnv& e, int qb, int prev_t) {
  if (__builtin_expect(__any(!(a.l + a.ps <= LTHR_V)), 0)) {
    const int rmin = qb * 128 + e.wq * 32;
    rare_fix<HASCUR>(a, e.sK, e.sQ, e.lane * 16, rfl((unsigned)(prev_t * 16384)), rfl((unsigned)((qb * 4 + e.wq) * 8192)),
                     prev_t * 64 + 4 * e.hi, rmin + e.l32, prev_t * 64 + 63 > rmin);
  }
  a.l += a.ps; a.ps = 0.f;
}
DI void steady_step(AttB& a, const AttEnv& e, int s, int qb) {
  if (s == DBG_S) STAMP(e.ws, 2, 0);
  StepCtx c; step_top(e, s, c);
  if (s == DBG_S) STAMP(e.ws, 2, 1);
  const int t = tile_of(e, s), rmin = qb * 128 + e.wq * 32;
  const bool mask = t * 64 + 63 > rmin;
  phaseA_<0>(a, c, e.sK, e.sV, std::make_integer_sequence<int, 8>{});
  if (s == DBG_S) STAMP(e.ws, 2, 2);
  a.ps += a.s1[15]; a.pw[15] = cvtpk(a.s1[14], a.s1[15]);
  check_prev<true>(a, e, qb, tile_of(e, s - 1));
  if (mask) cmask1(a.s0, t * 64 + 4 * e.hi, rmin + e.l32);
  SBAR();
  phaseA_<8>(a, c, e.sK, e.sV, std::make_integer_sequence<int, 8>{});
  if (s == DBG_S) STAMP(e.ws, 2, 3);
  if (mask) cmask1(a.s1, t * 64 + 32 + 4 * e.hi, rmin + e.l32);
  SBAR();
  phaseB_(a, c, std::make_integer_sequence<int, 16>{});
  if (s == DBG_S) STAMP(e.ws, 2, 4);
}
DI void drain(AttB& a, const AttEnv& e, const StepCtx& c, int s, int qb) {
  a.ps += a.s1[7];
#pragma unroll
  for (int r = 8; r < 16; ++r) { a.s1[r] = EXP2(a.s1[r]); a.ps += a.s1[r]; }
#pragma unroll
  for (int k = 0; k < 8; ++k) a.pw[8 + k] = cvtpk(a.s1[2 * k], a.s1[2 * k + 1]);
  check_prev<false>(a, e, qb, tile_of(e, s - 1));
  pv_plain(a, c.vp);
}

__global__ __launch_bounds__(512, 2) void k2_attn(K2P p) {
  extern __shared__ __attribute__((aligned(16))) char lds[];
  const int tid = threadIdx.x, lane = tid & 63, l32 = lane & 31, hi = lane >> 5;
  const int b = blockIdx.x, xcd = b & 7, ii = b >> 3, head = xcd * 4 + (ii & 3), pp = ii >> 2;
  const unsigned lds0 = (unsigned)(size_t)(lds_ptr)lds;
  const int w = rfl(tid >> 6), grp = w >> 2, wq = w & 3;
  const int L = 15 - pp, n1 = 2 * pp + 2;
  const unsigned voff = lane * 16;
  STAMP(p.ws, 1, 0);
  {
  AttEnv e; e.grp = grp; e.wq = wq; e.lane = lane; e.hi = hi; e.l32 = l32; e.n1 = n1; e.L = L; e.head = head; e.lds = lds; e.lds0 = lds0; e.ws = p.ws;
  e.sK = mksrd(p.ws + WS_KF + (size_t)head * S * 256, S * 256);
  e.sV = mksrd(p.ws + WS_VF + (size_t)head * S * 256, S * 256);
  e.sQ = mksrd(p.ws + WS_QF + (size_t)head * S * 256, S * 256);
  e.sC = mksrd(p.ws + WS_CF + (size_t)head * 8192, 64 * 256 * 1024 - head * 8192);
  AttB a;
  int qb = grp == 0 ? L : pp;
  {
    const unsigned gb = grp * 81920;
#pragma unroll
    for (int t = 0; t < 2; ++t)
#pragma unroll
      for (int i = 0; i < 4; ++i) dma16(rfl(lds0 + gb + t * 16384 + wq * 4096 + i * 1024), voff, e.sK, rfl((unsigned)(t * 16384 + wq * 4096 + i * 1024)));
#pragma unroll
    for (int ds = 0; ds < 8; ++ds) a.qf[ds] = bld16(e.sQ, voff, rfl((unsigned)((qb * 4 + wq) * 8192 + ds * 1024)));
  }
  int s = 0;
  StepCtx c; step_top(e, 0, c); dma_all(e, c);
  const int nseg = grp == 0 ? 1 : 2;
  for (int seg = 0; seg < nseg; ++seg) {
    const int send = (grp == 1 && seg == 0) ? n1 : 17;
    const int rmin = qb * 128 + wq * 32;
    int nt = 0;
    { const int tmax = (rmin + 31) >> 6, t0 = tile_of(e, s); nt = tmax - t0 + 1; if (nt > send - s) nt = send - s; if (nt < 0) nt = 0; }
#pragma unroll
    for (int db = 0; db < 4; ++db) a.o[db] = f32x16{};
    a.m = -1e30f; a.l = 0.f; a.ps = 0.f;
    const int sfirst = s;
    if (nt > 0) {
      const int t = tile_of(e, s);
      a.s0 = f32x16{}; a.s1 = f32x16{};
#pragma unroll
      for (int ds = 0; ds < 8; ++ds) { a.s0 = MFMA(lds_rd16(c.kp + ds * 1024), a.qf[ds], a.s0); a.s1 = MFMA(lds_rd16(c.kp + 8192 + ds * 1024), a.qf[ds], a.s1); if (ds & 1) SBAR(); }
      if (t * 64 + 63 > rmin) { cmask1(a.s0, t * 64 + 4 * hi, rmin + l32); cmask1(a.s1, t * 64 + 32 + 4 * hi, rmin + l32); }
      const float tm = fmaxf(hmax(fmaxf(max16(a.s0), max16(a.s1))), -1e30f);
      a.m = tm; a.negm = splat16(-tm); PIN(a.negm);
      float ps = 0.f;
#pragma unroll
      for (int r = 0; r < 16; ++r) { a.s0[r] = EXP2(a.s0[r] - tm); ps += a.s0[r]; a.s1[r] -= tm; }
#pragma unroll
      for (int k = 0; k < 8; ++k) a.pw[k] = cvtpk(a.s0[2 * k], a.s0[2 * k + 1]);
#pragma unroll
      for (int r = 0; r < 8; ++r) { a.s1[r] = EXP2(a.s1[r]); if (r < 7) ps += a.s1[r]; }
      a.ps = ps;
#pragma unroll
      for (int J = 0; J < 3; ++J) a.fr[J] = lds_rd16(c.kpn + J * 1024);
      ++s;
      const int slast = sfirst + nt;
      for (; s < slast; ++s) steady_step(a, e, s, qb);
      step_top(e, s, c); dma_all(e, c);
      if (grp == 1 && seg == 0) {
#pragma unroll
        for (int ds = 0; ds < 8; ++ds) a.qf[ds] = bld16(e.sQ, voff, rfl((unsigned)((L * 4 + wq) * 8192 + ds * 1024)));
      }
      drain(a, e, c, s, qb);
    } else {
      ++s; step_top(e, s, c); dma_all(e, c);
    }
    if (grp == 1 && seg == 0) {
      const float inv = 1.f / hsum(a.l);
#pragma unroll
      for (int db = 0; db < 4; ++db)
#pragma unroll
        for (int g = 0; g < 2; ++g)
          bst16(pack8(a.o[db], g, inv), e.sC, voff, rfl((unsigned)(((qb * 4 + wq) * 256 + db * 2 + g) * 1024)));
      qb = L;
    }
    while (s < send) { ++s; step_top(e, s, c); dma_all(e, c); }
  }
  STAMP(p.ws, 1, 1);
  asm volatile("s_waitcnt vmcnt(0) lgkmcnt(0)\n\ts_barrier" ::: "memory");
  {
    const float lt = hsum(a.l);
    char* xb = lds + w * 8192;
    float* st = (float*)(lds + 65536) + w * 128;
#pragma unroll
    for (int i = 0; i < 2; ++i)
#pragma unroll
      for (int q = 0; q < 4; ++q) {
        const f32x16& o = grp == 0 ? a.o[2 + i] : a.o[i];
        *(f32x4*)(xb + (i * 4 + q) * 1024 + lane * 16) = f32x4{o[4 * q], o[4 * q + 1], o[4 * q + 2], o[4 * q + 3]};
      }
    st[lane] = a.m; st[64 + lane] = lt;
    __syncthreads();
    const int ow = w ^ 4;
    const char* yb = lds + ow * 8192; const float* so = (const float*)(lds + 65536) + ow * 128;
    const float m2 = so[lane], l2 = so[64 + lane];
    const float mn = fmaxf(a.m, m2), f1 = EXP2(a.m - mn), f2 = EXP2(m2 - mn);
    const float inv = 1.f / (lt * f1 + l2 * f2);
    const float g1 = f1 * inv, g2 = f2 * inv;
#pragma unroll
    for (int i = 0; i < 2; ++i) {
      f32x16 o = grp == 0 ? a.o[i] : a.o[2 + i];
#pragma unroll
      for (int q = 0; q < 4; ++q) { const f32x4 y = *(const f32x4*)(yb + (i * 4 + q) * 1024 + lane * 16);
#pragma unroll
        for (int e = 0; e < 4; ++e) o[4 * q + e] = o[4 * q + e] * g1 + y[e] * g2; }
      const int db = (grp == 0 ? 0 : 2) + i;
#pragma unroll
      for (int g = 0; g < 2; ++g)
        bst16(pack8n(o, g), e.sC, voff, rfl((unsigned)(((L * 4 + wq) * 256 + db * 2 + g) * 1024)));
    }
  }
  }
  __syncthreads();
  STAMP(p.ws, 1, 2);
  {
    const __amdgpu_buffer_rsrc_t sAr = mksrd(p.ws + WS_KA, 65536 + 16384 + 77824);
    for (int pc = w; pc < 156; pc += 8) dma16(rfl(lds0 + pc * 1024), voff, sAr, rfl((unsigned)(pc * 1024)));
    const int ah = head, aj = pp;
    const int qrow = aj * 256 + w * 32 + l32;
    const char* qp = p.ws + WS_QA + (size_t)qrow * 1152 + ah * 36;
    u32x4 q0; { const unsigned* qq = (const unsigned*)(qp + 16 * hi); q0 = u32x4{qq[0], qq[1], qq[2], qq[3]}; }
    const unsigned q16 = *(const unsigned*)(qp + 32);
    asm volatile("s_waitcnt vmcnt(0)\n\ts_barrier" ::: "memory");
    float km = 0.f;
#pragma unroll
    for (int i = 0; i < 4; ++i) {
      const int key = tid * 4 + i, kt = key >> 5, kl = key & 31;
      const bf16x8 a0 = lds_rd16((lds_cptr)lds + kt * 1024 + kl * 16), a1 = lds_rd16((lds_cptr)lds + kt * 1024 + (kl + 32) * 16);
      const unsigned a2 = *(const __attribute__((address_space(3))) unsigned*)((lds_cptr)lds + 65536 + key * 8);
      float s2 = 0.f;
#pragma unroll
      for (int e = 0; e < 8; ++e) { const float x = __uint_as_float((unsigned)(unsigned short)a0[e] << 16), y = __uint_as_float((unsigned)(unsigned short)a1[e] << 16); s2 += x * x + y * y; }
      { const float x = __uint_as_float(a2 << 16), y = __uint_as_float(a2 & 0xFFFF0000u); s2 += x * x + y * y; }
      km = fmaxf(km, s2);
    }
#pragma unroll
    for (int o = 32; o >= 1; o >>= 1) km = fmaxf(km, __shfl_xor(km, o));
    float* red = (float*)(lds + 159744);
    if (lane == 0) red[w] = km;
    __syncthreads();
    km = red[0];
#pragma unroll
    for (int i = 1; i < 8; ++i) km = fmaxf(km, red[i]);
    float qs = 0.f;
#pragma unroll
    for (int e = 0; e < 4; ++e) { const float x = __uint_as_float(q0[e] << 16), y = __uint_as_float(q0[e] & 0xFFFF0000u); qs += x * x + y * y; }
    if (hi == 0) { const float x = __uint_as_float(q16 << 16), y = __uint_as_float(q16 & 0xFFFF0000u); qs += x * x + y * y; }
    qs = hsum(qs);
    const float bound = sqrtf(qs * km) * 1.0009765625f + 1e-30f;
    const unsigned bb = (__float_as_uint(bound) + 0xFFFFu) >> 16;
    const bf16x8 qf0 = __builtin_bit_cast(bf16x8, q0);
    s16x4 qf1; { u32x2 t = hi == 0 ? u32x2{q16, bb | 0x8000u} : u32x2{0u, 0u}; qf1 = __builtin_bit_cast(s16x4, t); }
    f32x16 o = f32x16{};
    const int vrow = l32 < 19 ? l32 : (((l32 >= 20 && l32 <= 27)) ? 0 : 4);
    lds_cptr k0p = (lds_cptr)lds + lane * 16;
    lds_cptr k1p = (lds_cptr)lds + 65536 + l32 * 8;
    lds_cptr vp = (lds_cptr)lds + 81920 + (hi * 19 + vrow) * 16;
    STAMP(p.ws, 1, 3);
    f32x16 sA, sB; u32x4 pa0, pa1, pb0, pb1;
    bf16x8 k0, v0, v1; s16x4 k1;
#define LDK(kt) do { k0 = lds_rd16(k0p + (kt) * 1024); k1 = *(const __attribute__((address_space(3))) s16x4*)(k1p + (kt) * 256); } while (0)
#define LDV(kt) do { v0 = lds_rd16(vp + (2 * (kt)) * 608); v1 = lds_rd16(vp + (2 * (kt) + 1) * 608); } while (0)
#define QKA(sc) do { sc = MFMA(k0, qf0, f32x16{}); sc = __builtin_amdgcn_mfma_f32_32x32x8bf16_1k(k1, qf1, sc, 0, 0, 0); } while (0)
#define E4(sc, i) do { sc[i] = EXP2(sc[i]); sc[i + 1] = EXP2(sc[i + 1]); sc[i + 2] = EXP2(sc[i + 2]); sc[i + 3] = EXP2(sc[i + 3]); } while (0)
#define C2(pw, j, sc, i) do { pw[j] = cvtpk(sc[i], sc[i + 1]); pw[j + 1] = cvtpk(sc[i + 2], sc[i + 3]); } while (0)
#define ASTEP(SC, SN, PP0, PP1, PN0, PN1, kt, DOQK, DOPV, DOLDK) do { \
      if (DOQK) { SN = MFMA(k0, qf0, f32x16{}); } E4(SC, 0); PIN(SC); SBAR(); \
      if (DOQK) { SN = __builtin_amdgcn_mfma_f32_32x32x8bf16_1k(k1, qf1, SN, 0, 0, 0); } if (DOLDK) LDK((kt) + 2); E4(SC, 4); C2(PN0, 0, SC, 0); PIN(SC); PIN(PN0); SBAR(); \
      if (DOPV) { o = MFMA(v0, PKB(PP0), o); } E4(SC, 8); C2(PN0, 2, SC, 4); PIN(SC); PIN(PN0); SBAR(); \
      if (DOPV) { o = MFMA(v1, PKB(PP1), o); } LDV(kt); E4(SC, 12); C2(PN1, 0, SC, 8); PIN(SC); PIN(PN1); SBAR(); \
      C2(PN1, 2, SC, 12); PIN(PN1); SBAR(); } while (0)
    LDK(0); QKA(sA); LDK(1);
    pa0 = u32x4{0,0,0,0}; pa1 = pa0; pb0 = pa0; pb1 = pa0;
    ASTEP(sA, sB, pb0, pb1, pa0, pa1, 0, true, false, true);
    for (int kt = 1; kt < 61; kt += 2) {
      ASTEP(sB, sA, pa0, pa1, pb0, pb1, kt, true, true, true);
      ASTEP(sA, sB, pb0, pb1, pa0, pa1, kt + 1, true, true, true);
    }
    ASTEP(sB, sA, pa0, pa1, pb0, pb1, 61, true, true, true);
    ASTEP(sA, sB, pb0, pb1, pa0, pa1, 62, true, true, false);
    ASTEP(sB, sA, pa0, pa1, pb0, pb1, 63, false, true, false);
    o = MFMA(v0, PKB(pb0), o); o = MFMA(v1, PKB(pb1), o);
    STAMP(p.ws, 1, 4);
    float l = o[10];
    { auto rr = __builtin_amdgcn_permlane32_swap(__float_as_uint(l), __float_as_uint(l), false, false); l = __uint_as_float(rr[0]); }
    if (__builtin_expect(__any(!(l >= 8.6736174e-19f)), 0)) {
      float m = -1e30f; o = f32x16{};
      s16x4 qz; { u32x2 t = hi == 0 ? u32x2{q16, 0u} : u32x2{0u, 0u}; qz = __builtin_bit_cast(s16x4, t); }
      for (int kt = 0; kt < 64; ++kt) {
        LDK(kt); LDV(kt);
        f32x16 sc = MFMA(k0, qf0, f32x16{});
        sc = __builtin_amdgcn_mfma_f32_32x32x8bf16_1k(k1, qz, sc, 0, 0, 0);
        const float mn = fmaxf(m, hmax(max16(sc))), f = EXP2(m - mn);
        m = mn;
#pragma unroll
        for (int r = 0; r < 16; ++r) { sc[r] = EXP2(sc[r] - mn); o[r] *= f; }
        o = MFMA(v0, pack8n(sc, 0), o);
        o = MFMA(v1, pack8n(sc, 1), o);
      }
      l = o[10];
      auto rr = __builtin_amdgcn_permlane32_swap(__float_as_uint(l), __float_as_uint(l), false, false); l = __uint_as_float(rr[0]);
    }
    const float inv = 1.f / l;
    {
      char* ob = p.ws + WS_OA + (size_t)(qrow >> 5) * 36 * 1024 + l32 * 16;
      auto st2 = [&](int d, float x, float y) { const int c = ah * 18 + d; *(unsigned*)(ob + (c >> 4) * 1024 + ((c >> 2) & 1) * 512 + (((c >> 3) & 1) * 4 + (c & 3)) * 2) = cvtpk(x * inv, y * inv); };
      st2(4 * hi, o[0], o[1]); st2(4 * hi + 2, o[2], o[3]); st2(8 + 4 * hi, o[4], o[5]); st2(10 + 4 * hi, o[6], o[7]);
      if (hi == 0) st2(16, o[8], o[9]);
    }
    STAMP(p.ws, 1, 5);
  }
}

struct K3P { char* ws; const void* wo; const void* awo; void* out; };
constexpr int K3_WS = 24576;
struct K3C {
  unsigned lds0, w, xvoff, wvoff;
  __amdgpu_buffer_rsrc_t sX, sW;
  lds_cptr wb;
};
DI void xload(bf16x8& d, unsigned voff, __amdgpu_buffer_rsrc_t srd, unsigned soff) {
  asm volatile("buffer_load_dwordx4 %0, %1, %2, %3 offen" : "=v"(d) : "v"(voff), "s"(srd), "s"(soff) : "memory");
}
template <int I> DI void k3_dmaW(const K3C& c, int st) {
  constexpr int gg = I / 3, cp = I % 3;
  dma16(c.lds0 + (st & 3) * K3_WS + ((2 * c.w + gg) * 3 + cp) * 1024, c.wvoff + cp * 128, c.sW, (unsigned)((st * 64 + (2 * c.w + gg) * 8) * 1152));
}
struct K3F { s16x4 wl[6], wh[6]; };
template <int KS, int D0, int NDMA> DI void k3_kstep(const K3C& c, f32x16 (&acc)[6], bf16x8& x, const K3F& cur, K3F& nxt, int st, int slot_next_off, unsigned xso) {
  constexpr int KN = (KS + 1) & 3;
#pragma unroll
  for (int m = 0; m < 6; ++m) {
    acc[m] = MFMA(cat(cur.wl[m], cur.wh[m]), x, acc[m]);
    lds_cptr p = c.wb + slot_next_off + (2 * KN) * 3072 + m * 512;
    nxt.wl[m] = lds_tr(p); nxt.wh[m] = lds_tr(p + 3072);
    if (m == 1) k3_dmaW<D0>(c, st + 3);
    if (m == 3 && NDMA >= 2) k3_dmaW<(D0 + 1) % 6>(c, st + 3);
    SBAR();
  }
  xload(x, c.xvoff, c.sX, xso + KS * 1024);
  SBAR();
}
__global__ __launch_bounds__(256, 1) void k3_out(K3P p) {
  extern __shared__ __attribute__((aligned(16))) char lds[];
  const int tid = threadIdx.x, lane = tid & 63, l32 = lane & 31, hi = lane >> 5;
  const int b = blockIdx.x;
  const bool isB = b < 192;
  int rb, cb, sk;
  if (isB) { const int t = b >> 2; sk = b & 3; rb = t / 3; cb = t - rb * 3; } else { const int t = b - 192; sk = 0; rb = t / 3; cb = t - rb * 3; }
  K3C c;
  c.lds0 = (unsigned)(size_t)(lds_ptr)lds; c.w = rfl(tid >> 6);
  const int blk = (lane >> 4) & 1, q4 = (lane & 15) >> 2, p4 = lane & 3;
  const int nst = isB ? 16 : 9;
  c.sW = isB ? mksrd((const char*)p.wo + (size_t)sk * 1024 * 1152 + cb * 384, 1024 * 1152 - cb * 384) : mksrd((const char*)p.awo + cb * 384, 576 * 1152 - cb * 384);
  {
    const char* xb = isB ? p.ws + WS_CF + ((size_t)(rb * 4 + c.w) * 256 + sk * 64) * 1024 : p.ws + WS_OA + (size_t)(rb * 4 + c.w) * 36 * 1024;
    c.sX = mksrd(xb, nst * 4096);
  }
  c.xvoff = lane * 16;
  { const int sub = lane >> 5, rr = (lane & 31) >> 2, c4 = lane & 3; c.wvoff = (unsigned)(rr * 1152 + (32 * sub + 8 * c4) * 2); }
  c.wb = (lds_cptr)lds + 32 * blk + 8 * p4 + (4 * hi + q4) * 64;
  f32x16 acc[6];
#pragma unroll
  for (int ct = 0; ct < 6; ++ct) acc[ct] = f32x16{};
  bf16x8 xa[4], xb_[4];
  STAMP(p.ws, 2, 0);
  k3_dmaW<0>(c, 0); k3_dmaW<1>(c, 0); k3_dmaW<2>(c, 0); k3_dmaW<3>(c, 0); k3_dmaW<4>(c, 0); k3_dmaW<5>(c, 0);
#pragma unroll
  for (int ks = 0; ks < 4; ++ks) xload(xa[ks], c.xvoff, c.sX, ks * 1024);
  k3_dmaW<0>(c, 1); k3_dmaW<1>(c, 1); k3_dmaW<2>(c, 1); k3_dmaW<3>(c, 1); k3_dmaW<4>(c, 1); k3_dmaW<5>(c, 1);
#pragma unroll
  for (int ks = 0; ks < 4; ++ks) xload(xb_[ks], c.xvoff, c.sX, 4096 + ks * 1024);
  k3_dmaW<0>(c, 2); k3_dmaW<1>(c, 2); k3_dmaW<2>(c, 2); k3_dmaW<3>(c, 2); k3_dmaW<4>(c, 2); k3_dmaW<5>(c, 2);
  K3F fa, fb;
#define K3_WAIT(N, X) asm volatile("s_waitcnt vmcnt(" #N ")\n\ts_barrier" : "+v"(X[0]), "+v"(X[1]), "+v"(X[2]), "+v"(X[3]) :: "memory")
#define K3_STAGE(X, st) do { \
    const int so = ((st) & 3) * K3_WS, sn = (((st) + 1) & 3) * K3_WS; const unsigned xso = (unsigned)(((st) + 2) * 4096); \
    k3_kstep<0, 0, 2>(c, acc, X[0], fa, fb, st, so, xso); \
    k3_kstep<1, 2, 2>(c, acc, X[1], fb, fa, st, so, xso); \
    k3_kstep<2, 4, 1>(c, acc, X[2], fa, fb, st, so, xso); \
    k3_kstep<3, 5, 1>(c, acc, X[3], fb, fa, st, sn, xso); } while (0)
  K3_WAIT(10, xa); SBAR();
  STAMP(p.ws, 2, 1);
#pragma unroll
  for (int m = 0; m < 6; ++m) { fa.wl[m] = lds_tr(c.wb + m * 512); fa.wh[m] = lds_tr(c.wb + m * 512 + 3072); }
  SBAR();
  int st = 0;
  for (; st + 1 < nst; st += 2) {
    K3_STAGE(xa, st);
    K3_WAIT(10, xb_); SBAR();
    K3_STAGE(xb_, st + 1);
    K3_WAIT(10, xa); SBAR();
  }
  if (st < nst) K3_STAGE(xa, st);
  STAMP(p.ws, 2, 2);
  asm volatile("s_waitcnt vmcnt(0)" : "+v"(xa[0]), "+v"(xa[1]), "+v"(xa[2]), "+v"(xa[3]) :: "memory");
  asm volatile("" : "+v"(xb_[0]), "+v"(xb_[1]), "+v"(xb_[2]), "+v"(xb_[3]));
  if (isB) {
    const __amdgpu_buffer_rsrc_t sP = mksrd(p.ws + WS_PART + ((size_t)(sk * 48 + (rb * 3 + cb)) * 4 + c.w) * 24576, 24576);
#pragma unroll
    for (int ct = 0; ct < 6; ++ct)
#pragma unroll
      for (int g = 0; g < 4; ++g)
        __builtin_amdgcn_raw_buffer_store_b128(__builtin_bit_cast(u32x4, f32x4{acc[ct][4 * g], acc[ct][4 * g + 1], acc[ct][4 * g + 2], acc[ct][4 * g + 3]}), sP, lane * 16 + (ct * 4 + g) * 1024, 0, ST_AUX);
  } else {
    const int row = rb * 128 + c.w * 32 + l32;
#pragma unroll
    for (int ct = 0; ct < 6; ++ct)
#pragma unroll
      for (int g = 0; g < 4; ++g)
        *(u32x2*)((char*)p.out + (size_t)row * 2304 + (cb * 192 + ct * 32 + 8 * g + 4 * hi) * 2) = u32x2{cvtpk(acc[ct][4 * g], acc[ct][4 * g + 1]), cvtpk(acc[ct][4 * g + 2], acc[ct][4 * g + 3])};
  }
  asm volatile("s_waitcnt vmcnt(0)" ::: "memory");
  STAMP(p.ws, 2, 3);
}
__global__ __launch_bounds__(256) void k4_red(const char* part, char* out) {
  const int i = blockIdx.x * 256 + threadIdx.x;
  const int lane = i & 63, it = i >> 6;
  const int g = it & 3, ct = (it >> 2) % 6, tw = (it >> 2) / 6, w = tw & 3, tile = tw >> 2;
  const int rb = tile / 3, cb = tile - rb * 3;
  f32x4 s = *(const f32x4*)(part + (size_t)i * 16);
#pragma unroll
  for (int k = 1; k < 4; ++k) s += *(const f32x4*)(part + (size_t)k * 48 * 4 * 24576 + (size_t)i * 16);
  const int row = rb * 128 + w * 32 + (lane & 31), col = cb * 192 + ct * 32 + 8 * g + 4 * (lane >> 5);
  *(u32x2*)(out + (size_t)row * 2304 + 1152 + col * 2) = u32x2{cvtpk(s[0], s[1]), cvtpk(s[2], s[3])};
}

extern "C" void kernel_launch(void* const* d_in, const int* in_sizes, int n_in, void* d_out, int out_size, void* d_ws, size_t ws_size, hipStream_t stream) {
  static bool attr = false;
  if (!attr) {
    hipFuncSetAttribute((const void*)k1_proj, hipFuncAttributeMaxDynamicSharedMemorySize, 163840);
    hipFuncSetAttribute((const void*)k2_attn, hipFuncAttributeMaxDynamicSharedMemorySize, 163840);
    hipFuncSetAttribute((const void*)k3_out, hipFuncAttributeMaxDynamicSharedMemorySize, 4 * K3_WS);
    attr = true;
  }
  K1P p1; p1.cx = d_in[6]; p1.bx = d_in[5]; p1.wuk = d_in[8]; p1.wuv = d_in[9]; p1.wq = d_in[7];
  p1.ax = d_in[0]; p1.awq = d_in[1]; p1.awk = d_in[2]; p1.awv = d_in[3]; p1.ws = (char*)d_ws;
  k1_proj<<<256, 512, 163840, stream>>>(p1);
  K2P p2; p2.ws = (char*)d_ws;
  k2_attn<<<256, 512, 163840, stream>>>(p2);
  K3P p3; p3.ws = (char*)d_ws; p3.wo = d_in[10]; p3.awo = d_in[4]; p3.out = d_out;
  k3_out<<<240, 256, 4 * K3_WS, stream>>>(p3);
  k4_red<<<1152, 256, 0, stream>>>((const char*)d_ws + WS_PART, (char*)d_out);
}
```

```cpp
#include <hip/hip_runtime.h>
#include <hip/hip_bf16.h>
#include <cstdio>
#include <cstdint>
#include <utility>

#define PUBMODE 0
#define WARM 1
#define ST_AUX 0
#define DI __device__ __forceinline__
typedef short bf16x8 __attribute__((ext_vector_type(8)));
typedef short s16x4 __attribute__((ext_vector_type(4)));
typedef float f32x16 __attribute__((ext_vector_type(16)));
typedef float f32x4 __attribute__((ext_vector_type(4)));
typedef float f32x2 __attribute__((ext_vector_type(2)));
typedef __bf16 bf16x2_t __attribute__((ext_vector_type(2)));
typedef unsigned u32x4 __attribute__((ext_vector_type(4)));
typedef unsigned u32x2 __attribute__((ext_vector_type(2)));
typedef __attribute__((address_space(3))) const char* lds_cptr;
typedef __attribute__((address_space(3))) char* lds_ptr;
#define SBAR() __builtin_amdgcn_sched_barrier(0)
#define PIN(x) asm volatile("" : "+v"(x))
#define MFMA(a, b, c) __builtin_amdgcn_mfma_f32_32x32x16_bf16(a, b, c, 0, 0, 0)
#define SRD_W3 0x00020000

DI unsigned rfl(unsigned x) { return __builtin_amdgcn_readfirstlane(x); }
DI unsigned cvtpk(float lo, float hi) { f32x2 v = {lo, hi}; bf16x2_t b = __builtin_convertvector(v, bf16x2_t); return __builtin_bit_cast(unsigned, b); }
DI void dma16(unsigned m0, unsigned voff, __amdgpu_buffer_rsrc_t srd, unsigned soff) {
  asm volatile("s_mov_b32 m0, %0\n\ts_nop 0\n\tbuffer_load_dwordx4 %1, %2, %3 offen lds" :: "s"(m0), "v"(voff), "s"(srd), "s"(soff) : "m0", "memory");
}
template <int OFF> DI void dma16o(unsigned m0, unsigned voff, __amdgpu_buffer_rsrc_t srd, unsigned soff) {
  asm volatile("s_mov_b32 m0, %0\n\ts_nop 0\n\tbuffer_load_dwordx4 %1, %2, %3 offen offset:%4 lds" :: "s"(m0), "v"(voff), "s"(srd), "s"(soff), "i"(OFF) : "m0", "memory");
}
DI void dma4(unsigned m0, unsigned voff, __amdgpu_buffer_rsrc_t srd, unsigned soff) {
  asm volatile("s_mov_b32 m0, %0\n\ts_nop 0\n\tbuffer_load_dword %1, %2, %3 offen lds" :: "s"(m0), "v"(voff), "s"(srd), "s"(soff) : "m0", "memory");
}
DI __amdgpu_buffer_rsrc_t mksrd(const void* p, unsigned bytes) { return __builtin_amdgcn_make_buffer_rsrc((void*)p, (short)0, (int)bytes, SRD_W3); }
DI bf16x8 bld16(__amdgpu_buffer_rsrc_t srd, unsigned voff, unsigned soff) { return __builtin_bit_cast(bf16x8, __builtin_amdgcn_raw_buffer_load_b128(srd, voff, soff, 0)); }
DI void bst16(bf16x8 v, __amdgpu_buffer_rsrc_t srd, unsigned voff, unsigned soff) { __builtin_amdgcn_raw_buffer_store_b128(__builtin_bit_cast(u32x4, v), srd, voff + soff, 0, ST_AUX); }
template <int N> DI void wait_bar() { asm volatile("s_waitcnt vmcnt(%0)\n\ts_barrier" :: "i"(N) : "memory"); }
DI bf16x8 lds_rd16(lds_cptr p) { return *(const __attribute__((address_space(3))) bf16x8*)p; }
DI s16x4 lds_tr(lds_cptr p) { return __builtin_bit_cast(s16x4, __builtin_amdgcn_ds_read_tr16_b64_v4i16((__attribute__((address_space(3))) s16x4*)p)); }
DI bf16x8 cat(s16x4 a, s16x4 b) { return __builtin_shufflevector(a, b, 0, 1, 2, 3, 4, 5, 6, 7); }
DI bf16x8 pack8(const f32x16& x, int g, float sc) {
  u32x4 w;
#pragma unroll
  for (int e = 0; e < 4; ++e) w[e] = cvtpk(x[8 * g + 2 * e] * sc, x[8 * g + 2 * e + 1] * sc);
  return __builtin_bit_cast(bf16x8, w);
}
DI bf16x8 pack8n(const f32x16& x, int g) {
  u32x4 w;
#pragma unroll
  for (int e = 0; e < 4; ++e) w[e] = cvtpk(x[8 * g + 2 * e], x[8 * g + 2 * e + 1]);
  return __builtin_bit_cast(bf16x8, w);
}

#define DBG_S 1
#define STAMP(ws, k, i)
DI float hmax(float m) { auto rr = __builtin_amdgcn_permlane32_swap(__float_as_uint(m), __float_as_uint(m), false, false); return fmaxf(__uint_as_float(rr[0]), __uint_as_float(rr[1])); }
DI float hsum(float m) { auto rr = __builtin_amdgcn_permlane32_swap(__float_as_uint(m), __float_as_uint(m), false, false); return __uint_as_float(rr[0]) + __uint_as_float(rr[1]); }
constexpr int S = 2048, H = 32, DB = 128, MB = 576, DC = 512, DA = 576, DHA = 18;
constexpr float CB = 0.08838834764831845f * 1.4426950408889634f;
constexpr float CA = 0.23570226039551584f * 1.4426950408889634f;
constexpr size_t WS_KF = 0;
constexpr size_t WS_VF = WS_KF + (size_t)H * S * DB * 2;
constexpr size_t WS_QF = WS_VF + (size_t)H * S * DB * 2;
constexpr size_t WS_CF = WS_QF + (size_t)H * S * DB * 2;
constexpr size_t WS_QA = WS_CF + (size_t)H * S * DB * 2;
constexpr int AHB = 88064;
constexpr size_t WS_KA = WS_QA + (size_t)S * DA * 2 + 256;
constexpr size_t WS_KMAX = WS_KA + 2 * AHB;
constexpr size_t WS_OA = WS_KMAX + 1024;
constexpr size_t WS_PART = WS_OA + (size_t)S * DA * 2;
constexpr size_t WS_FLAG = WS_PART + (size_t)4 * S * 576 * 4;
constexpr size_t WS_END = WS_FLAG + 256 * 128;

struct K1P {
  const void *cx, *bx, *wuk, *wuv, *wq;
  const void *ax, *awq, *awk, *awv;
  const void *wo, *awo;
  char* ws;
};
constexpr int XSLOT = 32768, WRING = 98304, WSLOT = 16384;
template <int PART> constexpr int NSTEP = PART == 0 ? 16 : 18;
template <int PART> constexpr int NXT = PART == 0 ? 8 : 9;
template <int PART> constexpr int nWp(int s) { return s < NSTEP<PART> ? (PART == 0 ? 2 : 1) : 0; }
template <int PART> constexpr int nXp(int g) { return ((g >> 1) + 2) < NXT<PART> ? 2 : 0; }
template <int PART> constexpr int nD(int g) { return nWp<PART>(g + 3) + nXp<PART>(g); }
template <int PART> constexpr int waitN(int g) { return g == 0 ? nWp<PART>(2) : nD<PART>(g - 1); }

struct K1C {
  unsigned lds0, w;
  unsigned xoffC, xoffX;
  unsigned woff;
  __amdgpu_buffer_rsrc_t sC, sX, sUK, sUV, sWQ;
  lds_cptr xb[4];
  lds_cptr wb;
  unsigned rowL, rowS;
};
DI void st16_sc1(bf16x8 v, unsigned voff, __amdgpu_buffer_rsrc_t srd, unsigned soff) {
  asm volatile("buffer_store_dwordx4 %0, %1, %2, %3 offen sc1\n\ts_nop 1" :: "v"(v), "v"(voff), "s"(srd), "s"(soff) : "memory");
}
template <int PART, int T, int i> DI void k1_x1(const K1C& c) {
  const unsigned m0 = c.lds0 + (T % 3) * XSLOT + i * 8192 + c.w * 1024;
  if constexpr (PART == 0) dma16(m0, c.xoffC, c.sC, (unsigned)(i * 64 * 1024 + T * 128));
  else                     dma16(m0, c.xoffX, c.sX, (i < 2 ? c.rowL : c.rowS) + (unsigned)((i & 1) * 64 * 1152 + T * 128));
}
template <int PART, int T> DI void k1_issueX(const K1C& c) { k1_x1<PART, T, 0>(c); k1_x1<PART, T, 1>(c); k1_x1<PART, T, 2>(c); k1_x1<PART, T, 3>(c); }
template <int PART, int G, int I> DI void k1_w1(const K1C& c) {
  const unsigned m0 = c.lds0 + WRING + (G & 3) * WSLOT + c.w * 1024;
  if constexpr (PART == 0) { if constexpr (I == 0) dma16(m0, c.woff, c.sUK, (unsigned)(G * 32 * 8192)); else dma16(m0 + 8192, c.woff, c.sUV, (unsigned)(G * 32 * 8192)); }
  else if constexpr (I == 0) dma16(m0, c.woff, c.sWQ, (unsigned)(G * 32 * 8192));
}
template <int PART, int G> DI void k1_issueW(const K1C& c) { k1_w1<PART, G, 0>(c); k1_w1<PART, G, 1>(c); }
struct FragKV { bf16x8 x[4]; s16x4 wl[2], wh[2]; };
struct FragQ { bf16x8 x[2]; s16x4 wl[2], wh[2]; };
template <int G, int SS, int I> DI void kv_ld(const K1C& c, FragKV& f) {
  constexpr int T = G >> 1;
  lds_cptr xp = c.xb[2 * (G & 1) + SS] + (T % 3) * XSLOT;
  lds_cptr wp = c.wb + (G & 3) * WSLOT + SS * 4096;
  if constexpr (I == 0) f.x[0] = lds_rd16(xp);
  else if constexpr (I == 1) f.wl[0] = lds_tr(wp);
  else if constexpr (I == 2) f.wh[0] = lds_tr(wp + 256);
  else if constexpr (I == 3) f.wl[1] = lds_tr(wp + 512);
  else if constexpr (I == 4) f.wh[1] = lds_tr(wp + 768);
  else f.x[I - 4] = lds_rd16(xp + (I - 4) * 4096);
}
template <int PART, int G, int I> DI void k1_dma_piece(const K1C& c) {
  constexpr int GW = G + 3, TX = (G >> 1) + 2;
  if constexpr (I < 2) { if constexpr (GW < NSTEP<PART>) k1_w1<PART, GW, I>(c); }
  else if constexpr (TX < NXT<PART>) k1_x1<PART, TX, 2 * (G & 1) + (I - 2)>(c);
}
template <bool ISV, int M> DI void kv_mfma(f32x16 (&acc)[4][2], const FragKV& f) {
  constexpr int r = M >> 1, ct = M & 1;
  const bf16x8 wf = cat(f.wl[ct], f.wh[ct]);
  acc[r][ct] = ISV ? MFMA(f.x[r], wf, acc[r][ct]) : MFMA(wf, f.x[r], acc[r][ct]);
}
template <bool ISV, int GN, int SN, int DMAG, bool LOAD, int M> DI void kv_gap(const K1C& c, f32x16 (&acc)[4][2], const FragKV& cur, FragKV& nxt) {
  kv_mfma<ISV, M>(acc, cur);
  if constexpr (LOAD) kv_ld<GN, SN, M>(c, nxt);
  if constexpr (DMAG >= 0 && (M & 1) && M < 8) k1_dma_piece<0, DMAG, (M >> 1)>(c);
  SBAR();
}
template <bool ISV, int GN, int SN, int DMAG, bool LOAD> DI void kv_phase(const K1C& c, f32x16 (&acc)[4][2], const FragKV& cur, FragKV& nxt) {
  kv_gap<ISV, GN, SN, DMAG, LOAD, 0>(c, acc, cur, nxt); kv_gap<ISV, GN, SN, DMAG, LOAD, 1>(c, acc, cur, nxt);
  kv_gap<ISV, GN, SN, DMAG, LOAD, 2>(c, acc, cur, nxt); kv_gap<ISV, GN, SN, DMAG, LOAD, 3>(c, acc, cur, nxt);
  kv_gap<ISV, GN, SN, DMAG, LOAD, 4>(c, acc, cur, nxt); kv_gap<ISV, GN, SN, DMAG, LOAD, 5>(c, acc, cur, nxt);
  kv_gap<ISV, GN, SN, DMAG, LOAD, 6>(c, acc, cur, nxt); kv_gap<ISV, GN, SN, DMAG, LOAD, 7>(c, acc, cur, nxt);
}
template <int G, bool ISV> DI void k1_step_kv(const K1C& c, f32x16 (&acc)[4][2], FragKV& fa, FragKV& fb) {
  wait_bar<waitN<0>(G)>(); SBAR();
  if constexpr (G == 0) {
    kv_ld<0, 0, 0>(c, fa); kv_ld<0, 0, 1>(c, fa); kv_ld<0, 0, 2>(c, fa); kv_ld<0, 0, 3>(c, fa);
    kv_ld<0, 0, 4>(c, fa); kv_ld<0, 0, 5>(c, fa); kv_ld<0, 0, 6>(c, fa); kv_ld<0, 0, 7>(c, fa); SBAR();
  }
  kv_phase<ISV, G, 1, G, true>(c, acc, fa, fb);
  kv_phase<ISV, G + 1, 0, -1, (G < 15)>(c, acc, fb, fa);
}
template <int G, bool ISV> struct KVLoop { static DI void run(const K1C& c, f32x16 (&acc)[4][2], FragKV& fa, FragKV& fb) { k1_step_kv<G, ISV>(c, acc, fa, fb); if constexpr (G < 15) KVLoop<G + 1, ISV>::run(c, acc, fa, fb); } };

template <int G, int SS, int I> DI void q_ld(const K1C& c, FragQ& f) {
  constexpr int T = G >> 1;
  lds_cptr xp = c.xb[2 * (G & 1) + SS] + (T % 3) * XSLOT;
  lds_cptr wp = c.wb + (G & 3) * WSLOT + SS * 4096;
  if constexpr (I == 0) f.x[0] = lds_rd16(xp);
  else if constexpr (I == 1) f.wl[0] = lds_tr(wp);
  else if constexpr (I == 2) f.wh[0] = lds_tr(wp + 256);
  else if constexpr (I == 3) f.x[1] = lds_rd16(xp + 4096);
  else if constexpr (I == 4) f.wl[1] = lds_tr(wp + 512);
  else f.wh[1] = lds_tr(wp + 768);
}
template <int M> DI void q_mfma(f32x16 (&acc)[2][2], const FragQ& f) {
  constexpr int r = M & 1, ct = M >> 1;
  acc[r][ct] = MFMA(cat(f.wl[ct], f.wh[ct]), f.x[r], acc[r][ct]);
}
template <int GN, int SN, int DMAG, bool LOAD> DI void q_phase(const K1C& c, f32x16 (&acc)[2][2], const FragQ& cur, FragQ& nxt) {
  q_mfma<0>(acc, cur); if constexpr (LOAD) { q_ld<GN, SN, 0>(c, nxt); q_ld<GN, SN, 1>(c, nxt); } SBAR();
  q_mfma<1>(acc, cur); if constexpr (LOAD) { q_ld<GN, SN, 2>(c, nxt); q_ld<GN, SN, 3>(c, nxt); } if constexpr (DMAG >= 0) k1_dma_piece<1, DMAG, 0>(c); SBAR();
  q_mfma<2>(acc, cur); if constexpr (LOAD) { q_ld<GN, SN, 4>(c, nxt); } if constexpr (DMAG >= 0) k1_dma_piece<1, DMAG, 2>(c); SBAR();
  q_mfma<3>(acc, cur); if constexpr (LOAD) { q_ld<GN, SN, 5>(c, nxt); } if constexpr (DMAG >= 0) k1_dma_piece<1, DMAG, 3>(c); SBAR();
}
template <int G> DI void k1_step_q(const K1C& c, f32x16 (&acc)[2][2], FragQ& fa, FragQ& fb) {
  wait_bar<waitN<1>(G)>(); SBAR();
  if constexpr (G == 0) {
    q_ld<G, 0, 0>(c, fa); q_ld<G, 0, 1>(c, fa); q_ld<G, 0, 2>(c, fa); q_ld<G, 0, 3>(c, fa); q_ld<G, 0, 4>(c, fa); q_ld<G, 0, 5>(c, fa); SBAR();
  }
  q_phase<G, 1, G, true>(c, acc, fa, fb);
  q_phase<G + 1, 0, -1, (G < 17)>(c, acc, fb, fa);
}
template <int G> struct QLoop { static DI void run(const K1C& c, f32x16 (&acc)[2][2], FragQ& fa, FragQ& fb) { k1_step_q<G>(c, acc, fa, fb); if constexpr (G < 17) QLoop<G + 1>::run(c, acc, fa, fb); } };

constexpr int relK(int i) { return ((i >> 2) * 8 + ((i >> 1) & 1) * 2 + (i & 1)) * 1024; }
constexpr int relV(int i) { return (((i >> 2) >> 1) * 16 + (2 * ((i >> 2) & 1) + (i & 1)) * 4 + ((i >> 1) & 1)) * 1024; }
template <bool ISV> DI void k1_kv(K1C& c, bf16x8 (&kvp)[16]) {
  f32x16 acc[4][2];
#pragma unroll
  for (int r = 0; r < 4; ++r) { acc[r][0] = f32x16{}; acc[r][1] = f32x16{}; }
  FragKV fa, fb;
  KVLoop<0, ISV>::run(c, acc, fa, fb);
#pragma unroll
  for (int r = 0; r < 4; ++r)
#pragma unroll
    for (int ct = 0; ct < 2; ++ct)
#pragma unroll
      for (int g = 0; g < 2; ++g) kvp[r * 4 + ct * 2 + g] = pack8n(acc[r][ct], g);
  SBAR();
}

__global__ __launch_bounds__(512, 2) void k0_aproj(K1P p) {
  extern __shared__ __attribute__((aligned(16))) char lds[];
  const int tid = threadIdx.x, lane = tid & 63, l32 = lane & 31, hi = lane >> 5;
  const int b = blockIdx.x, xcd = b & 7, ii = b >> 3;
  struct { unsigned lds0; } c; c.lds0 = (unsigned)(size_t)(lds_ptr)lds;
  const int w = rfl(tid >> 6);
  const int blk = (lane >> 4) & 1, q4 = (lane & 15) >> 2, p4 = lane & 3;
  if (tid == 0) *(unsigned*)(p.ws + WS_FLAG + b * 128) = 0u;
  if (ii < 20) {
    const int rg = 4 * xcd + ii / 5, cg = ii % 5;
    const __amdgpu_buffer_rsrc_t sAX = mksrd((const char*)p.ax + (size_t)rg * 64 * 1152, 64 * 1152);
    const __amdgpu_buffer_rsrc_t sAQ = mksrd((const char*)p.awq + cg * 256, 576 * 1152 - cg * 256);
    constexpr int ASL = 73728, AWO = 24576;
    const unsigned xv = (unsigned)((lane >> 3) * 1152 + (((lane & 7) ^ ((4 * (w & 1) + (lane >> 4)) & 7)) * 16));
    const unsigned wv = (unsigned)(((lane & 31) >> 2) * 1152 + (32 * (lane >> 5) + 8 * (lane & 3)) * 2);
    auto issue = [&](int th) {
      const unsigned base = c.lds0 + (th & 1) * ASL;
#pragma unroll
      for (int kc = 0; kc < 3; ++kc) dma16(rfl(base + (kc * 8 + w) * 1024), xv, sAX, rfl((unsigned)(w * 8 * 1152 + (th * 3 + kc) * 128)));
#pragma unroll
      for (int i = 0; i < 6; ++i) {
        const int g = 3 * w + (i >> 1), pr = i & 1;
        dma16(rfl(base + AWO + (g * 2 + pr) * 1024), wv, sAQ, rfl((unsigned)((th * 192 + g * 8) * 1152 + pr * 128)));
      }
    };
    const int rt = w >> 2, ct = w & 3, tile = 4 * cg + ct;
    f32x16 acc = f32x16{};
    issue(0); issue(1);
    for (int th = 0; th < 3; ++th) {
      if (th < 2) asm volatile("s_waitcnt vmcnt(9) lgkmcnt(0)\n\ts_barrier" ::: "memory");
      else asm volatile("s_waitcnt vmcnt(0) lgkmcnt(0)\n\ts_barrier" ::: "memory");
      lds_cptr xp = (lds_cptr)lds + (th & 1) * ASL + rt * 4096 + 128 * l32;
      lds_cptr wp = (lds_cptr)lds + (th & 1) * ASL + AWO + ct * 512 + 2048 * hi + 64 * q4 + 32 * blk + 8 * p4;
      bf16x8 xf[4]; s16x4 wl[4], wh[4];
#define ALD(k) do { xf[(k) & 3] = lds_rd16(xp + ((k) >> 2) * 8192 + 16 * ((2 * ((k) & 3) + hi) ^ ((l32 >> 1) & 7))); wl[(k) & 3] = lds_tr(wp + (k) * 4096); wh[(k) & 3] = lds_tr(wp + (k) * 4096 + 256); } while (0)
      if (tile < 18) {
        ALD(0); ALD(1); ALD(2);
#pragma unroll
        for (int k = 0; k < 12; ++k) {
          if (k + 3 < 12) ALD(k + 3);
          acc = MFMA(cat(wl[k & 3], wh[k & 3]), xf[k & 3], acc);
          SBAR();
        }
      }
      if (th == 0) { asm volatile("s_waitcnt lgkmcnt(0)\n\ts_barrier" ::: "memory"); issue(2); }
    }
    const int row = (rg * 2 + rt) * 32 + l32;
    if (tile < 18) {
#pragma unroll
      for (int g = 0; g < 4; ++g) {
        u32x2 v = {cvtpk(acc[4 * g] * CA, acc[4 * g + 1] * CA), cvtpk(acc[4 * g + 2] * CA, acc[4 * g + 3] * CA)};
        *(u32x2*)(p.ws + WS_QA + (size_t)row * 1152 + (tile * 32 + 8 * g + 4 * hi) * 2) = v;
      }
    }
  } else if (ii < 28) {
    const int r32 = 8 * xcd + (ii - 20);
    const __amdgpu_buffer_rsrc_t sAX = mksrd((const char*)p.ax + (size_t)r32 * 32 * 1152, 32 * 1152);
    for (int pc = w; pc < 36; pc += 8) {
      const int kc = pc >> 2, rq = pc & 3, r = lane >> 3, row = 8 * rq + r, ch = (lane & 7) ^ ((row >> 1) & 7);
      dma16(rfl(c.lds0 + pc * 1024), (unsigned)(row * 1152 + ch * 16), sAX, rfl((unsigned)(kc * 128)));
    }
    {
      const unsigned* gk = (const unsigned*)p.awk; const unsigned* gv = (const unsigned*)p.awv;
      unsigned vk[11], vv[11];
#pragma unroll
      for (int it = 0; it < 11; ++it) { const int i = tid + it * 512; const int ic = i < 5184 ? i : 0; vk[it] = gk[ic]; vv[it] = gv[ic]; }
#pragma unroll
      for (int it = 0; it < 11; ++it) {
        const int i = tid + it * 512;
        if (i < 5184) { const int r = i / 9, d = i - r * 9; char* dst = lds + 36864 + (r >> 3) * 1024 + (r & 7) * 64 + d * 4; *(unsigned*)dst = vk[it]; *(unsigned*)(dst + 512) = vv[it]; }
      }
    }
    asm volatile("s_waitcnt vmcnt(0) lgkmcnt(0)\n\ts_barrier" ::: "memory");
    const int isv = w >> 2, kq = w & 3;
    f32x16 acc = f32x16{};
    lds_cptr xp = (lds_cptr)lds + 128 * l32;
    lds_cptr wp = (lds_cptr)lds + 36864 + isv * 512 + 1024 * hi + 64 * q4 + 32 * blk + 8 * p4;
#pragma unroll
    for (int k9 = 0; k9 < 9; ++k9) {
      const int k = kq * 9 + k9;
      const bf16x8 xf = lds_rd16(xp + (k >> 2) * 4096 + 16 * ((2 * (k & 3) + hi) ^ ((l32 >> 1) & 7)));
      const bf16x8 wf = cat(lds_tr(wp + k * 2048), lds_tr(wp + k * 2048 + 256));
      if (isv) acc = MFMA(xf, wf, acc); else acc = MFMA(wf, xf, acc);
    }
    asm volatile("s_waitcnt lgkmcnt(0)\n\ts_barrier" ::: "memory");
    float* red = (float*)lds + w * 1024;
#pragma unroll
    for (int q = 0; q < 4; ++q) *(f32x4*)(red + q * 256 + lane * 4) = f32x4{acc[4 * q], acc[4 * q + 1], acc[4 * q + 2], acc[4 * q + 3]};
    __syncthreads();
    if (kq == 0) {
#pragma unroll
      for (int o = 1; o < 4; ++o)
#pragma unroll
        for (int q = 0; q < 4; ++q) { const f32x4 y = *(const f32x4*)(red + o * 1024 + q * 256 + lane * 4); acc[4 * q] += y[0]; acc[4 * q + 1] += y[1]; acc[4 * q + 2] += y[2]; acc[4 * q + 3] += y[3]; }
      const int row = r32 * 32 + l32;
      char* hb = p.ws + WS_KA + (size_t)(r32 >> 5) * AHB; const int rl = r32 & 31;
      if (!isv) {
        char* k0 = hb + rl * 1024;
        const unsigned w0 = cvtpk(acc[0], acc[1]), w1 = cvtpk(acc[2], acc[3]), w2 = cvtpk(acc[4], acc[5]), w3 = cvtpk(acc[6], acc[7]), w4 = cvtpk(acc[8], acc[9]);
        *(u32x2*)(k0 + (l32) * 16 + 8 * hi) = u32x2{w0, w1};
        *(u32x2*)(k0 + (l32 + 32) * 16 + 8 * hi) = u32x2{w2, w3};
        if (hi == 0) *(u32x4*)(hb + 32768 + (size_t)(rl * 32 + l32) * 16) = u32x4{w4, 0x00003F80u, 0u, 0u};
        auto sq = [](unsigned x) { const float a = __uint_as_float(x << 16), bq = __uint_as_float(x & 0xFFFF0000u); return a * a + bq * bq; };
        float s2 = sq(w0) + sq(w1) + sq(w2) + sq(w3) + (hi == 0 ? sq(w4) : 0.f);
        s2 = hsum(s2);
#pragma unroll
        for (int o = 16; o >= 1; o >>= 1) s2 = fmaxf(s2, __shfl_xor(s2, o));
        if (lane == 0) *(float*)(p.ws + WS_KMAX + r32 * 4) = s2;
      } else {
#pragma unroll
        for (int g = 0; g < 2; ++g) {
          bf16x8 v = pack8n(acc, g);
          if (l32 == 18) v = bf16x8{0x3F80, 0x3F80, 0x3F80, 0x3F80, 0x3F80, 0x3F80, 0x3F80, 0x3F80};
          if (l32 < 19) *(bf16x8*)(hb + 49152 + ((size_t)((2 * rl + g) * 2 + hi) * 19 + l32) * 16) = v;
        }
      }
    }
  }

}

constexpr float LTHR = 7.9228162514264338e28f;
#define LTHR_V LTHR
struct AttB {
  f32x16 o[4]; bf16x8 qf[8]; f32x16 negm; float m, l;
  f32x16 s0, s1;
  unsigned pw[16];
  bf16x8 fr[4];
  float ps;
};
DI void cmask1(f32x16& s, int key0, int row) {
  const int d = row - key0;
#pragma unroll
  for (int r = 0; r < 16; ++r) { const int c = (r & 3) + 8 * (r >> 2); s[r] = (c > d) ? -INFINITY : s[r]; }
}
DI float max16(const f32x16& s) { float a = fmaxf(fmaxf(s[0], s[1]), s[2]);
#pragma unroll
  for (int r = 3; r < 15; r += 2) a = fmaxf(fmaxf(a, s[r]), s[r + 1]);
  return fmaxf(a, s[15]); }
DI f32x16 splat16(float x) { f32x16 v;
#pragma unroll
  for (int r = 0; r < 16; ++r) v[r] = x;
  return v; }
#define PKB(w) __builtin_bit_cast(bf16x8, w)
#define EXP2(x) __builtin_amdgcn_exp2f(x)
DI bf16x8 pfrag(const AttB& a, int ks) { u32x4 w = {a.pw[4 * ks], a.pw[4 * ks + 1], a.pw[4 * ks + 2], a.pw[4 * ks + 3]}; return __builtin_bit_cast(bf16x8, w); }

template <bool HASCUR> DI void rare_fix(AttB& a, __amdgpu_buffer_rsrc_t sK, __amdgpu_buffer_rsrc_t sQ, unsigned voff, unsigned kso, unsigned qso, int key0, int row, bool mask) {
  float tm = -INFINITY;
  f32x16 t;
#pragma unroll 1
  for (int kb = 0; kb < 2; ++kb) {
    t = f32x16{};
#pragma unroll 2
    for (int ds = 0; ds < 8; ++ds) t = MFMA(bld16(sK, voff, kso + kb * 8192 + ds * 1024), bld16(sQ, voff, qso + ds * 1024), t);
    if (mask) cmask1(t, key0 + 32 * kb, row);
    tm = fmaxf(tm, max16(t));
  }
  tm = hmax(tm);
  const float mn = fmaxf(a.m, tm), dl = mn - a.m, f = EXP2(-dl);
#pragma unroll
  for (int db = 0; db < 4; ++db)
#pragma unroll
    for (int r = 0; r < 16; ++r) a.o[db][r] *= f;
  float ps = 0.f;
#pragma unroll 1
  for (int kb = 0; kb < 2; ++kb) {
    t = f32x16{};
#pragma unroll 2
    for (int ds = 0; ds < 8; ++ds) t = MFMA(bld16(sK, voff, kso + kb * 8192 + ds * 1024), bld16(sQ, voff, qso + ds * 1024), t);
    if (mask) cmask1(t, key0 + 32 * kb, row);
#pragma unroll
    for (int r = 0; r < 16; ++r) { t[r] = EXP2(t[r] - mn); ps += t[r]; }
    if (kb == 0) {
#pragma unroll
      for (int k = 0; k < 8; ++k) a.pw[k] = cvtpk(t[2 * k], t[2 * k + 1]);
    } else {
#pragma unroll
      for (int k = 0; k < 8; ++k) a.pw[8 + k] = cvtpk(t[2 * k], t[2 * k + 1]);
    }
  }
  a.l *= f; a.ps = ps;
  if (HASCUR) {
#pragma unroll
    for (int r = 0; r < 16; ++r) a.s0[r] -= dl;
  }
  a.m = mn; a.negm = splat16(-mn); PIN(a.negm);
}

struct StepCtx {
  lds_cptr kp;
  lds_cptr kpn;
  lds_cptr vp;
  unsigned m0k, m0v, sok, sov; bool dk, dv;
  unsigned voff;
};
constexpr int M_STEADY = 0, M_FIRST = 1, M_SWITCH = 2, M_DRAIN = 3;
template <int I, int MODE> DI void gapA(AttB& a, const StepCtx& c, __amdgpu_buffer_rsrc_t sK, __amdgpu_buffer_rsrc_t sV) {
  constexpr bool PREV = MODE != M_FIRST, CUR = MODE != M_DRAIN, NEW = MODE == M_FIRST || MODE == M_SWITCH;
  constexpr int ds = I & 7;
  if constexpr (CUR) {
    if constexpr (I < 8) { if constexpr (ds == 0) a.s0 = NEW ? MFMA(a.fr[I & 3], a.qf[0], f32x16{}) : MFMA(a.fr[I & 3], a.qf[0], a.negm); else a.s0 = MFMA(a.fr[I & 3], a.qf[ds], a.s0); }
    else { if constexpr (ds == 0) a.s1 = NEW ? MFMA(a.fr[I & 3], a.qf[0], f32x16{}) : MFMA(a.fr[I & 3], a.qf[0], a.negm); else a.s1 = MFMA(a.fr[I & 3], a.qf[ds], a.s1); }
  }
  if constexpr (I + 3 < 16) { if constexpr (CUR) a.fr[(I + 3) & 3] = lds_rd16(c.kp + (I + 3) * 1024); }
  else if constexpr (PREV) a.fr[(I + 3) & 3] = lds_rd16(c.vp + (I + 3 - 16) * 1024);
  if constexpr (I < 8) {
    if constexpr (PREV) {
      a.s1[8 + I] = EXP2(a.s1[8 + I]);
      a.ps += a.s1[7 + I];
      if constexpr (I < 4) { a.pw[8 + I] = cvtpk(a.s1[2 * I], a.s1[2 * I + 1]); PIN(a.pw[8 + I]); }
      else if constexpr (I != 6) { constexpr int k = I == 7 ? 6 : I; a.pw[8 + k] = cvtpk(a.s1[2 * k], a.s1[2 * k + 1]); PIN(a.pw[8 + k]); }
    }
    if constexpr (I & 1) dma16o<(I >> 1) * 1024>(c.m0v, c.voff, sV, c.sov);
  } else {
    constexpr int i = I - 8;
    if constexpr (MODE == M_STEADY) {
      a.s0[i] = EXP2(a.s0[i]);
      if constexpr (i >= 1) a.ps += a.s0[i - 1];
    }
    if constexpr (i & 1) dma16o<(i >> 1) * 1024>(c.m0k, c.voff, sK, c.sok);
  }
  PIN(a.ps);
  SBAR();
}
template <int I, int MODE> DI void gapB(AttB& a, const StepCtx& c, float& mx0, float& mx1) {
  constexpr bool PREV = MODE != M_FIRST, NEW = MODE == M_FIRST || MODE == M_SWITCH;
  constexpr int ks = I >> 2, db = I & 3;
  if constexpr (PREV) {
    a.o[db] = MFMA(a.fr[I & 3], pfrag(a, ks), a.o[db]);
    if constexpr (I + 3 < 16) a.fr[(I + 3) & 3] = lds_rd16(c.vp + (I + 3) * 1024);
  }
  if constexpr (I + 3 >= 16) a.fr[(I + 3) & 3] = lds_rd16(c.kpn + (I + 3 - 16) * 1024);
  if constexpr (MODE == M_STEADY) {
    if constexpr (I < 8) {
      a.s0[8 + I] = EXP2(a.s0[8 + I]);
      a.ps += a.s0[7 + I];
      if constexpr (I >= 4) { constexpr int k = I - 4; a.pw[k] = cvtpk(a.s0[2 * k], a.s0[2 * k + 1]); PIN(a.pw[k]); }
    } else {
      constexpr int i = I - 8;
      a.s1[i] = EXP2(a.s1[i]);
      if constexpr (i == 0) a.ps += a.s0[15]; else a.ps += a.s1[i - 1];
      if constexpr (I < 12) { constexpr int k = I - 4; a.pw[k] = cvtpk(a.s0[2 * k], a.s0[2 * k + 1]); PIN(a.pw[k]); }
    }
  } else if constexpr (NEW) {
    if constexpr (I < 4) {
      constexpr int r = 4 * I;
      if constexpr (I == 0) { mx0 = fmaxf(fmaxf(a.s0[0], a.s0[1]), a.s0[2]); mx1 = fmaxf(fmaxf(a.s1[0], a.s1[1]), a.s1[2]); mx0 = fmaxf(mx0, a.s0[3]); mx1 = fmaxf(mx1, a.s1[3]); }
      else { mx0 = fmaxf(fmaxf(mx0, a.s0[r]), a.s0[r + 1]); mx1 = fmaxf(fmaxf(mx1, a.s1[r]), a.s1[r + 1]); mx0 = fmaxf(fmaxf(mx0, a.s0[r + 2]), a.s0[r + 3]); mx1 = fmaxf(fmaxf(mx1, a.s1[r + 2]), a.s1[r + 3]); }
      PIN(mx0); PIN(mx1);
    } else {
      if constexpr (I == 4) { mx0 = fmaxf(hmax(fmaxf(mx0, mx1)), -1e30f); a.m = mx0; a.ps = 0.f; }
      if constexpr (I < 12) {
        constexpr int i = 2 * (I - 4);
        a.s0[i] = EXP2(a.s0[i] - mx0); a.s0[i + 1] = EXP2(a.s0[i + 1] - mx0);
        if constexpr (i >= 2) { a.ps += a.s0[i - 2]; a.ps += a.s0[i - 1]; }
      } else {
        constexpr int i = 2 * (I - 12);
        if constexpr (I == 12) { a.ps += a.s0[14]; a.ps += a.s0[15]; }
        a.s1[i] = EXP2(a.s1[i] - mx0); a.s1[i + 1] = EXP2(a.s1[i + 1] - mx0);
        a.s1[8 + i] -= mx0; a.s1[9 + i] -= mx0;
        if constexpr (i >= 2) { a.ps += a.s1[i - 2]; a.ps += a.s1[i - 1]; }
      }
      if constexpr (I >= 8) { constexpr int k = I - 8; if constexpr (2 * k + 1 < 2 * (I - 4)) { a.pw[k] = cvtpk(a.s0[2 * k], a.s0[2 * k + 1]); PIN(a.pw[k]); } }
    }
  }
  PIN(a.ps);
  SBAR();
}
template <int O, int MODE, int... Is> DI void phaseA_(AttB& a, const StepCtx& c, __amdgpu_buffer_rsrc_t sK, __amdgpu_buffer_rsrc_t sV, std::integer_sequence<int, Is...>) { (gapA<O + Is, MODE>(a, c, sK, sV), ...); }
template <int MODE, int... Is> DI void phaseB_(AttB& a, const StepCtx& c, float& mx0, float& mx1, std::integer_sequence<int, Is...>) { (gapB<Is, MODE>(a, c, mx0, mx1), ...); }

struct K2P { char* ws; };
struct AttEnv {
  int grp, wq, lane, hi, l32, n1, L, head; char* lds; unsigned lds0; char* ws;
  __amdgpu_buffer_rsrc_t sK, sV, sQ, sC;
};
DI int tile_of(const AttEnv& e, int x) { return e.grp == 0 ? x : (x < e.n1 ? x : 17 + (x - e.n1)); }
DI void step_top(const AttEnv& e, int s, StepCtx& c) {
  asm volatile("s_waitcnt vmcnt(0)\n\ts_barrier" ::: "memory");
  SBAR();
  const unsigned gb = e.grp * 81920;
  const int k3 = s % 3, k3n = (s + 1) % 3, k3d = (s + 2) % 3;
  c.kp = (lds_cptr)e.lds + gb + k3 * 16384 + e.lane * 16;
  c.kpn = (lds_cptr)e.lds + gb + k3n * 16384 + e.lane * 16;
  c.vp = (lds_cptr)e.lds + gb + 49152 + ((s + 1) & 1) * 16384 + e.lane * 16;
  c.dk = (s + 2 < 17); c.dv = (s < 17);
  c.m0k = rfl(e.lds0 + gb + k3d * 16384 + e.wq * 4096); c.m0v = rfl(e.lds0 + gb + 49152 + (s & 1) * 16384 + e.wq * 4096);
  c.sok = rfl((unsigned)(tile_of(e, s + 2) * 16384 + e.wq * 4096)); c.sov = rfl((unsigned)(tile_of(e, s) * 16384 + e.wq * 4096));
  c.voff = e.lane * 16;
}
DI void dma_all(const AttEnv& e, const StepCtx& c) {
  if (c.dk) {
#pragma unroll
    for (int i = 0; i < 4; ++i) dma16(c.m0k + i * 1024, c.voff, e.sK, c.sok + i * 1024); }
  if (c.dv) {
#pragma unroll
    for (int i = 0; i < 4; ++i) dma16(c.m0v + i * 1024, c.voff, e.sV, c.sov + i * 1024); }
}
template <bool HASCUR> DI void check_prev(AttB& a, const AttEnv& e, int qb, int prev_t) {
  if (__builtin_expect(__any(!(a.l + a.ps <= LTHR_V)), 0)) {
    const int rmin = qb * 128 + e.wq * 32;
    rare_fix<HASCUR>(a, e.sK, e.sQ, e.lane * 16, rfl((unsigned)(prev_t * 16384)), rfl((unsigned)(((qb == e.L ? 0 : 4) + e.wq) * 8192)),
                     prev_t * 64 + 4 * e.hi, rmin + e.l32, prev_t * 64 + 63 > rmin);
  }
  a.l += a.ps; a.ps = 0.f;
}
DI void store_block(AttB& a, const AttEnv& e, int qb) {
  const float inv = 1.f / hsum(a.l);
#pragma unroll
  for (int db = 0; db < 4; ++db)
#pragma unroll
    for (int g = 0; g < 2; ++g)
      bst16(pack8(a.o[db], g, inv), e.sC, e.lane * 16, rfl((unsigned)(((qb * 4 + e.wq) * 256 + db * 2 + g) * 1024)));
}
template <int MODE> DI void att_step(AttB& a, const AttEnv& e, int s, int qb_prev, int qb, bool loadq = false) {
  constexpr bool PREV = MODE != M_FIRST, CUR = MODE != M_DRAIN, NEW = MODE == M_FIRST || MODE == M_SWITCH;
  StepCtx c; step_top(e, s, c);
  if constexpr (NEW) asm volatile("" : "+v"(a.qf[0]), "+v"(a.qf[1]), "+v"(a.qf[2]), "+v"(a.qf[3]), "+v"(a.qf[4]), "+v"(a.qf[5]), "+v"(a.qf[6]), "+v"(a.qf[7]));
  const int t = tile_of(e, s), rmin = qb * 128 + e.wq * 32;
  const bool mask = t * 64 + 63 > rmin;
  if constexpr (MODE == M_FIRST) {
#pragma unroll
    for (int J = 0; J < 3; ++J) a.fr[J] = lds_rd16(c.kp + J * 1024);
  }
  phaseA_<0, MODE>(a, c, e.sK, e.sV, std::make_integer_sequence<int, 8>{});
  if constexpr (PREV) {
    a.ps += a.s1[15]; a.pw[15] = cvtpk(a.s1[14], a.s1[15]);
    check_prev<MODE == M_STEADY>(a, e, qb_prev, tile_of(e, s - 1));
  }
  if constexpr (MODE == M_STEADY) { if (mask) cmask1(a.s0, t * 64 + 4 * e.hi, rmin + e.l32); }
  SBAR();
  phaseA_<8, MODE>(a, c, e.sK, e.sV, std::make_integer_sequence<int, 8>{});
  if constexpr (CUR) {
    if (mask) { if constexpr (NEW) cmask1(a.s0, t * 64 + 4 * e.hi, rmin + e.l32); cmask1(a.s1, t * 64 + 32 + 4 * e.hi, rmin + e.l32); }
  }
  if constexpr (MODE == M_FIRST || MODE == M_STEADY) {
    if (loadq) {
#pragma unroll
      for (int ds = 0; ds < 8; ++ds)
        asm volatile("buffer_load_dwordx4 %0, %1, %2, %3 offen" : "=v"(a.qf[ds]) : "v"(e.lane * 16), "s"(e.sQ), "s"(rfl((unsigned)(e.wq * 8192 + ds * 1024))) : "memory");
    }
  }
  SBAR();
  float mx0 = 0.f, mx1 = 0.f; const float lold = a.l;
  phaseB_<MODE>(a, c, mx0, mx1, std::make_integer_sequence<int, 16>{});
  if constexpr (MODE == M_FIRST || MODE == M_STEADY) {
    if (loadq) asm volatile("s_waitcnt vmcnt(0)" : "+v"(a.qf[0]), "+v"(a.qf[1]), "+v"(a.qf[2]), "+v"(a.qf[3]), "+v"(a.qf[4]), "+v"(a.qf[5]), "+v"(a.qf[6]), "+v"(a.qf[7]) :: "memory");
  }
  if constexpr (NEW) {
    a.ps += a.s1[6];
    if constexpr (MODE == M_SWITCH) { a.l = lold; store_block(a, e, qb_prev); }
#pragma unroll
    for (int db = 0; db < 4; ++db) a.o[db] = f32x16{};
    a.l = 0.f; a.negm = splat16(-a.m); PIN(a.negm);
  }
}

struct AttA { f32x16 o; bf16x8 qf0, qf1; };
constexpr int AH_L = 65536;
#define E4(sc, i) do { sc[i] = EXP2(sc[i]); sc[i + 1] = EXP2(sc[i + 1]); sc[i + 2] = EXP2(sc[i + 2]); sc[i + 3] = EXP2(sc[i + 3]); } while (0)
#define C2(pw, j, sc, i) do { pw[j] = cvtpk(sc[i], sc[i + 1]); pw[j + 1] = cvtpk(sc[i + 2], sc[i + 3]); } while (0)
struct KVSt { __amdgpu_buffer_rsrc_t srd; unsigned voff; bool isv; };
template <bool ST> DI void a_half(AttA& A, char* lds, int lane, const bf16x8 (&kvp)[16], const KVSt& ks) {
  const int l32 = lane & 31, hi = lane >> 5;
  const int vrow = l32 < 19 ? l32 : (((l32 >= 20 && l32 <= 27)) ? 0 : 4);
  lds_cptr k0p = (lds_cptr)lds + AH_L + lane * 16;
  lds_cptr k1p = (lds_cptr)lds + AH_L + 32768 + l32 * 16;
  lds_cptr vp = (lds_cptr)lds + AH_L + 49152 + (hi * 19 + vrow) * 16;
  f32x16 sA, sB; u32x4 pa0, pa1, pb0, pb1;
  bf16x8 k0, k1, v0, v1, w0, w1;
#define LDK(kt) do { k0 = lds_rd16(k0p + (kt) * 1024); k1 = lds_rd16(k1p + (kt) * 512); } while (0)
#define ASTEP(SC, SN, PP0, PP1, PN0, PN1, kt, VC0, VC1, VN0, VN1, DOQK, DOPV, DOLDK) do { \
    if (DOQK) { SN = MFMA(k0, A.qf0, f32x16{}); } SBAR(); VN0 = lds_rd16(vp + (2 * (kt)) * 608); VN1 = lds_rd16(vp + (2 * (kt) + 1) * 608); E4(SC, 0); PIN(SC); SBAR(); \
    if (DOPV) { A.o = MFMA(VC0, PKB(PP0), A.o); } SBAR(); E4(SC, 4); C2(PN0, 0, SC, 0); PIN(SC); PIN(PN0); SBAR(); \
    if (DOQK) { SN = MFMA(k1, A.qf1, SN); } SBAR(); if (DOLDK) LDK((kt) + 2); E4(SC, 8); C2(PN0, 2, SC, 4); PIN(SC); PIN(PN0); SBAR(); \
    if (DOPV) { A.o = MFMA(VC1, PKB(PP1), A.o); } SBAR(); E4(SC, 12); C2(PN1, 0, SC, 8); C2(PN1, 2, SC, 12); PIN(SC); PIN(PN1); SBAR(); } while (0)
  pa0 = u32x4{0,0,0,0}; pa1 = pa0; pb0 = pa0; pb1 = pa0; v0 = bf16x8{}; v1 = v0; w0 = v0; w1 = v0;
  LDK(0); sA = MFMA(k0, A.qf0, f32x16{}); sA = MFMA(k1, A.qf1, sA); LDK(1);
  ASTEP(sA, sB, pb0, pb1, pa0, pa1, 0, v0, v1, w0, w1, true, false, true);
#define KVST(i) do { if (ST) { st16_sc1(kvp[i], ks.voff, ks.srd, ks.isv ? (unsigned)relV(i) : (unsigned)relK(i)); SBAR(); } } while (0)
  if constexpr (ST) {
    KVST(0);
#define APAIR(kt, i) ASTEP(sB, sA, pa0, pa1, pb0, pb1, kt, w0, w1, v0, v1, true, true, true); KVST(i); ASTEP(sA, sB, pb0, pb1, pa0, pa1, (kt) + 1, v0, v1, w0, w1, true, true, true)
    APAIR(1, 1); APAIR(3, 2); APAIR(5, 3); APAIR(7, 4); APAIR(9, 5); APAIR(11, 6); APAIR(13, 7); APAIR(15, 8); APAIR(17, 9); APAIR(19, 10); APAIR(21, 11); APAIR(23, 12); APAIR(25, 13); APAIR(27, 14);
    KVST(15);
#undef APAIR
  } else {
    for (int kt = 1; kt < 29; kt += 2) {
      ASTEP(sB, sA, pa0, pa1, pb0, pb1, kt, w0, w1, v0, v1, true, true, true);
      ASTEP(sA, sB, pb0, pb1, pa0, pa1, kt + 1, v0, v1, w0, w1, true, true, true);
    }
  }
  ASTEP(sB, sA, pa0, pa1, pb0, pb1, 29, w0, w1, v0, v1, true, true, true);
  ASTEP(sA, sB, pb0, pb1, pa0, pa1, 30, v0, v1, w0, w1, true, true, false);
  ASTEP(sB, sA, pa0, pa1, pb0, pb1, 31, w0, w1, v0, v1, false, true, false);
  A.o = MFMA(v0, PKB(pb0), A.o); A.o = MFMA(v1, PKB(pb1), A.o);
#undef LDK
#undef ASTEP
#undef KVST
}
DI void a_dma(unsigned lds0, int w, unsigned voff, char* ws, int hf) {
  const __amdgpu_buffer_rsrc_t sr = mksrd(ws + WS_KA + (size_t)hf * AHB, AHB);
#pragma unroll
  for (int i = 0; i < 11; ++i) { const int pc = w + 8 * i; dma16(rfl(lds0 + AH_L + (pc < 86 ? pc : 85) * 1024), voff, sr, rfl((unsigned)((pc < 86 ? pc : 85) * 1024))); }
}
DI void a_finish(AttA& A, char* ws, int lane, int qrow, int ah) {
  const int l32 = lane & 31, hi = lane >> 5;
  f32x16 o = A.o;
  float l = o[10];
  { auto rr = __builtin_amdgcn_permlane32_swap(__float_as_uint(l), __float_as_uint(l), false, false); l = __uint_as_float(rr[0]); }
  if (__builtin_expect(__any(!(l >= 8.6736174e-19f)), 0)) {
    float m = -1e30f; o = f32x16{};
    bf16x8 qz = A.qf1; qz[2] = 0;
    const int vrow = l32 < 19 ? l32 : 0;
    for (int kt = 0; kt < 64; ++kt) {
      const char* hb = ws + WS_KA + (size_t)(kt >> 5) * AHB; const int kl = kt & 31;
      const bf16x8 k0 = *(const bf16x8*)(hb + kl * 1024 + lane * 16), k1 = *(const bf16x8*)(hb + 32768 + (kl * 32 + l32) * 16);
      const bf16x8 v0 = *(const bf16x8*)(hb + 49152 + ((2 * kl) * 38 + hi * 19 + vrow) * 16), v1 = *(const bf16x8*)(hb + 49152 + ((2 * kl + 1) * 38 + hi * 19 + vrow) * 16);
      f32x16 sc = MFMA(k0, A.qf0, f32x16{});
      sc = MFMA(k1, qz, sc);
      const float mn = fmaxf(m, hmax(max16(sc))), f = EXP2(m - mn);
      m = mn;
#pragma unroll
      for (int r = 0; r < 16; ++r) { sc[r] = EXP2(sc[r] - mn); o[r] *= f; }
      o = MFMA(v0, pack8n(sc, 0), o);
      o = MFMA(v1, pack8n(sc, 1), o);
    }
    l = o[10];
    auto rr = __builtin_amdgcn_permlane32_swap(__float_as_uint(l), __float_as_uint(l), false, false); l = __uint_as_float(rr[0]);
  }
  const float inv = 1.f / l;
  char* ob = ws + WS_OA + (size_t)(qrow >> 5) * 36 * 1024 + l32 * 16;
  auto st2 = [&](int d, float x, float y) { const int c = ah * 18 + d; *(unsigned*)(ob + (c >> 4) * 1024 + ((c >> 2) & 1) * 512 + (((c >> 3) & 1) * 4 + (c & 3)) * 2) = cvtpk(x * inv, y * inv); };
  st2(4 * hi, o[0], o[1]); st2(4 * hi + 2, o[2], o[3]); st2(8 + 4 * hi, o[4], o[5]); st2(10 + 4 * hi, o[6], o[7]);
  if (hi == 0) st2(16, o[8], o[9]);
}

__global__ __launch_bounds__(512, 2) void kb_main(K1P p) {
  extern __shared__ __attribute__((aligned(16))) char lds[];
  const int tid = threadIdx.x, lane = tid & 63, l32 = lane & 31, hi = lane >> 5;
  const int b = blockIdx.x, xcd = b & 7, ii = b >> 3, head = xcd * 4 + (ii & 3), j = ii >> 2;
  K1C c;
  c.lds0 = (unsigned)(size_t)(lds_ptr)lds; c.w = rfl(tid >> 6);
  const int w = c.w;
  {
    const int r = lane >> 3, chp = lane & 7, row = 8 * w + r, ch = chp ^ ((4 * (w & 1) + (r >> 1)) & 7);
    c.xoffC = (unsigned)(row * 1024 + ch * 16); c.xoffX = (unsigned)(row * 1152 + ch * 16);
    const int sub = lane >> 5, rr = (lane & 31) >> 2, c4 = lane & 3;
    c.woff = (unsigned)((8 * (w >> 1) + rr) * 8192 + (32 * (2 * (w & 1) + sub) + 8 * c4) * 2);
  }
  c.sC = mksrd((const char*)p.cx + (size_t)j * 256 * 1024, 256 * 1024);
  c.sX = mksrd((const char*)p.bx, 2048 * 1152);
  c.rowL = (unsigned)((15 - j) * 128 * 1152); c.rowS = (unsigned)(j * 128 * 1152);
  c.sUK = mksrd((const char*)p.wuk + head * 256, 512 * 8192 - head * 256);
  c.sUV = mksrd((const char*)p.wuv + head * 256, 512 * 8192 - head * 256);
  c.sWQ = mksrd((const char*)p.wq + head * 256, 576 * 8192 - head * 256);
  const int blk = (lane >> 4) & 1, q4 = (lane & 15) >> 2, p4 = lane & 3;
  const unsigned wlane = 2048 * hi + 64 * q4 + 32 * blk + 8 * p4;
  {
    const int wr = w >> 2, wc = w & 3;
#pragma unroll
    for (int s = 0; s < 4; ++s) c.xb[s] = (lds_cptr)lds + (wr * 4) * 4096 + 128 * l32 + 16 * ((2 * s + hi) ^ ((l32 >> 1) & 7));
    c.wb = (lds_cptr)lds + WRING + (wc >> 1) * 8192 + (2 * (wc & 1)) * 512 + wlane;
  }
  STAMP(p.ws, 0, 0);
  const unsigned lds0 = c.lds0, voff = lane * 16;
  AttA A;
  const int aqrow = j * 256 + w * 32 + l32;
  unsigned wv[7] = {0, 0, 0, 0, 0, 0, 0};
  {
    a_dma(lds0, w, voff, p.ws, 0);
    k1_issueX<0, 0>(c); k1_issueX<0, 1>(c);
    const char* qp = p.ws + WS_QA + (size_t)aqrow * 1152 + head * 36;
    u32x4 q0; { const unsigned* qq = (const unsigned*)(qp + 16 * hi); q0 = u32x4{qq[0], qq[1], qq[2], qq[3]}; }
    const unsigned q16 = *(const unsigned*)(qp + 32);
    float km = *(const float*)(p.ws + WS_KMAX + lane * 4);
#pragma unroll
    for (int o = 32; o >= 1; o >>= 1) km = fmaxf(km, __shfl_xor(km, o));
    float qs = 0.f;
#pragma unroll
    for (int e = 0; e < 4; ++e) { const float x = __uint_as_float(q0[e] << 16), y = __uint_as_float(q0[e] & 0xFFFF0000u); qs += x * x + y * y; }
    if (hi == 0) { const float x = __uint_as_float(q16 << 16), y = __uint_as_float(q16 & 0xFFFF0000u); qs += x * x + y * y; }
    qs = hsum(qs);
    const float bound = sqrtf(qs * km) * 1.0009765625f + 1e-30f;
    const unsigned bb = (__float_as_uint(bound) + 0xFFFFu) >> 16;
    A.qf0 = __builtin_bit_cast(bf16x8, q0);
    A.qf1 = __builtin_bit_cast(bf16x8, hi == 0 ? u32x4{q16, bb | 0x8000u, 0u, 0u} : u32x4{0u, 0u, 0u, 0u});
    A.o = f32x16{};
    asm volatile("s_waitcnt vmcnt(0)\n\ts_barrier" ::: "memory");
    STAMP(p.ws, 0, 6);
    if (WARM) {
      const char* src[7] = {(const char*)p.cx, (const char*)p.wuk, (const char*)p.wuv, (const char*)p.bx, (const char*)p.wq, (const char*)p.wo, (const char*)p.awo};
      const unsigned nline[7] = {2048u * 1024 / 128, 512u * 8192 / 128, 512u * 8192 / 128, 2048u * 1152 / 128, 576u * 8192 / 128, 4096u * 1152 / 128, 576u * 1152 / 128};
#pragma unroll
      for (int a7 = 0; a7 < 7; ++a7) {
        const unsigned i = (unsigned)(b * 192 + tid);
        if (tid < 192 && i < nline[a7]) wv[a7] = *(const unsigned*)(src[a7] + (size_t)i * 128);
      }
    }
    { bf16x8 dummy[16]; KVSt ks0; a_half<false>(A, lds, lane, dummy, ks0); }
    if ((wv[0] ^ wv[1] ^ wv[2] ^ wv[3] ^ wv[4] ^ wv[5] ^ wv[6]) == 0x9E3779B9u) *(unsigned*)(p.ws + WS_FLAG + 256 * 128 + 64) = 1u;
    STAMP(p.ws, 0, 7);
  }
  asm volatile("s_waitcnt lgkmcnt(0)\n\ts_barrier" ::: "memory");
  k1_issueW<0, 0>(c); k1_issueW<0, 1>(c); k1_issueW<0, 2>(c);
  {
    bf16x8 kvp[16];
    if ((w & 2) == 0) k1_kv<false>(c, kvp); else k1_kv<true>(c, kvp);
    STAMP(p.ws, 0, 1);
    asm volatile("s_waitcnt lgkmcnt(0)\n\ts_barrier" ::: "memory");
    a_dma(lds0, w, voff, p.ws, 1);
    k1_issueX<1, 0>(c); k1_issueX<1, 1>(c);
    const int wr = w >> 2, wc1 = w & 1;
    KVSt ks; ks.isv = (w & 2) != 0; ks.voff = voff;
    const size_t base = ks.isv ? WS_VF + ((size_t)(head * 32 + 4 * j + 2 * wr) * 16 + 2 * wc1) * 1024 : WS_KF + ((size_t)(head * 64 + 8 * j + 4 * wr) * 8 + 4 * wc1) * 1024;
    ks.srd = mksrd(p.ws + base, 65536);
    asm volatile("s_waitcnt vmcnt(8)\n\ts_barrier" ::: "memory");
    STAMP(p.ws, 0, 8);
    a_half<true>(A, lds, lane, kvp, ks);
  }
  a_finish(A, p.ws, lane, aqrow, head);
  STAMP(p.ws, 0, 5);
  asm volatile("s_waitcnt vmcnt(0) lgkmcnt(0)\n\ts_barrier" ::: "memory");
  if (tid == 0) __hip_atomic_store((__attribute__((address_space(1))) unsigned*)(p.ws + WS_FLAG + (head * 8 + j) * 128), 1u, __ATOMIC_RELAXED, __HIP_MEMORY_SCOPE_AGENT);
  k1_issueW<1, 0>(c); k1_issueW<1, 1>(c); k1_issueW<1, 2>(c);
  AttB a;
  const int grp = w >> 2, wq = w & 3, pp = j;
  {
    const int wr4 = w >> 1, wc2 = w & 1;
#pragma unroll
    for (int s = 0; s < 4; ++s) c.xb[s] = (lds_cptr)lds + (wr4 * 2) * 4096 + 128 * l32 + 16 * ((2 * s + hi) ^ ((l32 >> 1) & 7));
    c.wb = (lds_cptr)lds + WRING + (2 * wc2) * 512 + wlane;
    f32x16 acc[2][2];
    acc[0][0] = f32x16{}; acc[0][1] = f32x16{}; acc[1][0] = f32x16{}; acc[1][1] = f32x16{};
    FragQ fa, fb;
    QLoop<0>::run(c, acc, fa, fb);
    STAMP(p.ws, 0, 2);
    asm volatile("s_waitcnt lgkmcnt(0)\n\ts_barrier" ::: "memory");
    const __amdgpu_buffer_rsrc_t sQLw = mksrd(p.ws + WS_QF + (size_t)b * 65536, 65536);
#pragma unroll
    for (int r = 0; r < 2; ++r)
#pragma unroll
      for (int ct = 0; ct < 2; ++ct)
#pragma unroll
        for (int g = 0; g < 2; ++g) {
          const int fi = (2 * wr4 + r) * 8 + 2 * (2 * wc2 + ct) + g;
          const bf16x8 v = pack8(acc[r][ct], g, CB);
          *(bf16x8*)(lds + fi * 1024 + lane * 16) = v;
          bst16(v, sQLw, voff, (unsigned)(fi * 1024));
        }
    asm volatile("s_waitcnt vmcnt(0) lgkmcnt(0)\n\ts_barrier" ::: "memory");
#pragma unroll
    for (int ds = 0; ds < 8; ++ds) a.qf[ds] = lds_rd16((lds_cptr)lds + ((grp * 4 + wq) * 8 + ds) * 1024 + lane * 16);
    STAMP(p.ws, 0, 3);
    if (w == 0) {
      const __attribute__((address_space(1))) unsigned* fl = (const __attribute__((address_space(1))) unsigned*)(p.ws + WS_FLAG + (head * 8 + (lane & 7)) * 128);
      for (unsigned spin = 0; spin < (1u << 22); ++spin) {
        const unsigned v = __hip_atomic_load(fl, __ATOMIC_RELAXED, __HIP_MEMORY_SCOPE_AGENT);
        if (__all(v == 1u)) break;
        __builtin_amdgcn_s_sleep(2);
      }
      __builtin_amdgcn_fence(__ATOMIC_ACQUIRE, "agent");
      asm volatile("s_waitcnt vmcnt(0)" ::: "memory");
    }
    asm volatile("s_waitcnt lgkmcnt(0)\n\ts_barrier" ::: "memory");
    STAMP(p.ws, 0, 4);
  }
  const int L = 15 - pp, n1 = 2 * pp + 2;
  STAMP(p.ws, 0, 9);
  {
  AttEnv e; e.grp = grp; e.wq = wq; e.lane = lane; e.hi = hi; e.l32 = l32; e.n1 = n1; e.L = L; e.head = head; e.lds = lds; e.lds0 = lds0; e.ws = p.ws;
  e.sK = mksrd(p.ws + WS_KF + (size_t)head * S * 256, S * 256);
  e.sV = mksrd(p.ws + WS_VF + (size_t)head * S * 256, S * 256);
  e.sQ = mksrd(p.ws + WS_QF + (size_t)b * 65536, 65536);
  e.sC = mksrd(p.ws + WS_CF + (size_t)head * 8192, 64 * 256 * 1024 - head * 8192);
  int qb = grp == 0 ? L : pp;
  {
    const unsigned gb = grp * 81920;
#pragma unroll
    for (int t = 0; t < 2; ++t)
#pragma unroll
      for (int i = 0; i < 4; ++i) dma16(rfl(lds0 + gb + t * 16384 + wq * 4096 + i * 1024), voff, e.sK, rfl((unsigned)(t * 16384 + wq * 4096 + i * 1024)));
  }
  {
    const int send0 = grp == 0 ? 17 : n1;
    const int rmin0 = qb * 128 + wq * 32;
    int nt0 = ((rmin0 + 31) >> 6) + 1; if (nt0 > send0) nt0 = send0;
    int nt1 = 0;
    if (grp == 1) { const int rminL = L * 128 + wq * 32; nt1 = ((rminL + 31) >> 6) - 17 + 1; if (nt1 > 17 - n1) nt1 = 17 - n1; if (nt1 < 0) nt1 = 0; }
    a.l = 0.f; a.ps = 0.f; a.m = -1e30f;
#pragma unroll
    for (int db = 0; db < 4; ++db) a.o[db] = f32x16{};
    int s = 0; bool carry = false; int qprev = qb;
    const int nseg = grp == 0 ? 1 : 2;
#pragma unroll 1
    for (int seg = 0; seg < nseg; ++seg) {
      const int nt = seg == 0 ? nt0 : nt1;
      const int send = seg == 0 ? send0 : 17;
      const bool shortblk = grp == 1 && seg == 0;
      if (nt > 0) {
        const int slast = s + nt;
        if (carry) att_step<M_SWITCH>(a, e, s, qprev, qb); else att_step<M_FIRST>(a, e, s, qb, qb, shortblk && nt == 1);
        for (++s; s < slast; ++s) att_step<M_STEADY>(a, e, s, qb, qb, shortblk && s == slast - 1);
        carry = shortblk && s == send && nt1 > 0;
        if (!carry) {
          att_step<M_DRAIN>(a, e, s, qb, qb); ++s;
          if (shortblk) {
            store_block(a, e, qb);
#pragma unroll
            for (int db = 0; db < 4; ++db) a.o[db] = f32x16{};
            a.l = 0.f; a.ps = 0.f; a.m = -1e30f;
          }
        }
      }
      for (; s < send; ++s) { StepCtx sc; step_top(e, s, sc); dma_all(e, sc); }
      qprev = qb; qb = L;
    }
    for (; s < 18; ++s) { StepCtx sc; step_top(e, s, sc); dma_all(e, sc); }
  }
  STAMP(p.ws, 0, 10);
  asm volatile("s_waitcnt vmcnt(0) lgkmcnt(0)\n\ts_barrier" ::: "memory");
  {
    const float lt = hsum(a.l);
    char* xb = lds + w * 8192;
    float* st = (float*)(lds + 65536) + w * 128;
#pragma unroll
    for (int i = 0; i < 2; ++i)
#pragma unroll
      for (int q = 0; q < 4; ++q) {
        const f32x16& o = grp == 0 ? a.o[2 + i] : a.o[i];
        *(f32x4*)(xb + (i * 4 + q) * 1024 + lane * 16) = f32x4{o[4 * q], o[4 * q + 1], o[4 * q + 2], o[4 * q + 3]};
      }
    st[lane] = a.m; st[64 + lane] = lt;
    __syncthreads();
    const int ow = w ^ 4;
    const char* yb = lds + ow * 8192; const float* so = (const float*)(lds + 65536) + ow * 128;
    const float m2 = so[lane], l2 = so[64 + lane];
    const float mn = fmaxf(a.m, m2), f1 = EXP2(a.m - mn), f2 = EXP2(m2 - mn);
    const float inv = 1.f / (lt * f1 + l2 * f2);
    const float g1 = f1 * inv, g2 = f2 * inv;
#pragma unroll
    for (int i = 0; i < 2; ++i) {
      f32x16 o = grp == 0 ? a.o[i] : a.o[2 + i];
#pragma unroll
      for (int q = 0; q < 4; ++q) { const f32x4 y = *(const f32x4*)(yb + (i * 4 + q) * 1024 + lane * 16);
#pragma unroll
        for (int e = 0; e < 4; ++e) o[4 * q + e] = o[4 * q + e] * g1 + y[e] * g2; }
      const int db = (grp == 0 ? 0 : 2) + i;
#pragma unroll
      for (int g = 0; g < 2; ++g)
        bst16(pack8n(o, g), e.sC, voff, rfl((unsigned)(((L * 4 + wq) * 256 + db * 2 + g) * 1024)));
    }
  }
  }
  STAMP(p.ws, 0, 11);
}

struct K3P { char* ws; const void* wo; const void* awo; void* out; };
constexpr int K3_WS = 24576;
struct K3C {
  unsigned lds0, w, xvoff, wvoff;
  __amdgpu_buffer_rsrc_t sX, sW;
  lds_cptr wb;
};
DI void xload(bf16x8& d, unsigned voff, __amdgpu_buffer_rsrc_t srd, unsigned soff) {
  asm volatile("buffer_load_dwordx4 %0, %1, %2, %3 offen" : "=v"(d) : "v"(voff), "s"(srd), "s"(soff) : "memory");
}
template <int I> DI void k3_dmaW(const K3C& c, int st) {
  constexpr int gg = I / 3, cp = I % 3;
  dma16(c.lds0 + (st & 3) * K3_WS + ((2 * c.w + gg) * 3 + cp) * 1024, c.wvoff + cp * 128, c.sW, (unsigned)((st * 64 + (2 * c.w + gg) * 8) * 1152));
}
struct K3F { s16x4 wl[6], wh[6]; };
template <int KS, int D0, int NDMA> DI void k3_kstep(const K3C& c, f32x16 (&acc)[6], bf16x8& x, const K3F& cur, K3F& nxt, int st, int slot_next_off, unsigned xso) {
  constexpr int KN = (KS + 1) & 3;
#pragma unroll
  for (int m = 0; m < 6; ++m) {
    acc[m] = MFMA(cat(cur.wl[m], cur.wh[m]), x, acc[m]);
    lds_cptr p = c.wb + slot_next_off + (2 * KN) * 3072 + m * 512;
    nxt.wl[m] = lds_tr(p); nxt.wh[m] = lds_tr(p + 3072);
    if (m == 1) k3_dmaW<D0>(c, st + 3);
    if (m == 3 && NDMA >= 2) k3_dmaW<(D0 + 1) % 6>(c, st + 3);
    SBAR();
  }
  xload(x, c.xvoff, c.sX, xso + KS * 1024);
  SBAR();
}
__global__ __launch_bounds__(256, 1) void k3_out(K3P p) {
  extern __shared__ __attribute__((aligned(16))) char lds[];
  const int tid = threadIdx.x, lane = tid & 63, l32 = lane & 31, hi = lane >> 5;
  const int b = blockIdx.x;
  const bool isB = b < 192;
  int rb, cb, sk;
  if (isB) { const int t = b >> 2; sk = b & 3; rb = t / 3; cb = t - rb * 3; } else { const int t = b - 192; sk = 0; rb = t / 3; cb = t - rb * 3; }
  K3C c;
  c.lds0 = (unsigned)(size_t)(lds_ptr)lds; c.w = rfl(tid >> 6);
  const int blk = (lane >> 4) & 1, q4 = (lane & 15) >> 2, p4 = lane & 3;
  const int nst = isB ? 16 : 9;
  c.sW = isB ? mksrd((const char*)p.wo + (size_t)sk * 1024 * 1152 + cb * 384, 1024 * 1152 - cb * 384) : mksrd((const char*)p.awo + cb * 384, 576 * 1152 - cb * 384);
  {
    const char* xb = isB ? p.ws + WS_CF + ((size_t)(rb * 4 + c.w) * 256 + sk * 64) * 1024 : p.ws + WS_OA + (size_t)(rb * 4 + c.w) * 36 * 1024;
    c.sX = mksrd(xb, nst * 4096);
  }
  c.xvoff = lane * 16;
  { const int sub = lane >> 5, rr = (lane & 31) >> 2, c4 = lane & 3; c.wvoff = (unsigned)(rr * 1152 + (32 * sub + 8 * c4) * 2); }
  c.wb = (lds_cptr)lds + 32 * blk + 8 * p4 + (4 * hi + q4) * 64;
  f32x16 acc[6];
#pragma unroll
  for (int ct = 0; ct < 6; ++ct) acc[ct] = f32x16{};
  bf16x8 xa[4], xb_[4];
  STAMP(p.ws, 2, 0);
  k3_dmaW<0>(c, 0); k3_dmaW<1>(c, 0); k3_dmaW<2>(c, 0); k3_dmaW<3>(c, 0); k3_dmaW<4>(c, 0); k3_dmaW<5>(c, 0);
#pragma unroll
  for (int ks = 0; ks < 4; ++ks) xload(xa[ks], c.xvoff, c.sX, ks * 1024);
  k3_dmaW<0>(c, 1); k3_dmaW<1>(c, 1); k3_dmaW<2>(c, 1); k3_dmaW<3>(c, 1); k3_dmaW<4>(c, 1); k3_dmaW<5>(c, 1);
#pragma unroll
  for (int ks = 0; ks < 4; ++ks) xload(xb_[ks], c.xvoff, c.sX, 4096 + ks * 1024);
  k3_dmaW<0>(c, 2); k3_dmaW<1>(c, 2); k3_dmaW<2>(c, 2); k3_dmaW<3>(c, 2); k3_dmaW<4>(c, 2); k3_dmaW<5>(c, 2);
  K3F fa, fb;
#define K3_WAIT(N, X) asm volatile("s_waitcnt vmcnt(" #N ")\n\ts_barrier" : "+v"(X[0]), "+v"(X[1]), "+v"(X[2]), "+v"(X[3]) :: "memory")
#define K3_STAGE(X, st) do { \
    const int so = ((st) & 3) * K3_WS, sn = (((st) + 1) & 3) * K3_WS; const unsigned xso = (unsigned)(((st) + 2) * 4096); \
    k3_kstep<0, 0, 2>(c, acc, X[0], fa, fb, st, so, xso); \
    k3_kstep<1, 2, 2>(c, acc, X[1], fb, fa, st, so, xso); \
    k3_kstep<2, 4, 1>(c, acc, X[2], fa, fb, st, so, xso); \
    k3_kstep<3, 5, 1>(c, acc, X[3], fb, fa, st, sn, xso); } while (0)
  K3_WAIT(10, xa); SBAR();
  STAMP(p.ws, 2, 1);
#pragma unroll
  for (int m = 0; m < 6; ++m) { fa.wl[m] = lds_tr(c.wb + m * 512); fa.wh[m] = lds_tr(c.wb + m * 512 + 3072); }
  SBAR();
  int st = 0;
  for (; st + 1 < nst; st += 2) {
    K3_STAGE(xa, st);
    K3_WAIT(10, xb_); SBAR();
    K3_STAGE(xb_, st + 1);
    K3_WAIT(10, xa); SBAR();
  }
  if (st < nst) K3_STAGE(xa, st);
  STAMP(p.ws, 2, 2);
  asm volatile("s_waitcnt vmcnt(0)" : "+v"(xa[0]), "+v"(xa[1]), "+v"(xa[2]), "+v"(xa[3]) :: "memory");
  asm volatile("" : "+v"(xb_[0]), "+v"(xb_[1]), "+v"(xb_[2]), "+v"(xb_[3]));
  if (isB) {
    const __amdgpu_buffer_rsrc_t sP = mksrd(p.ws + WS_PART + ((size_t)(sk * 48 + (rb * 3 + cb)) * 4 + c.w) * 24576, 24576);
#pragma unroll
    for (int ct = 0; ct < 6; ++ct)
#pragma unroll
      for (int g = 0; g < 4; ++g)
        __builtin_amdgcn_raw_buffer_store_b128(__builtin_bit_cast(u32x4, f32x4{acc[ct][4 * g], acc[ct][4 * g + 1], acc[ct][4 * g + 2], acc[ct][4 * g + 3]}), sP, lane * 16 + (ct * 4 + g) * 1024, 0, ST_AUX);
  } else {
    const int row = rb * 128 + c.w * 32 + l32;
#pragma unroll
    for (int ct = 0; ct < 6; ++ct)
#pragma unroll
      for (int g = 0; g < 4; ++g)
        *(u32x2*)((char*)p.out + (size_t)row * 2304 + (cb * 192 + ct * 32 + 8 * g + 4 * hi) * 2) = u32x2{cvtpk(acc[ct][4 * g], acc[ct][4 * g + 1]), cvtpk(acc[ct][4 * g + 2], acc[ct][4 * g + 3])};
  }
  asm volatile("s_waitcnt vmcnt(0)" ::: "memory");
  STAMP(p.ws, 2, 3);
}
__global__ __launch_bounds__(256) void k4_red(const char* part, char* out) {
  const int i = blockIdx.x * 256 + threadIdx.x;
  const int lane = i & 63, it = i >> 6;
  const int g = it & 3, ct = (it >> 2) % 6, tw = (it >> 2) / 6, w = tw & 3, tile = tw >> 2;
  const int rb = tile / 3, cb = tile - rb * 3;
  f32x4 s = *(const f32x4*)(part + (size_t)i * 16);
#pragma unroll
  for (int k = 1; k < 4; ++k) s += *(const f32x4*)(part + (size_t)k * 48 * 4 * 24576 + (size_t)i * 16);
  const int row = rb * 128 + w * 32 + (lane & 31), col = cb * 192 + ct * 32 + 8 * g + 4 * (lane >> 5);
  *(u32x2*)(out + (size_t)row * 2304 + 1152 + col * 2) = u32x2{cvtpk(s[0], s[1]), cvtpk(s[2], s[3])};
}

extern "C" void kernel_launch(void* const* d_in, const int* in_sizes, int n_in, void* d_out, int out_size, void* d_ws, size_t ws_size, hipStream_t stream) {
  static bool attr = false;
  if (!attr) {
    hipFuncSetAttribute((const void*)k0_aproj, hipFuncAttributeMaxDynamicSharedMemorySize, 147456);
    hipFuncSetAttribute((const void*)kb_main, hipFuncAttributeMaxDynamicSharedMemorySize, 163840);
    hipFuncSetAttribute((const void*)k3_out, hipFuncAttributeMaxDynamicSharedMemorySize, 4 * K3_WS);
    attr = true;
  }
  K1P p1; p1.cx = d_in[6]; p1.bx = d_in[5]; p1.wuk = d_in[8]; p1.wuv = d_in[9]; p1.wq = d_in[7];
  p1.ax = d_in[0]; p1.awq = d_in[1]; p1.awk = d_in[2]; p1.awv = d_in[3]; p1.wo = d_in[10]; p1.awo = d_in[4]; p1.ws = (char*)d_ws;
  k0_aproj<<<256, 512, 147456, stream>>>(p1);
  kb_main<<<256, 512, 163840, stream>>>(p1);
  K3P p3; p3.ws = (char*)d_ws; p3.wo = d_in[10]; p3.awo = d_in[4]; p3.out = d_out;
  k3_out<<<240, 256, 4 * K3_WS, stream>>>(p3);
  k4_red<<<1152, 256, 0, stream>>>((const char*)d_ws + WS_PART, (char*)d_out);
}
```

```cpp
#include <hip/hip_runtime.h>
#include <hip/hip_bf16.h>
#include <cstdio>
#include <cstdint>
#include <utility>

#define PUBMODE 0
#define KV_ACQ 0
#define WARM 1
#define ST_AUX 0
#define DI __device__ __forceinline__
typedef short bf16x8 __attribute__((ext_vector_type(8)));
typedef short s16x4 __attribute__((ext_vector_type(4)));
typedef float f32x16 __attribute__((ext_vector_type(16)));
typedef float f32x4 __attribute__((ext_vector_type(4)));
typedef float f32x2 __attribute__((ext_vector_type(2)));
typedef __bf16 bf16x2_t __attribute__((ext_vector_type(2)));
typedef unsigned u32x4 __attribute__((ext_vector_type(4)));
typedef unsigned u32x2 __attribute__((ext_vector_type(2)));
typedef __attribute__((address_space(3))) const char* lds_cptr;
typedef __attribute__((address_space(3))) char* lds_ptr;
#define SBAR() __builtin_amdgcn_sched_barrier(0)
#define PIN(x) asm volatile("" : "+v"(x))
#define MFMA(a, b, c) __builtin_amdgcn_mfma_f32_32x32x16_bf16(a, b, c, 0, 0, 0)
#define SRD_W3 0x00020000

DI unsigned rfl(unsigned x) { return __builtin_amdgcn_readfirstlane(x); }
DI unsigned cvtpk(float lo, float hi) { f32x2 v = {lo, hi}; bf16x2_t b = __builtin_convertvector(v, bf16x2_t); return __builtin_bit_cast(unsigned, b); }
DI void dma16(unsigned m0, unsigned voff, __amdgpu_buffer_rsrc_t srd, unsigned soff) {
  asm volatile("s_mov_b32 m0, %0\n\ts_nop 0\n\tbuffer_load_dwordx4 %1, %2, %3 offen lds" :: "s"(m0), "v"(voff), "s"(srd), "s"(soff) : "m0", "memory");
}
template <int OFF> DI void dma16o(unsigned m0, unsigned voff, __amdgpu_buffer_rsrc_t srd, unsigned soff) {
  asm volatile("s_mov_b32 m0, %0\n\ts_nop 0\n\tbuffer_load_dwordx4 %1, %2, %3 offen offset:%4 lds" :: "s"(m0), "v"(voff), "s"(srd), "s"(soff), "i"(OFF) : "m0", "memory");
}
DI void dma16s(unsigned m0, unsigned voff, __amdgpu_buffer_rsrc_t srd, unsigned soff) {
  asm volatile("s_mov_b32 m0, %0\n\ts_nop 0\n\tbuffer_load_dwordx4 %1, %2, %3 offen sc1 lds" :: "s"(m0), "v"(voff), "s"(srd), "s"(soff) : "m0", "memory");
}
template <int OFF> DI void dma16os(unsigned m0, unsigned voff, __amdgpu_buffer_rsrc_t srd, unsigned soff) {
  asm volatile("s_mov_b32 m0, %0\n\ts_nop 0\n\tbuffer_load_dwordx4 %1, %2, %3 offen offset:%4 sc1 lds" :: "s"(m0), "v"(voff), "s"(srd), "s"(soff), "i"(OFF) : "m0", "memory");
}
DI bf16x8 bld16s(__amdgpu_buffer_rsrc_t srd, unsigned voff, unsigned soff) { return __builtin_bit_cast(bf16x8, __builtin_amdgcn_raw_buffer_load_b128(srd, voff, soff, 16)); }
DI void dma4(unsigned m0, unsigned voff, __amdgpu_buffer_rsrc_t srd, unsigned soff) {
  asm volatile("s_mov_b32 m0, %0\n\ts_nop 0\n\tbuffer_load_dword %1, %2, %3 offen lds" :: "s"(m0), "v"(voff), "s"(srd), "s"(soff) : "m0", "memory");
}
DI __amdgpu_buffer_rsrc_t mksrd(const void* p, unsigned bytes) { return __builtin_amdgcn_make_buffer_rsrc((void*)p, (short)0, (int)bytes, SRD_W3); }
DI bf16x8 bld16(__amdgpu_buffer_rsrc_t srd, unsigned voff, unsigned soff) { return __builtin_bit_cast(bf16x8, __builtin_amdgcn_raw_buffer_load_b128(srd, voff, soff, 0)); }
DI void bst16(bf16x8 v, __amdgpu_buffer_rsrc_t srd, unsigned voff, unsigned soff) { __builtin_amdgcn_raw_buffer_store_b128(__builtin_bit_cast(u32x4, v), srd, voff + soff, 0, ST_AUX); }
template <int N> DI void wait_bar() { asm volatile("s_waitcnt vmcnt(%0)\n\ts_barrier" :: "i"(N) : "memory"); }
DI bf16x8 lds_rd16(lds_cptr p) { return *(const __attribute__((address_space(3))) bf16x8*)p; }
DI s16x4 lds_tr(lds_cptr p) { return __builtin_bit_cast(s16x4, __builtin_amdgcn_ds_read_tr16_b64_v4i16((__attribute__((address_space(3))) s16x4*)p)); }
DI bf16x8 cat(s16x4 a, s16x4 b) { return __builtin_shufflevector(a, b, 0, 1, 2, 3, 4, 5, 6, 7); }
DI bf16x8 pack8(const f32x16& x, int g, float sc) {
  u32x4 w;
#pragma unroll
  for (int e = 0; e < 4; ++e) w[e] = cvtpk(x[8 * g + 2 * e] * sc, x[8 * g + 2 * e + 1] * sc);
  return __builtin_bit_cast(bf16x8, w);
}
DI bf16x8 pack8n(const f32x16& x, int g) {
  u32x4 w;
#pragma unroll
  for (int e = 0; e < 4; ++e) w[e] = cvtpk(x[8 * g + 2 * e], x[8 * g + 2 * e + 1]);
  return __builtin_bit_cast(bf16x8, w);
}

#define DBG_S 1
#define STAMP(ws, k, i)
DI float hmax(float m) { auto rr = __builtin_amdgcn_permlane32_swap(__float_as_uint(m), __float_as_uint(m), false, false); return fmaxf(__uint_as_float(rr[0]), __uint_as_float(rr[1])); }
DI float hsum(float m) { auto rr = __builtin_amdgcn_permlane32_swap(__float_as_uint(m), __float_as_uint(m), false, false); return __uint_as_float(rr[0]) + __uint_as_float(rr[1]); }
constexpr int S = 2048, H = 32, DB = 128, MB = 576, DC = 512, DA = 576, DHA = 18;
constexpr float CB = 0.08838834764831845f * 1.4426950408889634f;
constexpr float CA = 0.23570226039551584f * 1.4426950408889634f;
constexpr size_t WS_KF = 0;
constexpr size_t WS_VF = WS_KF + (size_t)H * S * DB * 2;
constexpr size_t WS_QF = WS_VF + (size_t)H * S * DB * 2;
constexpr size_t WS_CF = WS_QF + (size_t)H * S * DB * 2;
constexpr size_t WS_QA = WS_CF + (size_t)H * S * DB * 2;
constexpr int AHB = 88064;
constexpr size_t WS_KA = WS_QA + (size_t)S * DA * 2 + 256;
constexpr size_t WS_KMAX = WS_KA + 2 * AHB;
constexpr size_t WS_OA = WS_KMAX + 1024;
constexpr size_t WS_PART = WS_OA + (size_t)S * DA * 2;
constexpr size_t WS_FLAG = WS_PART + (size_t)4 * S * 576 * 4;
constexpr size_t WS_END = WS_FLAG + 256 * 128;

struct K1P {
  const void *cx, *bx, *wuk, *wuv, *wq;
  const void *ax, *awq, *awk, *awv;
  const void *wo, *awo;
  char* ws;
};
constexpr int XSLOT = 32768, WRING = 98304, WSLOT = 16384;
template <int PART> constexpr int NSTEP = PART == 0 ? 16 : 18;
template <int PART> constexpr int NXT = PART == 0 ? 8 : 9;
template <int PART> constexpr int nWp(int s) { return s < NSTEP<PART> ? (PART == 0 ? 2 : 1) : 0; }
template <int PART> constexpr int nXp(int g) { return ((g >> 1) + 2) < NXT<PART> ? 2 : 0; }
template <int PART> constexpr int nD(int g) { return nWp<PART>(g + 3) + nXp<PART>(g); }
template <int PART> constexpr int waitN(int g) { return g == 0 ? nWp<PART>(2) : nD<PART>(g - 1); }

struct K1C {
  unsigned lds0, w;
  unsigned xoffC, xoffX;
  unsigned woff;
  __amdgpu_buffer_rsrc_t sC, sX, sUK, sUV, sWQ;
  lds_cptr xb[4];
  lds_cptr wb;
  unsigned rowL, rowS;
  const char* flagp; unsigned flagv;
};
DI void st16_sc1(bf16x8 v, unsigned voff, __amdgpu_buffer_rsrc_t srd, unsigned soff) {
  asm volatile("buffer_store_dwordx4 %0, %1, %2, %3 offen sc1\n\ts_nop 1" :: "v"(v), "v"(voff), "s"(srd), "s"(soff) : "memory");
}
template <int PART, int T, int i> DI void k1_x1(const K1C& c) {
  const unsigned m0 = c.lds0 + (T % 3) * XSLOT + i * 8192 + c.w * 1024;
  if constexpr (PART == 0) dma16(m0, c.xoffC, c.sC, (unsigned)(i * 64 * 1024 + T * 128));
  else                     dma16(m0, c.xoffX, c.sX, (i < 2 ? c.rowL : c.rowS) + (unsigned)((i & 1) * 64 * 1152 + T * 128));
}
template <int PART, int T> DI void k1_issueX(const K1C& c) { k1_x1<PART, T, 0>(c); k1_x1<PART, T, 1>(c); k1_x1<PART, T, 2>(c); k1_x1<PART, T, 3>(c); }
template <int PART, int G, int I> DI void k1_w1(const K1C& c) {
  const unsigned m0 = c.lds0 + WRING + (G & 3) * WSLOT + c.w * 1024;
  if constexpr (PART == 0) { if constexpr (I == 0) dma16(m0, c.woff, c.sUK, (unsigned)(G * 32 * 8192)); else dma16(m0 + 8192, c.woff, c.sUV, (unsigned)(G * 32 * 8192)); }
  else if constexpr (I == 0) dma16(m0, c.woff, c.sWQ, (unsigned)(G * 32 * 8192));
}
template <int PART, int G> DI void k1_issueW(const K1C& c) { k1_w1<PART, G, 0>(c); k1_w1<PART, G, 1>(c); }
struct FragKV { bf16x8 x[4]; s16x4 wl[2], wh[2]; };
struct FragQ { bf16x8 x[2]; s16x4 wl[2], wh[2]; };
template <int G, int SS, int I> DI void kv_ld(const K1C& c, FragKV& f) {
  constexpr int T = G >> 1;
  lds_cptr xp = c.xb[2 * (G & 1) + SS] + (T % 3) * XSLOT;
  lds_cptr wp = c.wb + (G & 3) * WSLOT + SS * 4096;
  if constexpr (I == 0) f.x[0] = lds_rd16(xp);
  else if constexpr (I == 1) f.wl[0] = lds_tr(wp);
  else if constexpr (I == 2) f.wh[0] = lds_tr(wp + 256);
  else if constexpr (I == 3) f.wl[1] = lds_tr(wp + 512);
  else if constexpr (I == 4) f.wh[1] = lds_tr(wp + 768);
  else f.x[I - 4] = lds_rd16(xp + (I - 4) * 4096);
}
template <int PART, int G, int I> DI void k1_dma_piece(const K1C& c) {
  constexpr int GW = G + 3, TX = (G >> 1) + 2;
  if constexpr (I < 2) { if constexpr (GW < NSTEP<PART>) k1_w1<PART, GW, I>(c); }
  else if constexpr (TX < NXT<PART>) k1_x1<PART, TX, 2 * (G & 1) + (I - 2)>(c);
}
template <bool ISV, int M> DI void kv_mfma(f32x16 (&acc)[4][2], const FragKV& f) {
  constexpr int r = M >> 1, ct = M & 1;
  const bf16x8 wf = cat(f.wl[ct], f.wh[ct]);
  acc[r][ct] = ISV ? MFMA(f.x[r], wf, acc[r][ct]) : MFMA(wf, f.x[r], acc[r][ct]);
}
template <bool ISV, int GN, int SN, int DMAG, bool LOAD, int M> DI void kv_gap(const K1C& c, f32x16 (&acc)[4][2], const FragKV& cur, FragKV& nxt) {
  kv_mfma<ISV, M>(acc, cur);
  if constexpr (LOAD) kv_ld<GN, SN, M>(c, nxt);
  if constexpr (DMAG >= 0 && (M & 1) && M < 8) k1_dma_piece<0, DMAG, (M >> 1)>(c);
  SBAR();
}
template <bool ISV, int GN, int SN, int DMAG, bool LOAD> DI void kv_phase(const K1C& c, f32x16 (&acc)[4][2], const FragKV& cur, FragKV& nxt) {
  kv_gap<ISV, GN, SN, DMAG, LOAD, 0>(c, acc, cur, nxt); kv_gap<ISV, GN, SN, DMAG, LOAD, 1>(c, acc, cur, nxt);
  kv_gap<ISV, GN, SN, DMAG, LOAD, 2>(c, acc, cur, nxt); kv_gap<ISV, GN, SN, DMAG, LOAD, 3>(c, acc, cur, nxt);
  kv_gap<ISV, GN, SN, DMAG, LOAD, 4>(c, acc, cur, nxt); kv_gap<ISV, GN, SN, DMAG, LOAD, 5>(c, acc, cur, nxt);
  kv_gap<ISV, GN, SN, DMAG, LOAD, 6>(c, acc, cur, nxt); kv_gap<ISV, GN, SN, DMAG, LOAD, 7>(c, acc, cur, nxt);
}
template <int G, bool ISV> DI void k1_step_kv(const K1C& c, f32x16 (&acc)[4][2], FragKV& fa, FragKV& fb) {
  wait_bar<waitN<0>(G)>(); SBAR();
  if constexpr (G == 0) {
    kv_ld<0, 0, 0>(c, fa); kv_ld<0, 0, 1>(c, fa); kv_ld<0, 0, 2>(c, fa); kv_ld<0, 0, 3>(c, fa);
    kv_ld<0, 0, 4>(c, fa); kv_ld<0, 0, 5>(c, fa); kv_ld<0, 0, 6>(c, fa); kv_ld<0, 0, 7>(c, fa); SBAR();
  }
  kv_phase<ISV, G, 1, G, true>(c, acc, fa, fb);
  kv_phase<ISV, G + 1, 0, -1, (G < 15)>(c, acc, fb, fa);
}
template <int G, bool ISV> struct KVLoop { static DI void run(const K1C& c, f32x16 (&acc)[4][2], FragKV& fa, FragKV& fb) { k1_step_kv<G, ISV>(c, acc, fa, fb); if constexpr (G < 15) KVLoop<G + 1, ISV>::run(c, acc, fa, fb); } };

template <int G, int SS, int I> DI void q_ld(const K1C& c, FragQ& f) {
  constexpr int T = G >> 1;
  lds_cptr xp = c.xb[2 * (G & 1) + SS] + (T % 3) * XSLOT;
  lds_cptr wp = c.wb + (G & 3) * WSLOT + SS * 4096;
  if constexpr (I == 0) f.x[0] = lds_rd16(xp);
  else if constexpr (I == 1) f.wl[0] = lds_tr(wp);
  else if constexpr (I == 2) f.wh[0] = lds_tr(wp + 256);
  else if constexpr (I == 3) f.x[1] = lds_rd16(xp + 4096);
  else if constexpr (I == 4) f.wl[1] = lds_tr(wp + 512);
  else f.wh[1] = lds_tr(wp + 768);
}
template <int M> DI void q_mfma(f32x16 (&acc)[2][2], const FragQ& f) {
  constexpr int r = M & 1, ct = M >> 1;
  acc[r][ct] = MFMA(cat(f.wl[ct], f.wh[ct]), f.x[r], acc[r][ct]);
}
template <int GN, int SN, int DMAG, bool LOAD> DI void q_phase(const K1C& c, f32x16 (&acc)[2][2], const FragQ& cur, FragQ& nxt) {
  q_mfma<0>(acc, cur); if constexpr (LOAD) { q_ld<GN, SN, 0>(c, nxt); q_ld<GN, SN, 1>(c, nxt); } SBAR();
  q_mfma<1>(acc, cur); if constexpr (LOAD) { q_ld<GN, SN, 2>(c, nxt); q_ld<GN, SN, 3>(c, nxt); } if constexpr (DMAG >= 0) k1_dma_piece<1, DMAG, 0>(c); SBAR();
  q_mfma<2>(acc, cur); if constexpr (LOAD) { q_ld<GN, SN, 4>(c, nxt); } if constexpr (DMAG >= 0) k1_dma_piece<1, DMAG, 2>(c); SBAR();
  q_mfma<3>(acc, cur); if constexpr (LOAD) { q_ld<GN, SN, 5>(c, nxt); } if constexpr (DMAG >= 0) k1_dma_piece<1, DMAG, 3>(c); SBAR();
}
template <int G> DI void k1_step_q(K1C& c, f32x16 (&acc)[2][2], FragQ& fa, FragQ& fb) {
  wait_bar<waitN<1>(G)>(); SBAR();
  if constexpr (G == 0) {
    q_ld<G, 0, 0>(c, fa); q_ld<G, 0, 1>(c, fa); q_ld<G, 0, 2>(c, fa); q_ld<G, 0, 3>(c, fa); q_ld<G, 0, 4>(c, fa); q_ld<G, 0, 5>(c, fa); SBAR();
  }
  q_phase<G, 1, G, true>(c, acc, fa, fb);
  if constexpr (G == 13) asm volatile("global_load_dword %0, %1, off sc1" : "=v"(c.flagv) : "v"(c.flagp) : "memory");
  if constexpr (G == 16) PIN(c.flagv);
  q_phase<G + 1, 0, -1, (G < 17)>(c, acc, fb, fa);
}
template <int G> struct QLoop { static DI void run(K1C& c, f32x16 (&acc)[2][2], FragQ& fa, FragQ& fb) { k1_step_q<G>(c, acc, fa, fb); if constexpr (G < 17) QLoop<G + 1>::run(c, acc, fa, fb); } };

constexpr int relK(int i) { return ((i >> 2) * 8 + ((i >> 1) & 1) * 2 + (i & 1)) * 1024; }
constexpr int relV(int i) { return (((i >> 2) >> 1) * 16 + (2 * ((i >> 2) & 1) + (i & 1)) * 4 + ((i >> 1) & 1)) * 1024; }
template <bool ISV> DI void k1_kv(K1C& c, bf16x8 (&kvp)[16]) {
  f32x16 acc[4][2];
#pragma unroll
  for (int r = 0; r < 4; ++r) { acc[r][0] = f32x16{}; acc[r][1] = f32x16{}; }
  FragKV fa, fb;
  KVLoop<0, ISV>::run(c, acc, fa, fb);
#pragma unroll
  for (int r = 0; r < 4; ++r)
#pragma unroll
    for (int ct = 0; ct < 2; ++ct)
#pragma unroll
      for (int g = 0; g < 2; ++g) kvp[r * 4 + ct * 2 + g] = pack8n(acc[r][ct], g);
  SBAR();
}

__global__ __launch_bounds__(512, 2) void k0_aproj(K1P p) {
  extern __shared__ __attribute__((aligned(16))) char lds[];
  const int tid = threadIdx.x, lane = tid & 63, l32 = lane & 31, hi = lane >> 5;
  const int b = blockIdx.x, xcd = b & 7, ii = b >> 3;
  struct { unsigned lds0; } c; c.lds0 = (unsigned)(size_t)(lds_ptr)lds;
  const int w = rfl(tid >> 6);
  const int blk = (lane >> 4) & 1, q4 = (lane & 15) >> 2, p4 = lane & 3;
  if (tid == 0) *(unsigned*)(p.ws + WS_FLAG + b * 128) = 0u;
  if (ii < 20) {
    const int rg = 4 * xcd + ii / 5, cg = ii % 5;
    const __amdgpu_buffer_rsrc_t sAX = mksrd((const char*)p.ax + (size_t)rg * 64 * 1152, 64 * 1152);
    const __amdgpu_buffer_rsrc_t sAQ = mksrd((const char*)p.awq + cg * 256, 576 * 1152 - cg * 256);
    constexpr int ASL = 73728, AWO = 24576;
    const unsigned xv = (unsigned)((lane >> 3) * 1152 + (((lane & 7) ^ ((4 * (w & 1) + (lane >> 4)) & 7)) * 16));
    const unsigned wv = (unsigned)(((lane & 31) >> 2) * 1152 + (32 * (lane >> 5) + 8 * (lane & 3)) * 2);
    auto issue = [&](int th) {
      const unsigned base = c.lds0 + (th & 1) * ASL;
#pragma unroll
      for (int kc = 0; kc < 3; ++kc) dma16(rfl(base + (kc * 8 + w) * 1024), xv, sAX, rfl((unsigned)(w * 8 * 1152 + (th * 3 + kc) * 128)));
#pragma unroll
      for (int i = 0; i < 6; ++i) {
        const int g = 3 * w + (i >> 1), pr = i & 1;
        dma16(rfl(base + AWO + (g * 2 + pr) * 1024), wv, sAQ, rfl((unsigned)((th * 192 + g * 8) * 1152 + pr * 128)));
      }
    };
    const int rt = w >> 2, ct = w & 3, tile = 4 * cg + ct;
    f32x16 acc = f32x16{};
    issue(0); issue(1);
    for (int th = 0; th < 3; ++th) {
      if (th < 2) asm volatile("s_waitcnt vmcnt(9) lgkmcnt(0)\n\ts_barrier" ::: "memory");
      else asm volatile("s_waitcnt vmcnt(0) lgkmcnt(0)\n\ts_barrier" ::: "memory");
      lds_cptr xp = (lds_cptr)lds + (th & 1) * ASL + rt * 4096 + 128 * l32;
      lds_cptr wp = (lds_cptr)lds + (th & 1) * ASL + AWO + ct * 512 + 2048 * hi + 64 * q4 + 32 * blk + 8 * p4;
      bf16x8 xf[4]; s16x4 wl[4], wh[4];
#define ALD(k) do { xf[(k) & 3] = lds_rd16(xp + ((k) >> 2) * 8192 + 16 * ((2 * ((k) & 3) + hi) ^ ((l32 >> 1) & 7))); wl[(k) & 3] = lds_tr(wp + (k) * 4096); wh[(k) & 3] = lds_tr(wp + (k) * 4096 + 256); } while (0)
      if (tile < 18) {
        ALD(0); ALD(1); ALD(2);
#pragma unroll
        for (int k = 0; k < 12; ++k) {
          if (k + 3 < 12) ALD(k + 3);
          acc = MFMA(cat(wl[k & 3], wh[k & 3]), xf[k & 3], acc);
          SBAR();
        }
      }
      if (th == 0) { asm volatile("s_waitcnt lgkmcnt(0)\n\ts_barrier" ::: "memory"); issue(2); }
    }
    const int row = (rg * 2 + rt) * 32 + l32;
    if (tile < 18) {
#pragma unroll
      for (int g = 0; g < 4; ++g) {
        u32x2 v = {cvtpk(acc[4 * g] * CA, acc[4 * g + 1] * CA), cvtpk(acc[4 * g + 2] * CA, acc[4 * g + 3] * CA)};
        *(u32x2*)(p.ws + WS_QA + (size_t)row * 1152 + (tile * 32 + 8 * g + 4 * hi) * 2) = v;
      }
    }
  } else if (ii < 28) {
    const int r32 = 8 * xcd + (ii - 20);
    const __amdgpu_buffer_rsrc_t sAX = mksrd((const char*)p.ax + (size_t)r32 * 32 * 1152, 32 * 1152);
    for (int pc = w; pc < 36; pc += 8) {
      const int kc = pc >> 2, rq = pc & 3, r = lane >> 3, row = 8 * rq + r, ch = (lane & 7) ^ ((row >> 1) & 7);
      dma16(rfl(c.lds0 + pc * 1024), (unsigned)(row * 1152 + ch * 16), sAX, rfl((unsigned)(kc * 128)));
    }
    {
      const unsigned* gk = (const unsigned*)p.awk; const unsigned* gv = (const unsigned*)p.awv;
      unsigned vk[11], vv[11];
#pragma unroll
      for (int it = 0; it < 11; ++it) { const int i = tid + it * 512; const int ic = i < 5184 ? i : 0; vk[it] = gk[ic]; vv[it] = gv[ic]; }
#pragma unroll
      for (int it = 0; it < 11; ++it) {
        const int i = tid + it * 512;
        if (i < 5184) { const int r = i / 9, d = i - r * 9; char* dst = lds + 36864 + (r >> 3) * 1024 + (r & 7) * 64 + d * 4; *(unsigned*)dst = vk[it]; *(unsigned*)(dst + 512) = vv[it]; }
      }
    }
    asm volatile("s_waitcnt vmcnt(0) lgkmcnt(0)\n\ts_barrier" ::: "memory");
    const int isv = w >> 2, kq = w & 3;
    f32x16 acc = f32x16{};
    lds_cptr xp = (lds_cptr)lds + 128 * l32;
    lds_cptr wp = (lds_cptr)lds + 36864 + isv * 512 + 1024 * hi + 64 * q4 + 32 * blk + 8 * p4;
#pragma unroll
    for (int k9 = 0; k9 < 9; ++k9) {
      const int k = kq * 9 + k9;
      const bf16x8 xf = lds_rd16(xp + (k >> 2) * 4096 + 16 * ((2 * (k & 3) + hi) ^ ((l32 >> 1) & 7)));
      const bf16x8 wf = cat(lds_tr(wp + k * 2048), lds_tr(wp + k * 2048 + 256));
      if (isv) acc = MFMA(xf, wf, acc); else acc = MFMA(wf, xf, acc);
    }
    asm volatile("s_waitcnt lgkmcnt(0)\n\ts_barrier" ::: "memory");
    float* red = (float*)lds + w * 1024;
#pragma unroll
    for (int q = 0; q < 4; ++q) *(f32x4*)(red + q * 256 + lane * 4) = f32x4{acc[4 * q], acc[4 * q + 1], acc[4 * q + 2], acc[4 * q + 3]};
    __syncthreads();
    if (kq == 0) {
#pragma unroll
      for (int o = 1; o < 4; ++o)
#pragma unroll
        for (int q = 0; q < 4; ++q) { const f32x4 y = *(const f32x4*)(red + o * 1024 + q * 256 + lane * 4); acc[4 * q] += y[0]; acc[4 * q + 1] += y[1]; acc[4 * q + 2] += y[2]; acc[4 * q + 3] += y[3]; }
      const int row = r32 * 32 + l32;
      char* hb = p.ws + WS_KA + (size_t)(r32 >> 5) * AHB; const int rl = r32 & 31;
      if (!isv) {
        char* k0 = hb + rl * 1024;
        const unsigned w0 = cvtpk(acc[0], acc[1]), w1 = cvtpk(acc[2], acc[3]), w2 = cvtpk(acc[4], acc[5]), w3 = cvtpk(acc[6], acc[7]), w4 = cvtpk(acc[8], acc[9]);
        *(u32x2*)(k0 + (l32) * 16 + 8 * hi) = u32x2{w0, w1};
        *(u32x2*)(k0 + (l32 + 32) * 16 + 8 * hi) = u32x2{w2, w3};
        if (hi == 0) *(u32x4*)(hb + 32768 + (size_t)(rl * 32 + l32) * 16) = u32x4{w4, 0x00003F80u, 0u, 0u};
        auto sq = [](unsigned x) { const float a = __uint_as_float(x << 16), bq = __uint_as_float(x & 0xFFFF0000u); return a * a + bq * bq; };
        float s2 = sq(w0) + sq(w1) + sq(w2) + sq(w3) + (hi == 0 ? sq(w4) : 0.f);
        s2 = hsum(s2);
#pragma unroll
        for (int o = 16; o >= 1; o >>= 1) s2 = fmaxf(s2, __shfl_xor(s2, o));
        if (lane == 0) *(float*)(p.ws + WS_KMAX + r32 * 4) = s2;
      } else {
#pragma unroll
        for (int g = 0; g < 2; ++g) {
          bf16x8 v = pack8n(acc, g);
          if (l32 == 18) v = bf16x8{0x3F80, 0x3F80, 0x3F80, 0x3F80, 0x3F80, 0x3F80, 0x3F80, 0x3F80};
          if (l32 < 19) *(bf16x8*)(hb + 49152 + ((size_t)((2 * rl + g) * 2 + hi) * 19 + l32) * 16) = v;
        }
      }
    }
  }

}

constexpr float LTHR = 7.9228162514264338e28f;
#define LTHR_V LTHR
struct AttB {
  f32x16 o[4]; bf16x8 qf[8]; f32x16 negm; float m, l;
  f32x16 s0, s1;
  unsigned pw[16];
  bf16x8 fr[4];
  float ps;
};
DI void cmask1(f32x16& s, int key0, int row) {
  const int d = row - key0;
#pragma unroll
  for (int r = 0; r < 16; ++r) { const int c = (r & 3) + 8 * (r >> 2); s[r] = (c > d) ? -INFINITY : s[r]; }
}
DI float max16(const f32x16& s) { float a = fmaxf(fmaxf(s[0], s[1]), s[2]);
#pragma unroll
  for (int r = 3; r < 15; r += 2) a = fmaxf(fmaxf(a, s[r]), s[r + 1]);
  return fmaxf(a, s[15]); }
DI f32x16 splat16(float x) { f32x16 v;
#pragma unroll
  for (int r = 0; r < 16; ++r) v[r] = x;
  return v; }
#define PKB(w) __builtin_bit_cast(bf16x8, w)
#define EXP2(x) __builtin_amdgcn_exp2f(x)
DI bf16x8 pfrag(const AttB& a, int ks) { u32x4 w = {a.pw[4 * ks], a.pw[4 * ks + 1], a.pw[4 * ks + 2], a.pw[4 * ks + 3]}; return __builtin_bit_cast(bf16x8, w); }

template <bool HASCUR> DI void rare_fix(AttB& a, __amdgpu_buffer_rsrc_t sK, __amdgpu_buffer_rsrc_t sQ, unsigned voff, unsigned kso, unsigned qso, int key0, int row, bool mask) {
  float tm = -INFINITY;
  f32x16 t;
#pragma unroll 1
  for (int kb = 0; kb < 2; ++kb) {
    t = f32x16{};
#pragma unroll 2
    for (int ds = 0; ds < 8; ++ds) t = MFMA(bld16s(sK, voff, kso + kb * 8192 + ds * 1024), bld16(sQ, voff, qso + ds * 1024), t);
    if (mask) cmask1(t, key0 + 32 * kb, row);
    tm = fmaxf(tm, max16(t));
  }
  tm = hmax(tm);
  const float mn = fmaxf(a.m, tm), dl = mn - a.m, f = EXP2(-dl);
#pragma unroll
  for (int db = 0; db < 4; ++db)
#pragma unroll
    for (int r = 0; r < 16; ++r) a.o[db][r] *= f;
  float ps = 0.f;
#pragma unroll 1
  for (int kb = 0; kb < 2; ++kb) {
    t = f32x16{};
#pragma unroll 2
    for (int ds = 0; ds < 8; ++ds) t = MFMA(bld16s(sK, voff, kso + kb * 8192 + ds * 1024), bld16(sQ, voff, qso + ds * 1024), t);
    if (mask) cmask1(t, key0 + 32 * kb, row);
#pragma unroll
    for (int r = 0; r < 16; ++r) { t[r] = EXP2(t[r] - mn); ps += t[r]; }
    if (kb == 0) {
#pragma unroll
      for (int k = 0; k < 8; ++k) a.pw[k] = cvtpk(t[2 * k], t[2 * k + 1]);
    } else {
#pragma unroll
      for (int k = 0; k < 8; ++k) a.pw[8 + k] = cvtpk(t[2 * k], t[2 * k + 1]);
    }
  }
  a.l *= f; a.ps = ps;
  if (HASCUR) {
#pragma unroll
    for (int r = 0; r < 16; ++r) a.s0[r] -= dl;
  }
  a.m = mn; a.negm = splat16(-mn); PIN(a.negm);
}

struct StepCtx {
  lds_cptr kp;
  lds_cptr kpn;
  lds_cptr vp;
  unsigned m0k, m0v, sok, sov; bool dk, dv;
  unsigned voff;
};
constexpr int M_STEADY = 0, M_FIRST = 1, M_SWITCH = 2, M_DRAIN = 3;
template <int I, int MODE> DI void gapA(AttB& a, const StepCtx& c, __amdgpu_buffer_rsrc_t sK, __amdgpu_buffer_rsrc_t sV) {
  constexpr bool PREV = MODE != M_FIRST, CUR = MODE != M_DRAIN, NEW = MODE == M_FIRST || MODE == M_SWITCH;
  constexpr int ds = I & 7;
  if constexpr (CUR) {
    if constexpr (I < 8) { if constexpr (ds == 0) a.s0 = NEW ? MFMA(a.fr[I & 3], a.qf[0], f32x16{}) : MFMA(a.fr[I & 3], a.qf[0], a.negm); else a.s0 = MFMA(a.fr[I & 3], a.qf[ds], a.s0); }
    else { if constexpr (ds == 0) a.s1 = NEW ? MFMA(a.fr[I & 3], a.qf[0], f32x16{}) : MFMA(a.fr[I & 3], a.qf[0], a.negm); else a.s1 = MFMA(a.fr[I & 3], a.qf[ds], a.s1); }
  }
  if constexpr (I + 3 < 16) { if constexpr (CUR) a.fr[(I + 3) & 3] = lds_rd16(c.kp + (I + 3) * 1024); }
  else if constexpr (PREV) a.fr[(I + 3) & 3] = lds_rd16(c.vp + (I + 3 - 16) * 1024);
  if constexpr (I < 8) {
    if constexpr (PREV) {
      a.s1[8 + I] = EXP2(a.s1[8 + I]);
      a.ps += a.s1[7 + I];
      if constexpr (I < 4) { a.pw[8 + I] = cvtpk(a.s1[2 * I], a.s1[2 * I + 1]); PIN(a.pw[8 + I]); }
      else if constexpr (I != 6) { constexpr int k = I == 7 ? 6 : I; a.pw[8 + k] = cvtpk(a.s1[2 * k], a.s1[2 * k + 1]); PIN(a.pw[8 + k]); }
    }
    if constexpr (I & 1) dma16os<(I >> 1) * 1024>(c.m0v, c.voff, sV, c.sov);
  } else {
    constexpr int i = I - 8;
    if constexpr (MODE == M_STEADY) {
      a.s0[i] = EXP2(a.s0[i]);
      if constexpr (i >= 1) a.ps += a.s0[i - 1];
    }
    if constexpr (i & 1) dma16os<(i >> 1) * 1024>(c.m0k, c.voff, sK, c.sok);
  }
  PIN(a.ps);
  SBAR();
}
template <int I, int MODE> DI void gapB(AttB& a, const StepCtx& c, float& mx0, float& mx1) {
  constexpr bool PREV = MODE != M_FIRST, NEW = MODE == M_FIRST || MODE == M_SWITCH;
  constexpr int ks = I >> 2, db = I & 3;
  if constexpr (PREV) {
    a.o[db] = MFMA(a.fr[I & 3], pfrag(a, ks), a.o[db]);
    if constexpr (I + 3 < 16) a.fr[(I + 3) & 3] = lds_rd16(c.vp + (I + 3) * 1024);
  }
  if constexpr (I + 3 >= 16) a.fr[(I + 3) & 3] = lds_rd16(c.kpn + (I + 3 - 16) * 1024);
  if constexpr (MODE == M_STEADY) {
    if constexpr (I < 8) {
      a.s0[8 + I] = EXP2(a.s0[8 + I]);
      a.ps += a.s0[7 + I];
      if constexpr (I >= 4) { constexpr int k = I - 4; a.pw[k] = cvtpk(a.s0[2 * k], a.s0[2 * k + 1]); PIN(a.pw[k]); }
    } else {
      constexpr int i = I - 8;
      a.s1[i] = EXP2(a.s1[i]);
      if constexpr (i == 0) a.ps += a.s0[15]; else a.ps += a.s1[i - 1];
      if constexpr (I < 12) { constexpr int k = I - 4; a.pw[k] = cvtpk(a.s0[2 * k], a.s0[2 * k + 1]); PIN(a.pw[k]); }
    }
  } else if constexpr (NEW) {
    if constexpr (I < 4) {
      constexpr int r = 4 * I;
      if constexpr (I == 0) { mx0 = fmaxf(fmaxf(a.s0[0], a.s0[1]), a.s0[2]); mx1 = fmaxf(fmaxf(a.s1[0], a.s1[1]), a.s1[2]); mx0 = fmaxf(mx0, a.s0[3]); mx1 = fmaxf(mx1, a.s1[3]); }
      else { mx0 = fmaxf(fmaxf(mx0, a.s0[r]), a.s0[r + 1]); mx1 = fmaxf(fmaxf(mx1, a.s1[r]), a.s1[r + 1]); mx0 = fmaxf(fmaxf(mx0, a.s0[r + 2]), a.s0[r + 3]); mx1 = fmaxf(fmaxf(mx1, a.s1[r + 2]), a.s1[r + 3]); }
      PIN(mx0); PIN(mx1);
    } else {
      if constexpr (I == 4) { mx0 = fmaxf(hmax(fmaxf(mx0, mx1)), -1e30f); a.m = mx0; a.ps = 0.f; }
      if constexpr (I < 12) {
        constexpr int i = 2 * (I - 4);
        a.s0[i] = EXP2(a.s0[i] - mx0); a.s0[i + 1] = EXP2(a.s0[i + 1] - mx0);
        if constexpr (i >= 2) { a.ps += a.s0[i - 2]; a.ps += a.s0[i - 1]; }
      } else {
        constexpr int i = 2 * (I - 12);
        if constexpr (I == 12) { a.ps += a.s0[14]; a.ps += a.s0[15]; }
        a.s1[i] = EXP2(a.s1[i] - mx0); a.s1[i + 1] = EXP2(a.s1[i + 1] - mx0);
        a.s1[8 + i] -= mx0; a.s1[9 + i] -= mx0;
        if constexpr (i >= 2) { a.ps += a.s1[i - 2]; a.ps += a.s1[i - 1]; }
      }
      if constexpr (I >= 8) { constexpr int k = I - 8; if constexpr (2 * k + 1 < 2 * (I - 4)) { a.pw[k] = cvtpk(a.s0[2 * k], a.s0[2 * k + 1]); PIN(a.pw[k]); } }
    }
  }
  PIN(a.ps);
  SBAR();
}
template <int O, int MODE, int... Is> DI void phaseA_(AttB& a, const StepCtx& c, __amdgpu_buffer_rsrc_t sK, __amdgpu_buffer_rsrc_t sV, std::integer_sequence<int, Is...>) { (gapA<O + Is, MODE>(a, c, sK, sV), ...); }
template <int MODE, int... Is> DI void phaseB_(AttB& a, const StepCtx& c, float& mx0, float& mx1, std::integer_sequence<int, Is...>) { (gapB<Is, MODE>(a, c, mx0, mx1), ...); }

struct K2P { char* ws; };
struct AttEnv {
  int grp, wq, lane, hi, l32, n1, L, head; char* lds; unsigned lds0; char* ws;
  __amdgpu_buffer_rsrc_t sK, sV, sQ, sC;
};
DI int tile_of(const AttEnv& e, int x) { return e.grp == 0 ? x : (x < e.n1 ? x : 17 + (x - e.n1)); }
DI void step_top(const AttEnv& e, int s, StepCtx& c) {
  asm volatile("s_waitcnt vmcnt(0)\n\ts_barrier" ::: "memory");
  SBAR();
  const unsigned gb = e.grp * 81920;
  const int k3 = s % 3, k3n = (s + 1) % 3, k3d = (s + 2) % 3;
  c.kp = (lds_cptr)e.lds + gb + k3 * 16384 + e.lane * 16;
  c.kpn = (lds_cptr)e.lds + gb + k3n * 16384 + e.lane * 16;
  c.vp = (lds_cptr)e.lds + gb + 49152 + ((s + 1) & 1) * 16384 + e.lane * 16;
  c.dk = (s + 2 < 17); c.dv = (s < 17);
  c.m0k = rfl(e.lds0 + gb + k3d * 16384 + e.wq * 4096); c.m0v = rfl(e.lds0 + gb + 49152 + (s & 1) * 16384 + e.wq * 4096);
  c.sok = rfl((unsigned)(tile_of(e, s + 2) * 16384 + e.wq * 4096)); c.sov = rfl((unsigned)(tile_of(e, s) * 16384 + e.wq * 4096));
  c.voff = e.lane * 16;
}
DI void dma_all(const AttEnv& e, const StepCtx& c) {
  if (c.dk) {
#pragma unroll
    for (int i = 0; i < 4; ++i) dma16s(c.m0k + i * 1024, c.voff, e.sK, c.sok + i * 1024); }
  if (c.dv) {
#pragma unroll
    for (int i = 0; i < 4; ++i) dma16s(c.m0v + i * 1024, c.voff, e.sV, c.sov + i * 1024); }
}
template <bool HASCUR> DI void check_prev(AttB& a, const AttEnv& e, int qb, int prev_t) {
  if (__builtin_expect(__any(!(a.l + a.ps <= LTHR_V)), 0)) {
    const int rmin = qb * 128 + e.wq * 32;
    rare_fix<HASCUR>(a, e.sK, e.sQ, e.lane * 16, rfl((unsigned)(prev_t * 16384)), rfl((unsigned)(((qb == e.L ? 0 : 4) + e.wq) * 8192)),
                     prev_t * 64 + 4 * e.hi, rmin + e.l32, prev_t * 64 + 63 > rmin);
  }
  a.l += a.ps; a.ps = 0.f;
}
DI void store_block(AttB& a, const AttEnv& e, int qb) {
  const float inv = 1.f / hsum(a.l);
#pragma unroll
  for (int db = 0; db < 4; ++db)
#pragma unroll
    for (int g = 0; g < 2; ++g)
      bst16(pack8(a.o[db], g, inv), e.sC, e.lane * 16, rfl((unsigned)(((qb * 4 + e.wq) * 256 + db * 2 + g) * 1024)));
}
template <int MODE> DI void att_step(AttB& a, const AttEnv& e, int s, int qb_prev, int qb, bool loadq = false) {
  constexpr bool PREV = MODE != M_FIRST, CUR = MODE != M_DRAIN, NEW = MODE == M_FIRST || MODE == M_SWITCH;
  StepCtx c; step_top(e, s, c);
  if constexpr (NEW) asm volatile("" : "+v"(a.qf[0]), "+v"(a.qf[1]), "+v"(a.qf[2]), "+v"(a.qf[3]), "+v"(a.qf[4]), "+v"(a.qf[5]), "+v"(a.qf[6]), "+v"(a.qf[7]));
  const int t = tile_of(e, s), rmin = qb * 128 + e.wq * 32;
  const bool mask = t * 64 + 63 > rmin;
  if constexpr (MODE == M_FIRST) {
#pragma unroll
    for (int J = 0; J < 3; ++J) a.fr[J] = lds_rd16(c.kp + J * 1024);
  }
  phaseA_<0, MODE>(a, c, e.sK, e.sV, std::make_integer_sequence<int, 8>{});
  if constexpr (PREV) {
    a.ps += a.s1[15]; a.pw[15] = cvtpk(a.s1[14], a.s1[15]);
    check_prev<MODE == M_STEADY>(a, e, qb_prev, tile_of(e, s - 1));
  }
  if constexpr (MODE == M_STEADY) { if (mask) cmask1(a.s0, t * 64 + 4 * e.hi, rmin + e.l32); }
  SBAR();
  phaseA_<8, MODE>(a, c, e.sK, e.sV, std::make_integer_sequence<int, 8>{});
  if constexpr (CUR) {
    if (mask) { if constexpr (NEW) cmask1(a.s0, t * 64 + 4 * e.hi, rmin + e.l32); cmask1(a.s1, t * 64 + 32 + 4 * e.hi, rmin + e.l32); }
  }
  if constexpr (MODE == M_FIRST || MODE == M_STEADY) {
    if (loadq) {
#pragma unroll
      for (int ds = 0; ds < 8; ++ds)
        asm volatile("buffer_load_dwordx4 %0, %1, %2, %3 offen" : "=v"(a.qf[ds]) : "v"(e.lane * 16), "s"(e.sQ), "s"(rfl((unsigned)(e.wq * 8192 + ds * 1024))) : "memory");
    }
  }
  SBAR();
  float mx0 = 0.f, mx1 = 0.f; const float lold = a.l;
  phaseB_<MODE>(a, c, mx0, mx1, std::make_integer_sequence<int, 16>{});
  if constexpr (MODE == M_FIRST || MODE == M_STEADY) {
    if (loadq) asm volatile("s_waitcnt vmcnt(0)" : "+v"(a.qf[0]), "+v"(a.qf[1]), "+v"(a.qf[2]), "+v"(a.qf[3]), "+v"(a.qf[4]), "+v"(a.qf[5]), "+v"(a.qf[6]), "+v"(a.qf[7]) :: "memory");
  }
  if constexpr (NEW) {
    a.ps += a.s1[6];
    if constexpr (MODE == M_SWITCH) { a.l = lold; store_block(a, e, qb_prev); }
#pragma unroll
    for (int db = 0; db < 4; ++db) a.o[db] = f32x16{};
    a.l = 0.f; a.negm = splat16(-a.m); PIN(a.negm);
  }
}

struct AttA { f32x16 o; bf16x8 qf0, qf1; };
constexpr int AH_L = 65536;
#define E4(sc, i) do { sc[i] = EXP2(sc[i]); sc[i + 1] = EXP2(sc[i + 1]); sc[i + 2] = EXP2(sc[i + 2]); sc[i + 3] = EXP2(sc[i + 3]); } while (0)
#define C2(pw, j, sc, i) do { pw[j] = cvtpk(sc[i], sc[i + 1]); pw[j + 1] = cvtpk(sc[i + 2], sc[i + 3]); } while (0)
struct KVSt { __amdgpu_buffer_rsrc_t srd; unsigned voff; bool isv; };
template <bool ST> DI void a_half(AttA& A, char* lds, int lane, const bf16x8 (&kvp)[16], const KVSt& ks) {
  const int l32 = lane & 31, hi = lane >> 5;
  const int vrow = l32 < 19 ? l32 : (((l32 >= 20 && l32 <= 27)) ? 0 : 4);
  lds_cptr k0p = (lds_cptr)lds + AH_L + lane * 16;
  lds_cptr k1p = (lds_cptr)lds + AH_L + 32768 + l32 * 16;
  lds_cptr vp = (lds_cptr)lds + AH_L + 49152 + (hi * 19 + vrow) * 16;
  f32x16 sA, sB; u32x4 pa0, pa1, pb0, pb1;
  bf16x8 k0, k1, v0, v1, w0, w1;
#define LDK(kt) do { k0 = lds_rd16(k0p + (kt) * 1024); k1 = lds_rd16(k1p + (kt) * 512); } while (0)
#define ASTEP(SC, SN, PP0, PP1, PN0, PN1, kt, VC0, VC1, VN0, VN1, DOQK, DOPV, DOLDK) do { \
    if (DOQK) { SN = MFMA(k0, A.qf0, f32x16{}); } SBAR(); VN0 = lds_rd16(vp + (2 * (kt)) * 608); VN1 = lds_rd16(vp + (2 * (kt) + 1) * 608); E4(SC, 0); PIN(SC); SBAR(); \
    if (DOPV) { A.o = MFMA(VC0, PKB(PP0), A.o); } SBAR(); E4(SC, 4); C2(PN0, 0, SC, 0); PIN(SC); PIN(PN0); SBAR(); \
    if (DOQK) { SN = MFMA(k1, A.qf1, SN); } SBAR(); if (DOLDK) LDK((kt) + 2); E4(SC, 8); C2(PN0, 2, SC, 4); PIN(SC); PIN(PN0); SBAR(); \
    if (DOPV) { A.o = MFMA(VC1, PKB(PP1), A.o); } SBAR(); E4(SC, 12); C2(PN1, 0, SC, 8); C2(PN1, 2, SC, 12); PIN(SC); PIN(PN1); SBAR(); } while (0)
  pa0 = u32x4{0,0,0,0}; pa1 = pa0; pb0 = pa0; pb1 = pa0; v0 = bf16x8{}; v1 = v0; w0 = v0; w1 = v0;
  LDK(0); sA = MFMA(k0, A.qf0, f32x16{}); sA = MFMA(k1, A.qf1, sA); LDK(1);
  ASTEP(sA, sB, pb0, pb1, pa0, pa1, 0, v0, v1, w0, w1, true, false, true);
#define KVST(i) do { if (ST) { st16_sc1(kvp[i], ks.voff, ks.srd, ks.isv ? (unsigned)relV(i) : (unsigned)relK(i)); SBAR(); } } while (0)
  if constexpr (ST) {
    KVST(0);
#define APAIR(kt, i) ASTEP(sB, sA, pa0, pa1, pb0, pb1, kt, w0, w1, v0, v1, true, true, true); KVST(i); ASTEP(sA, sB, pb0, pb1, pa0, pa1, (kt) + 1, v0, v1, w0, w1, true, true, true)
    APAIR(1, 1); APAIR(3, 2); APAIR(5, 3); APAIR(7, 4); APAIR(9, 5); APAIR(11, 6); APAIR(13, 7); APAIR(15, 8); APAIR(17, 9); APAIR(19, 10); APAIR(21, 11); APAIR(23, 12); APAIR(25, 13); APAIR(27, 14);
    KVST(15);
#undef APAIR
  } else {
    for (int kt = 1; kt < 29; kt += 2) {
      ASTEP(sB, sA, pa0, pa1, pb0, pb1, kt, w0, w1, v0, v1, true, true, true);
      ASTEP(sA, sB, pb0, pb1, pa0, pa1, kt + 1, v0, v1, w0, w1, true, true, true);
    }
  }
  ASTEP(sB, sA, pa0, pa1, pb0, pb1, 29, w0, w1, v0, v1, true, true, true);
  ASTEP(sA, sB, pb0, pb1, pa0, pa1, 30, v0, v1, w0, w1, true, true, false);
  ASTEP(sB, sA, pa0, pa1, pb0, pb1, 31, w0, w1, v0, v1, false, true, false);
  A.o = MFMA(v0, PKB(pb0), A.o); A.o = MFMA(v1, PKB(pb1), A.o);
#undef LDK
#undef ASTEP
#undef KVST
}
DI void a_dma(unsigned lds0, int w, unsigned voff, char* ws, int hf) {
  const __amdgpu_buffer_rsrc_t sr = mksrd(ws + WS_KA + (size_t)hf * AHB, AHB);
#pragma unroll
  for (int i = 0; i < 11; ++i) { const int pc = w + 8 * i; dma16(rfl(lds0 + AH_L + (pc < 86 ? pc : 85) * 1024), voff, sr, rfl((unsigned)((pc < 86 ? pc : 85) * 1024))); }
}
DI void a_finish(AttA& A, char* ws, int lane, int qrow, int ah) {
  const int l32 = lane & 31, hi = lane >> 5;
  f32x16 o = A.o;
  float l = o[10];
  { auto rr = __builtin_amdgcn_permlane32_swap(__float_as_uint(l), __float_as_uint(l), false, false); l = __uint_as_float(rr[0]); }
  if (__builtin_expect(__any(!(l >= 8.6736174e-19f)), 0)) {
    float m = -1e30f; o = f32x16{};
    bf16x8 qz = A.qf1; qz[2] = 0;
    const int vrow = l32 < 19 ? l32 : 0;
    for (int kt = 0; kt < 64; ++kt) {
      const char* hb = ws + WS_KA + (size_t)(kt >> 5) * AHB; const int kl = kt & 31;
      const bf16x8 k0 = *(const bf16x8*)(hb + kl * 1024 + lane * 16), k1 = *(const bf16x8*)(hb + 32768 + (kl * 32 + l32) * 16);
      const bf16x8 v0 = *(const bf16x8*)(hb + 49152 + ((2 * kl) * 38 + hi * 19 + vrow) * 16), v1 = *(const bf16x8*)(hb + 49152 + ((2 * kl + 1) * 38 + hi * 19 + vrow) * 16);
      f32x16 sc = MFMA(k0, A.qf0, f32x16{});
      sc = MFMA(k1, qz, sc);
      const float mn = fmaxf(m, hmax(max16(sc))), f = EXP2(m - mn);
      m = mn;
#pragma unroll
      for (int r = 0; r < 16; ++r) { sc[r] = EXP2(sc[r] - mn); o[r] *= f; }
      o = MFMA(v0, pack8n(sc, 0), o);
      o = MFMA(v1, pack8n(sc, 1), o);
    }
    l = o[10];
    auto rr = __builtin_amdgcn_permlane32_swap(__float_as_uint(l), __float_as_uint(l), false, false); l = __uint_as_float(rr[0]);
  }
  const float inv = 1.f / l;
  char* ob = ws + WS_OA + (size_t)(qrow >> 5) * 36 * 1024 + l32 * 16;
  auto st2 = [&](int d, float x, float y) { const int c = ah * 18 + d; *(unsigned*)(ob + (c >> 4) * 1024 + ((c >> 2) & 1) * 512 + (((c >> 3) & 1) * 4 + (c & 3)) * 2) = cvtpk(x * inv, y * inv); };
  st2(4 * hi, o[0], o[1]); st2(4 * hi + 2, o[2], o[3]); st2(8 + 4 * hi, o[4], o[5]); st2(10 + 4 * hi, o[6], o[7]);
  if (hi == 0) st2(16, o[8], o[9]);
}

__global__ __launch_bounds__(512, 2) void kb_main(K1P p) {
  extern __shared__ __attribute__((aligned(16))) char lds[];
  const int tid = threadIdx.x, lane = tid & 63, l32 = lane & 31, hi = lane >> 5;
  const int b = blockIdx.x, xcd = b & 7, ii = b >> 3, head = xcd * 4 + (ii & 3), j = ii >> 2;
  K1C c;
  c.lds0 = (unsigned)(size_t)(lds_ptr)lds; c.w = rfl(tid >> 6);
  const int w = c.w;
  {
    const int r = lane >> 3, chp = lane & 7, row = 8 * w + r, ch = chp ^ ((4 * (w & 1) + (r >> 1)) & 7);
    c.xoffC = (unsigned)(row * 1024 + ch * 16); c.xoffX = (unsigned)(row * 1152 + ch * 16);
    const int sub = lane >> 5, rr = (lane & 31) >> 2, c4 = lane & 3;
    c.woff = (unsigned)((8 * (w >> 1) + rr) * 8192 + (32 * (2 * (w & 1) + sub) + 8 * c4) * 2);
  }
  c.sC = mksrd((const char*)p.cx + (size_t)j * 256 * 1024, 256 * 1024);
  c.sX = mksrd((const char*)p.bx, 2048 * 1152);
  c.rowL = (unsigned)((15 - j) * 128 * 1152); c.rowS = (unsigned)(j * 128 * 1152);
  c.sUK = mksrd((const char*)p.wuk + head * 256, 512 * 8192 - head * 256);
  c.sUV = mksrd((const char*)p.wuv + head * 256, 512 * 8192 - head * 256);
  c.sWQ = mksrd((const char*)p.wq + head * 256, 576 * 8192 - head * 256);
  const int blk = (lane >> 4) & 1, q4 = (lane & 15) >> 2, p4 = lane & 3;
  const unsigned wlane = 2048 * hi + 64 * q4 + 32 * blk + 8 * p4;
  {
    const int wr = w >> 2, wc = w & 3;
#pragma unroll
    for (int s = 0; s < 4; ++s) c.xb[s] = (lds_cptr)lds + (wr * 4) * 4096 + 128 * l32 + 16 * ((2 * s + hi) ^ ((l32 >> 1) & 7));
    c.wb = (lds_cptr)lds + WRING + (wc >> 1) * 8192 + (2 * (wc & 1)) * 512 + wlane;
  }
  STAMP(p.ws, 0, 0);
  const unsigned lds0 = c.lds0, voff = lane * 16;
  AttA A;
  const int aqrow = j * 256 + w * 32 + l32;
  unsigned wv[7] = {0, 0, 0, 0, 0, 0, 0};
  {
    a_dma(lds0, w, voff, p.ws, 0);
    const char* qp = p.ws + WS_QA + (size_t)aqrow * 1152 + head * 36;
    u32x4 q0; { const unsigned* qq = (const unsigned*)(qp + 16 * hi); q0 = u32x4{qq[0], qq[1], qq[2], qq[3]}; }
    const unsigned q16 = *(const unsigned*)(qp + 32);
    float km = *(const float*)(p.ws + WS_KMAX + lane * 4);
#pragma unroll
    for (int o = 32; o >= 1; o >>= 1) km = fmaxf(km, __shfl_xor(km, o));
    float qs = 0.f;
#pragma unroll
    for (int e = 0; e < 4; ++e) { const float x = __uint_as_float(q0[e] << 16), y = __uint_as_float(q0[e] & 0xFFFF0000u); qs += x * x + y * y; }
    if (hi == 0) { const float x = __uint_as_float(q16 << 16), y = __uint_as_float(q16 & 0xFFFF0000u); qs += x * x + y * y; }
    qs = hsum(qs);
    const float bound = sqrtf(qs * km) * 1.0009765625f + 1e-30f;
    const unsigned bb = (__float_as_uint(bound) + 0xFFFFu) >> 16;
    A.qf0 = __builtin_bit_cast(bf16x8, q0);
    A.qf1 = __builtin_bit_cast(bf16x8, hi == 0 ? u32x4{q16, bb | 0x8000u, 0u, 0u} : u32x4{0u, 0u, 0u, 0u});
    A.o = f32x16{};
    PIN(A.qf0); PIN(A.qf1);
    k1_issueX<0, 0>(c); k1_issueX<0, 1>(c);
    asm volatile("s_waitcnt vmcnt(8)\n\ts_barrier" ::: "memory");
    STAMP(p.ws, 0, 6);
    if (WARM) {
      const char* src[7] = {(const char*)p.cx, (const char*)p.wuk, (const char*)p.wuv, (const char*)p.bx, (const char*)p.wq, (const char*)p.wo, (const char*)p.awo};
      const unsigned nline[7] = {2048u * 1024 / 128, 512u * 8192 / 128, 512u * 8192 / 128, 2048u * 1152 / 128, 576u * 8192 / 128, 4096u * 1152 / 128, 576u * 1152 / 128};
#pragma unroll
      for (int a7 = 0; a7 < 7; ++a7) {
        const unsigned i = (unsigned)(b * 192 + tid);
        if (tid < 192 && i < nline[a7]) wv[a7] = *(const unsigned*)(src[a7] + (size_t)i * 128);
      }
    }
    unsigned wv7 = 0;
    if (WARM && tid >= 192 && tid < 192 + 22) { const unsigned i = (unsigned)(ii * 22 + tid - 192); if (i < 688) wv7 = *(const unsigned*)(p.ws + WS_KA + AHB + (size_t)i * 128); }
    { bf16x8 dummy[16]; KVSt ks0; a_half<false>(A, lds, lane, dummy, ks0); }
    wv[0] ^= wv7;
    if ((wv[0] ^ wv[1] ^ wv[2] ^ wv[3] ^ wv[4] ^ wv[5] ^ wv[6]) == 0x9E3779B9u) *(unsigned*)(p.ws + WS_FLAG + 256 * 128 + 64) = 1u;
    STAMP(p.ws, 0, 7);
  }
  asm volatile("s_waitcnt lgkmcnt(0)\n\ts_barrier" ::: "memory");
  k1_issueW<0, 0>(c); k1_issueW<0, 1>(c); k1_issueW<0, 2>(c);
  {
    bf16x8 kvp[16];
    if ((w & 2) == 0) k1_kv<false>(c, kvp); else k1_kv<true>(c, kvp);
    STAMP(p.ws, 0, 1);
    asm volatile("s_waitcnt lgkmcnt(0)\n\ts_barrier" ::: "memory");
    a_dma(lds0, w, voff, p.ws, 1);
    k1_issueX<1, 0>(c); k1_issueX<1, 1>(c);
    const int wr = w >> 2, wc1 = w & 1;
    KVSt ks; ks.isv = (w & 2) != 0; ks.voff = voff;
    const size_t base = ks.isv ? WS_VF + ((size_t)(head * 32 + 4 * j + 2 * wr) * 16 + 2 * wc1) * 1024 : WS_KF + ((size_t)(head * 64 + 8 * j + 4 * wr) * 8 + 4 * wc1) * 1024;
    ks.srd = mksrd(p.ws + base, 65536);
    asm volatile("s_waitcnt vmcnt(8)\n\ts_barrier" ::: "memory");
    STAMP(p.ws, 0, 8);
    a_half<true>(A, lds, lane, kvp, ks);
  }
  a_finish(A, p.ws, lane, aqrow, head);
  STAMP(p.ws, 0, 5);
  asm volatile("s_waitcnt vmcnt(0) lgkmcnt(0)\n\ts_barrier" ::: "memory");
  if (tid == 0) __hip_atomic_store((__attribute__((address_space(1))) unsigned*)(p.ws + WS_FLAG + (head * 8 + j) * 128), 1u, __ATOMIC_RELAXED, __HIP_MEMORY_SCOPE_AGENT);
  k1_issueW<1, 0>(c); k1_issueW<1, 1>(c); k1_issueW<1, 2>(c);
  AttB a;
  const int grp = w >> 2, wq = w & 3, pp = j;
  {
    const int wr4 = w >> 1, wc2 = w & 1;
#pragma unroll
    for (int s = 0; s < 4; ++s) c.xb[s] = (lds_cptr)lds + (wr4 * 2) * 4096 + 128 * l32 + 16 * ((2 * s + hi) ^ ((l32 >> 1) & 7));
    c.wb = (lds_cptr)lds + WRING + (2 * wc2) * 512 + wlane;
    f32x16 acc[2][2];
    acc[0][0] = f32x16{}; acc[0][1] = f32x16{}; acc[1][0] = f32x16{}; acc[1][1] = f32x16{};
    FragQ fa, fb;
    c.flagp = p.ws + WS_FLAG + (head * 8 + (lane & 7)) * 128; c.flagv = 0;
    QLoop<0>::run(c, acc, fa, fb);
    STAMP(p.ws, 0, 2);
    asm volatile("s_waitcnt lgkmcnt(0)\n\ts_barrier" ::: "memory");
    if (w == 0) {
      if (!__all(c.flagv == 1u)) {
        const __attribute__((address_space(1))) unsigned* fl = (const __attribute__((address_space(1))) unsigned*)c.flagp;
        for (unsigned spin = 0; spin < (1u << 20); ++spin) {
          const unsigned v = __hip_atomic_load(fl, __ATOMIC_RELAXED, __HIP_MEMORY_SCOPE_AGENT);
          if (__all(v == 1u)) break;
          __builtin_amdgcn_s_sleep(2);
        }
      }
      __builtin_amdgcn_fence(__ATOMIC_ACQUIRE, "wavefront");
    }
    const __amdgpu_buffer_rsrc_t sQLw = mksrd(p.ws + WS_QF + (size_t)b * 65536, 65536);
#pragma unroll
    for (int r = 0; r < 2; ++r)
#pragma unroll
      for (int ct = 0; ct < 2; ++ct)
#pragma unroll
        for (int g = 0; g < 2; ++g) {
          const int fi = (2 * wr4 + r) * 8 + 2 * (2 * wc2 + ct) + g;
          const bf16x8 v = pack8(acc[r][ct], g, CB);
          *(bf16x8*)(lds + fi * 1024 + lane * 16) = v;
        }
    asm volatile("s_waitcnt lgkmcnt(0)\n\ts_barrier" ::: "memory");
#pragma unroll
    for (int ds = 0; ds < 8; ++ds) a.qf[ds] = lds_rd16((lds_cptr)lds + ((grp * 4 + wq) * 8 + ds) * 1024 + lane * 16);
    STAMP(p.ws, 0, 3);
    if (w == 0) asm volatile("s_waitcnt vmcnt(0)" ::: "memory");
    asm volatile("s_waitcnt lgkmcnt(0)\n\ts_barrier" ::: "memory");
#pragma unroll
    for (int ds = 0; ds < 8; ++ds) bst16(a.qf[ds], sQLw, voff, (unsigned)(((grp * 4 + wq) * 8 + ds) * 1024));
    STAMP(p.ws, 0, 4);
  }
  const int L = 15 - pp, n1 = 2 * pp + 2;
  STAMP(p.ws, 0, 9);
  {
  AttEnv e; e.grp = grp; e.wq = wq; e.lane = lane; e.hi = hi; e.l32 = l32; e.n1 = n1; e.L = L; e.head = head; e.lds = lds; e.lds0 = lds0; e.ws = p.ws;
  e.sK = mksrd(p.ws + WS_KF + (size_t)head * S * 256, S * 256);
  e.sV = mksrd(p.ws + WS_VF + (size_t)head * S * 256, S * 256);
  e.sQ = mksrd(p.ws + WS_QF + (size_t)b * 65536, 65536);
  e.sC = mksrd(p.ws + WS_CF + (size_t)head * 8192, 64 * 256 * 1024 - head * 8192);
  int qb = grp == 0 ? L : pp;
  {
    const unsigned gb = grp * 81920;
#pragma unroll
    for (int t = 0; t < 2; ++t)
#pragma unroll
      for (int i = 0; i < 4; ++i) dma16s(rfl(lds0 + gb + t * 16384 + wq * 4096 + i * 1024), voff, e.sK, rfl((unsigned)(t * 16384 + wq * 4096 + i * 1024)));
  }
  {
    const int send0 = grp == 0 ? 17 : n1;
    const int rmin0 = qb * 128 + wq * 32;
    int nt0 = ((rmin0 + 31) >> 6) + 1; if (nt0 > send0) nt0 = send0;
    int nt1 = 0;
    if (grp == 1) { const int rminL = L * 128 + wq * 32; nt1 = ((rminL + 31) >> 6) - 17 + 1; if (nt1 > 17 - n1) nt1 = 17 - n1; if (nt1 < 0) nt1 = 0; }
    a.l = 0.f; a.ps = 0.f; a.m = -1e30f;
#pragma unroll
    for (int db = 0; db < 4; ++db) a.o[db] = f32x16{};
    int s = 0; bool carry = false; int qprev = qb;
    const int nseg = grp == 0 ? 1 : 2;
#pragma unroll 1
    for (int seg = 0; seg < nseg; ++seg) {
      const int nt = seg == 0 ? nt0 : nt1;
      const int send = seg == 0 ? send0 : 17;
      const bool shortblk = grp == 1 && seg == 0;
      if (nt > 0) {
        const int slast = s + nt;
        if (carry) att_step<M_SWITCH>(a, e, s, qprev, qb); else att_step<M_FIRST>(a, e, s, qb, qb, shortblk && nt == 1);
        for (++s; s < slast; ++s) att_step<M_STEADY>(a, e, s, qb, qb, shortblk && s == slast - 1);
        carry = shortblk && s == send && nt1 > 0;
        if (!carry) {
          att_step<M_DRAIN>(a, e, s, qb, qb); ++s;
          if (shortblk) {
            store_block(a, e, qb);
#pragma unroll
            for (int db = 0; db < 4; ++db) a.o[db] = f32x16{};
            a.l = 0.f; a.ps = 0.f; a.m = -1e30f;
          }
        }
      }
      for (; s < send; ++s) { StepCtx sc; step_top(e, s, sc); dma_all(e, sc); }
      qprev = qb; qb = L;
    }
    for (; s < 18; ++s) { StepCtx sc; step_top(e, s, sc); dma_all(e, sc); }
  }
  STAMP(p.ws, 0, 10);
  asm volatile("s_waitcnt vmcnt(0) lgkmcnt(0)\n\ts_barrier" ::: "memory");
  {
    const float lt = hsum(a.l);
    char* xb = lds + w * 8192;
    float* st = (float*)(lds + 65536) + w * 128;
#pragma unroll
    for (int i = 0; i < 2; ++i)
#pragma unroll
      for (int q = 0; q < 4; ++q) {
        const f32x16& o = grp == 0 ? a.o[2 + i] : a.o[i];
        *(f32x4*)(xb + (i * 4 + q) * 1024 + lane * 16) = f32x4{o[4 * q], o[4 * q + 1], o[4 * q + 2], o[4 * q + 3]};
      }
    st[lane] = a.m; st[64 + lane] = lt;
    __syncthreads();
    const int ow = w ^ 4;
    const char* yb = lds + ow * 8192; const float* so = (const float*)(lds + 65536) + ow * 128;
    const float m2 = so[lane], l2 = so[64 + lane];
    const float mn = fmaxf(a.m, m2), f1 = EXP2(a.m - mn), f2 = EXP2(m2 - mn);
    const float inv = 1.f / (lt * f1 + l2 * f2);
    const float g1 = f1 * inv, g2 = f2 * inv;
#pragma unroll
    for (int i = 0; i < 2; ++i) {
      f32x16 o = grp == 0 ? a.o[i] : a.o[2 + i];
#pragma unroll
      for (int q = 0; q < 4; ++q) { const f32x4 y = *(const f32x4*)(yb + (i * 4 + q) * 1024 + lane * 16);
#pragma unroll
        for (int e = 0; e < 4; ++e) o[4 * q + e] = o[4 * q + e] * g1 + y[e] * g2; }
      const int db = (grp == 0 ? 0 : 2) + i;
#pragma unroll
      for (int g = 0; g < 2; ++g)
        bst16(pack8n(o, g), e.sC, voff, rfl((unsigned)(((L * 4 + wq) * 256 + db * 2 + g) * 1024)));
    }
  }
  }
  STAMP(p.ws, 0, 11);
}

struct K3P { char* ws; const void* wo; const void* awo; void* out; };
constexpr int K3_WS = 24576;
struct K3C {
  unsigned lds0, w, xvoff, wvoff[3];
  __amdgpu_buffer_rsrc_t sX, sW;
  lds_cptr wb;
};
DI void xload(bf16x8& d, unsigned voff, __amdgpu_buffer_rsrc_t srd, unsigned soff) {
  asm volatile("buffer_load_dwordx4 %0, %1, %2, %3 offen" : "=v"(d) : "v"(voff), "s"(srd), "s"(soff) : "memory");
}
template <int I> DI void k3_dmaW(const K3C& c, int st) {
  dma16(c.lds0 + (st & 3) * K3_WS + (3 * c.w + I) * 1024, c.wvoff[I], c.sW, (unsigned)((st * 128 + c.w * 16) * 1152));
}
struct K3F { s16x4 wl[3], wh[3]; };
template <int KS> DI void k3_kstep(const K3C& c, f32x16 (&acc)[3], bf16x8& x, const K3F& cur, K3F& nxt, int st, int slot_next_off, unsigned xso) {
  constexpr int KN = KS ^ 1;
#pragma unroll
  for (int m = 0; m < 3; ++m) {
    acc[m] = MFMA(cat(cur.wl[m], cur.wh[m]), x, acc[m]);
    lds_cptr p = c.wb + slot_next_off + (2 * KN) * 1536 + m * 512;
    nxt.wl[m] = lds_tr(p); nxt.wh[m] = lds_tr(p + 1536);
    if (KS == 0 && m == 0) k3_dmaW<0>(c, st + 3);
    if (KS == 0 && m == 2) k3_dmaW<1>(c, st + 3);
    if (KS == 1 && m == 1) k3_dmaW<2>(c, st + 3);
    SBAR();
  }
  xload(x, c.xvoff, c.sX, xso + KS * 1024);
  SBAR();
}
__global__ __launch_bounds__(512, 2) void k3_out(K3P p) {
  extern __shared__ __attribute__((aligned(16))) char lds[];
  const int tid = threadIdx.x, lane = tid & 63, l32 = lane & 31, hi = lane >> 5;
  const int b = blockIdx.x;
  const bool isB = b < 192;
  K3C c;
  c.lds0 = (unsigned)(size_t)(lds_ptr)lds; c.w = rfl(tid >> 6);
  const int rt = c.w & 1, kq = c.w >> 1;
  const int blk = (lane >> 4) & 1, q4 = (lane & 15) >> 2, p4 = lane & 3;
  const int nst = isB ? 32 : 5;
  c.xvoff = lane * 16;
#pragma unroll
  for (int i = 0; i < 3; ++i) { const int sg = 2 * i + (lane >> 5), g = sg / 3, ct = sg - 3 * g, rr = (lane & 31) >> 2, c4 = lane & 3; c.wvoff[i] = (unsigned)((8 * g + rr) * 1152 + ct * 64 + c4 * 16); }
  c.wb = (lds_cptr)lds + (4 * kq) * 1536 + 32 * blk + 8 * p4 + (4 * hi + q4) * 64;
  STAMP(p.ws, 2, 0);
  const int ntile = isB ? 1 : 3;
#pragma unroll 1
  for (int it = 0; it < ntile; ++it) {
    const int t = isB ? ((b & 7) * 24 + (b >> 3)) : ((b - 192) * 3 + it);
    const int rb = t / 6, cb = t - rb * 6;
    c.sW = isB ? mksrd((const char*)p.wo + cb * 192, 4096 * 1152 - cb * 192) : mksrd((const char*)p.awo + cb * 192, 576 * 1152 - cb * 192);
    {
      const char* xb = isB ? p.ws + WS_CF + (size_t)(rb * 2 + rt) * 256 * 1024 : p.ws + WS_OA + (size_t)(rb * 2 + rt) * 36 * 1024;
      c.sX = mksrd(xb + kq * 2048, (isB ? 256 : 36) * 1024 - kq * 2048);
    }
    f32x16 acc[3];
#pragma unroll
    for (int ct = 0; ct < 3; ++ct) acc[ct] = f32x16{};
    bf16x8 xa[2], xb_[2];
    if (it) asm volatile("s_waitcnt lgkmcnt(0)\n\ts_barrier" ::: "memory");
    k3_dmaW<0>(c, 0); k3_dmaW<1>(c, 0); k3_dmaW<2>(c, 0);
    xload(xa[0], c.xvoff, c.sX, 0); xload(xa[1], c.xvoff, c.sX, 1024);
    k3_dmaW<0>(c, 1); k3_dmaW<1>(c, 1); k3_dmaW<2>(c, 1);
    xload(xb_[0], c.xvoff, c.sX, 8192); xload(xb_[1], c.xvoff, c.sX, 8192 + 1024);
    k3_dmaW<0>(c, 2); k3_dmaW<1>(c, 2); k3_dmaW<2>(c, 2);
    K3F fa, fb;
#define K3_WAIT(N, X) asm volatile("s_waitcnt vmcnt(" #N ")\n\ts_barrier" : "+v"(X[0]), "+v"(X[1]) :: "memory")
#define K3_STAGE(X, st) do { \
      const int so = ((st) & 3) * K3_WS, sn = (((st) + 1) & 3) * K3_WS; const unsigned xso = (unsigned)(((st) + 2) * 8192); \
      k3_kstep<0>(c, acc, X[0], fa, fb, st, so, xso); \
      k3_kstep<1>(c, acc, X[1], fb, fa, st, sn, xso); } while (0)
    K3_WAIT(5, xa); SBAR();
    STAMP(p.ws, 2, 1);
#pragma unroll
    for (int m = 0; m < 3; ++m) { fa.wl[m] = lds_tr(c.wb + m * 512); fa.wh[m] = lds_tr(c.wb + m * 512 + 1536); }
    SBAR();
    int st = 0;
    for (; st + 1 < nst; st += 2) {
      K3_STAGE(xa, st);
      K3_WAIT(5, xb_); SBAR();
      K3_STAGE(xb_, st + 1);
      K3_WAIT(5, xa); SBAR();
    }
    if (st < nst) K3_STAGE(xa, st);
    STAMP(p.ws, 2, 2);
    asm volatile("s_waitcnt vmcnt(0) lgkmcnt(0)\n\ts_barrier" : "+v"(xa[0]), "+v"(xa[1]), "+v"(xb_[0]), "+v"(xb_[1]) :: "memory");
#pragma unroll
    for (int ct = 0; ct < 3; ++ct)
#pragma unroll
      for (int g = 0; g < 4; ++g)
        *(f32x4*)(lds + ((c.w * 3 + ct) * 4 + g) * 1024 + lane * 16) = f32x4{acc[ct][4 * g], acc[ct][4 * g + 1], acc[ct][4 * g + 2], acc[ct][4 * g + 3]};
    __syncthreads();
#pragma unroll
    for (int i = 0; i < 3; ++i) {
      const int item = 3 * c.w + i, r2 = item / 12, ct = (item % 12) >> 2, g = item & 3;
      f32x4 sum = f32x4{0.f, 0.f, 0.f, 0.f};
#pragma unroll
      for (int k = 0; k < 4; ++k) sum += *(const f32x4*)(lds + (((2 * k + r2) * 3 + ct) * 4 + g) * 1024 + lane * 16);
      const int row = rb * 64 + r2 * 32 + l32, col = cb * 96 + ct * 32 + 8 * g + 4 * hi;
      *(u32x2*)((char*)p.out + (size_t)row * 2304 + (isB ? 1152 : 0) + col * 2) = u32x2{cvtpk(sum[0], sum[1]), cvtpk(sum[2], sum[3])};
    }
  }
  STAMP(p.ws, 2, 3);
}

extern "C" void kernel_launch(void* const* d_in, const int* in_sizes, int n_in, void* d_out, int out_size, void* d_ws, size_t ws_size, hipStream_t stream) {
  static bool attr = false;
  if (!attr) {
    hipFuncSetAttribute((const void*)k0_aproj, hipFuncAttributeMaxDynamicSharedMemorySize, 147456);
    hipFuncSetAttribute((const void*)kb_main, hipFuncAttributeMaxDynamicSharedMemorySize, 163840);
    hipFuncSetAttribute((const void*)k3_out, hipFuncAttributeMaxDynamicSharedMemorySize, 4 * K3_WS);
    attr = true;
  }
  K1P p1; p1.cx = d_in[6]; p1.bx = d_in[5]; p1.wuk = d_in[8]; p1.wuv = d_in[9]; p1.wq = d_in[7];
  p1.ax = d_in[0]; p1.awq = d_in[1]; p1.awk = d_in[2]; p1.awv = d_in[3]; p1.wo = d_in[10]; p1.awo = d_in[4]; p1.ws = (char*)d_ws;
  k0_aproj<<<256, 512, 147456, stream>>>(p1);
  kb_main<<<256, 512, 163840, stream>>>(p1);
  K3P p3; p3.ws = (char*)d_ws; p3.wo = d_in[10]; p3.awo = d_in[4]; p3.out = d_out;
  k3_out<<<256, 512, 4 * K3_WS, stream>>>(p3);
}
```

```cpp
#include <hip/hip_runtime.h>
#include <hip/hip_bf16.h>
#include <cstdio>
#include <cstdint>
#include <utility>

#define PUBMODE 0
#define KV_ACQ 0
#define BPRIO 1
#define BPRIO_G 1
#define WARM 1
#define ST_AUX 0
#define DI __device__ __forceinline__
typedef short bf16x8 __attribute__((ext_vector_type(8)));
typedef short s16x4 __attribute__((ext_vector_type(4)));
typedef float f32x16 __attribute__((ext_vector_type(16)));
typedef float f32x4 __attribute__((ext_vector_type(4)));
typedef float f32x2 __attribute__((ext_vector_type(2)));
typedef __bf16 bf16x2_t __attribute__((ext_vector_type(2)));
typedef unsigned u32x4 __attribute__((ext_vector_type(4)));
typedef unsigned u32x2 __attribute__((ext_vector_type(2)));
typedef __attribute__((address_space(3))) const char* lds_cptr;
typedef __attribute__((address_space(3))) char* lds_ptr;
#define SBAR() __builtin_amdgcn_sched_barrier(0)
#define PIN(x) asm volatile("" : "+v"(x))
#define MFMA(a, b, c) __builtin_amdgcn_mfma_f32_32x32x16_bf16(a, b, c, 0, 0, 0)
#define SRD_W3 0x00020000

DI unsigned rfl(unsigned x) { return __builtin_amdgcn_readfirstlane(x); }
DI unsigned cvtpk(float lo, float hi) { f32x2 v = {lo, hi}; bf16x2_t b = __builtin_convertvector(v, bf16x2_t); return __builtin_bit_cast(unsigned, b); }
DI void dma16(unsigned m0, unsigned voff, __amdgpu_buffer_rsrc_t srd, unsigned soff) {
  asm volatile("s_mov_b32 m0, %0\n\ts_nop 0\n\tbuffer_load_dwordx4 %1, %2, %3 offen lds" :: "s"(m0), "v"(voff), "s"(srd), "s"(soff) : "m0", "memory");
}
template <int OFF> DI void dma16o(unsigned m0, unsigned voff, __amdgpu_buffer_rsrc_t srd, unsigned soff) {
  asm volatile("s_mov_b32 m0, %0\n\ts_nop 0\n\tbuffer_load_dwordx4 %1, %2, %3 offen offset:%4 lds" :: "s"(m0), "v"(voff), "s"(srd), "s"(soff), "i"(OFF) : "m0", "memory");
}
DI void dma16s(unsigned m0, unsigned voff, __amdgpu_buffer_rsrc_t srd, unsigned soff) {
  asm volatile("s_mov_b32 m0, %0\n\ts_nop 0\n\tbuffer_load_dwordx4 %1, %2, %3 offen sc1 lds" :: "s"(m0), "v"(voff), "s"(srd), "s"(soff) : "m0", "memory");
}
template <int OFF> DI void dma16os(unsigned m0, unsigned voff, __amdgpu_buffer_rsrc_t srd, unsigned soff) {
  asm volatile("s_mov_b32 m0, %0\n\ts_nop 0\n\tbuffer_load_dwordx4 %1, %2, %3 offen offset:%4 sc1 lds" :: "s"(m0), "v"(voff), "s"(srd), "s"(soff), "i"(OFF) : "m0", "memory");
}
DI bf16x8 bld16s(__amdgpu_buffer_rsrc_t srd, unsigned voff, unsigned soff) { return __builtin_bit_cast(bf16x8, __builtin_amdgcn_raw_buffer_load_b128(srd, voff, soff, 16)); }
DI void dma4(unsigned m0, unsigned voff, __amdgpu_buffer_rsrc_t srd, unsigned soff) {
  asm volatile("s_mov_b32 m0, %0\n\ts_nop 0\n\tbuffer_load_dword %1, %2, %3 offen lds" :: "s"(m0), "v"(voff), "s"(srd), "s"(soff) : "m0", "memory");
}
DI __amdgpu_buffer_rsrc_t mksrd(const void* p, unsigned bytes) { return __builtin_amdgcn_make_buffer_rsrc((void*)p, (short)0, (int)bytes, SRD_W3); }
DI bf16x8 bld16(__amdgpu_buffer_rsrc_t srd, unsigned voff, unsigned soff) { return __builtin_bit_cast(bf16x8, __builtin_amdgcn_raw_buffer_load_b128(srd, voff, soff, 0)); }
DI void bst16(bf16x8 v, __amdgpu_buffer_rsrc_t srd, unsigned voff, unsigned soff) { __builtin_amdgcn_raw_buffer_store_b128(__builtin_bit_cast(u32x4, v), srd, voff + soff, 0, ST_AUX); }
template <int N> DI void wait_bar() { asm volatile("s_waitcnt vmcnt(%0)\n\ts_barrier" :: "i"(N) : "memory"); }
DI bf16x8 lds_rd16(lds_cptr p) { return *(const __attribute__((address_space(3))) bf16x8*)p; }
DI s16x4 lds_tr(lds_cptr p) { return __builtin_bit_cast(s16x4, __builtin_amdgcn_ds_read_tr16_b64_v4i16((__attribute__((address_space(3))) s16x4*)p)); }
DI bf16x8 cat(s16x4 a, s16x4 b) { return __builtin_shufflevector(a, b, 0, 1, 2, 3, 4, 5, 6, 7); }
DI bf16x8 pack8(const f32x16& x, int g, float sc) {
  u32x4 w;
#pragma unroll
  for (int e = 0; e < 4; ++e) w[e] = cvtpk(x[8 * g + 2 * e] * sc, x[8 * g + 2 * e + 1] * sc);
  return __builtin_bit_cast(bf16x8, w);
}
DI bf16x8 pack8n(const f32x16& x, int g) {
  u32x4 w;
#pragma unroll
  for (int e = 0; e < 4; ++e) w[e] = cvtpk(x[8 * g + 2 * e], x[8 * g + 2 * e + 1]);
  return __builtin_bit_cast(bf16x8, w);
}

#define DBG_S 1
#define STAMP(ws, k, i)
DI float hmax(float m) { auto rr = __builtin_amdgcn_permlane32_swap(__float_as_uint(m), __float_as_uint(m), false, false); return fmaxf(__uint_as_float(rr[0]), __uint_as_float(rr[1])); }
DI float hsum(float m) { auto rr = __builtin_amdgcn_permlane32_swap(__float_as_uint(m), __float_as_uint(m), false, false); return __uint_as_float(rr[0]) + __uint_as_float(rr[1]); }
constexpr int S = 2048, H = 32, DB = 128, MB = 576, DC = 512, DA = 576, DHA = 18;
constexpr float CB = 0.08838834764831845f * 1.4426950408889634f;
constexpr float CA = 0.23570226039551584f * 1.4426950408889634f;
constexpr size_t WS_KF = 0;
constexpr size_t WS_VF = WS_KF + (size_t)H * S * DB * 2;
constexpr size_t WS_QF = WS_VF + (size_t)H * S * DB * 2;
constexpr size_t WS_CF = WS_QF + (size_t)H * S * DB * 2;
constexpr size_t WS_QA = WS_CF + (size_t)H * S * DB * 2;
constexpr int AHB = 88064;
constexpr size_t WS_KA = WS_QA + (size_t)S * DA * 2 + 256;
constexpr size_t WS_KMAX = WS_KA + 2 * AHB;
constexpr size_t WS_OA = WS_KMAX + 1024;
constexpr size_t WS_PART = WS_OA + (size_t)S * DA * 2;
constexpr size_t WS_FLAG = WS_PART + (size_t)4 * S * 576 * 4;
constexpr size_t WS_CNT = WS_FLAG + 256 * 128 + 1024;
constexpr size_t WS_END = WS_CNT + 192 * 128;

struct K1P {
  const void *cx, *bx, *wuk, *wuv, *wq;
  const void *ax, *awq, *awk, *awv;
  const void *wo, *awo;
  char* ws;
};
constexpr int XSLOT = 32768, WRING = 98304, WSLOT = 16384;
template <int PART> constexpr int NSTEP = PART == 0 ? 16 : 18;
template <int PART> constexpr int NXT = PART == 0 ? 8 : 9;
template <int PART> constexpr int nWp(int s) { return s < NSTEP<PART> ? (PART == 0 ? 2 : 1) : 0; }
template <int PART> constexpr int nXp(int g) { return ((g >> 1) + 2) < NXT<PART> ? 2 : 0; }
template <int PART> constexpr int nD(int g) { return nWp<PART>(g + 3) + nXp<PART>(g); }
template <int PART> constexpr int waitN(int g) { return g == 0 ? nWp<PART>(2) : nD<PART>(g - 1); }

struct K1C {
  unsigned lds0, w;
  unsigned xoffC, xoffX;
  unsigned woff;
  __amdgpu_buffer_rsrc_t sC, sX, sUK, sUV, sWQ;
  lds_cptr xb[4];
  lds_cptr wb;
  unsigned rowL, rowS;
  const char* flagp; unsigned flagv;
};
DI void st16_sc1(bf16x8 v, unsigned voff, __amdgpu_buffer_rsrc_t srd, unsigned soff) {
  asm volatile("buffer_store_dwordx4 %0, %1, %2, %3 offen sc1\n\ts_nop 1" :: "v"(v), "v"(voff), "s"(srd), "s"(soff) : "memory");
}
template <int PART, int T, int i> DI void k1_x1(const K1C& c) {
  const unsigned m0 = c.lds0 + (T % 3) * XSLOT + i * 8192 + c.w * 1024;
  if constexpr (PART == 0) dma16(m0, c.xoffC, c.sC, (unsigned)(i * 64 * 1024 + T * 128));
  else                     dma16(m0, c.xoffX, c.sX, (i < 2 ? c.rowL : c.rowS) + (unsigned)((i & 1) * 64 * 1152 + T * 128));
}
template <int PART, int T> DI void k1_issueX(const K1C& c) { k1_x1<PART, T, 0>(c); k1_x1<PART, T, 1>(c); k1_x1<PART, T, 2>(c); k1_x1<PART, T, 3>(c); }
template <int PART, int G, int I> DI void k1_w1(const K1C& c) {
  const unsigned m0 = c.lds0 + WRING + (G & 3) * WSLOT + c.w * 1024;
  if constexpr (PART == 0) { if constexpr (I == 0) dma16(m0, c.woff, c.sUK, (unsigned)(G * 32 * 8192)); else dma16(m0 + 8192, c.woff, c.sUV, (unsigned)(G * 32 * 8192)); }
  else if constexpr (I == 0) dma16(m0, c.woff, c.sWQ, (unsigned)(G * 32 * 8192));
}
template <int PART, int G> DI void k1_issueW(const K1C& c) { k1_w1<PART, G, 0>(c); k1_w1<PART, G, 1>(c); }
struct FragKV { bf16x8 x[4]; s16x4 wl[2], wh[2]; };
struct FragQ { bf16x8 x[2]; s16x4 wl[2], wh[2]; };
template <int G, int SS, int I> DI void kv_ld(const K1C& c, FragKV& f) {
  constexpr int T = G >> 1;
  lds_cptr xp = c.xb[2 * (G & 1) + SS] + (T % 3) * XSLOT;
  lds_cptr wp = c.wb + (G & 3) * WSLOT + SS * 4096;
  if constexpr (I == 0) f.x[0] = lds_rd16(xp);
  else if constexpr (I == 1) f.wl[0] = lds_tr(wp);
  else if constexpr (I == 2) f.wh[0] = lds_tr(wp + 256);
  else if constexpr (I == 3) f.wl[1] = lds_tr(wp + 512);
  else if constexpr (I == 4) f.wh[1] = lds_tr(wp + 768);
  else f.x[I - 4] = lds_rd16(xp + (I - 4) * 4096);
}
template <int PART, int G, int I> DI void k1_dma_piece(const K1C& c) {
  constexpr int GW = G + 3, TX = (G >> 1) + 2;
  if constexpr (I < 2) { if constexpr (GW < NSTEP<PART>) k1_w1<PART, GW, I>(c); }
  else if constexpr (TX < NXT<PART>) k1_x1<PART, TX, 2 * (G & 1) + (I - 2)>(c);
}
template <bool ISV, int M> DI void kv_mfma(f32x16 (&acc)[4][2], const FragKV& f) {
  constexpr int r = M >> 1, ct = M & 1;
  const bf16x8 wf = cat(f.wl[ct], f.wh[ct]);
  acc[r][ct] = ISV ? MFMA(f.x[r], wf, acc[r][ct]) : MFMA(wf, f.x[r], acc[r][ct]);
}
template <bool ISV, int GN, int SN, int DMAG, bool LOAD, int M> DI void kv_gap(const K1C& c, f32x16 (&acc)[4][2], const FragKV& cur, FragKV& nxt) {
  kv_mfma<ISV, M>(acc, cur);
  if constexpr (LOAD) kv_ld<GN, SN, M>(c, nxt);
  if constexpr (DMAG >= 0 && (M & 1) && M < 8) k1_dma_piece<0, DMAG, (M >> 1)>(c);
  SBAR();
}
template <bool ISV, int GN, int SN, int DMAG, bool LOAD> DI void kv_phase(const K1C& c, f32x16 (&acc)[4][2], const FragKV& cur, FragKV& nxt) {
  kv_gap<ISV, GN, SN, DMAG, LOAD, 0>(c, acc, cur, nxt); kv_gap<ISV, GN, SN, DMAG, LOAD, 1>(c, acc, cur, nxt);
  kv_gap<ISV, GN, SN, DMAG, LOAD, 2>(c, acc, cur, nxt); kv_gap<ISV, GN, SN, DMAG, LOAD, 3>(c, acc, cur, nxt);
  kv_gap<ISV, GN, SN, DMAG, LOAD, 4>(c, acc, cur, nxt); kv_gap<ISV, GN, SN, DMAG, LOAD, 5>(c, acc, cur, nxt);
  kv_gap<ISV, GN, SN, DMAG, LOAD, 6>(c, acc, cur, nxt); kv_gap<ISV, GN, SN, DMAG, LOAD, 7>(c, acc, cur, nxt);
}
template <int G, bool ISV> DI void k1_step_kv(const K1C& c, f32x16 (&acc)[4][2], FragKV& fa, FragKV& fb) {
  wait_bar<waitN<0>(G)>(); SBAR();
  if constexpr (G == 0) {
    kv_ld<0, 0, 0>(c, fa); kv_ld<0, 0, 1>(c, fa); kv_ld<0, 0, 2>(c, fa); kv_ld<0, 0, 3>(c, fa);
    kv_ld<0, 0, 4>(c, fa); kv_ld<0, 0, 5>(c, fa); kv_ld<0, 0, 6>(c, fa); kv_ld<0, 0, 7>(c, fa); SBAR();
  }
  kv_phase<ISV, G, 1, G, true>(c, acc, fa, fb);
  kv_phase<ISV, G + 1, 0, -1, (G < 15)>(c, acc, fb, fa);
}
template <int G, bool ISV> struct KVLoop { static DI void run(const K1C& c, f32x16 (&acc)[4][2], FragKV& fa, FragKV& fb) { k1_step_kv<G, ISV>(c, acc, fa, fb); if constexpr (G < 15) KVLoop<G + 1, ISV>::run(c, acc, fa, fb); } };

template <int G, int SS, int I> DI void q_ld(const K1C& c, FragQ& f) {
  constexpr int T = G >> 1;
  lds_cptr xp = c.xb[2 * (G & 1) + SS] + (T % 3) * XSLOT;
  lds_cptr wp = c.wb + (G & 3) * WSLOT + SS * 4096;
  if constexpr (I == 0) f.x[0] = lds_rd16(xp);
  else if constexpr (I == 1) f.wl[0] = lds_tr(wp);
  else if constexpr (I == 2) f.wh[0] = lds_tr(wp + 256);
  else if constexpr (I == 3) f.x[1] = lds_rd16(xp + 4096);
  else if constexpr (I == 4) f.wl[1] = lds_tr(wp + 512);
  else f.wh[1] = lds_tr(wp + 768);
}
template <int M> DI void q_mfma(f32x16 (&acc)[2][2], const FragQ& f) {
  constexpr int r = M & 1, ct = M >> 1;
  acc[r][ct] = MFMA(cat(f.wl[ct], f.wh[ct]), f.x[r], acc[r][ct]);
}
template <int GN, int SN, int DMAG, bool LOAD> DI void q_phase(const K1C& c, f32x16 (&acc)[2][2], const FragQ& cur, FragQ& nxt) {
  q_mfma<0>(acc, cur); if constexpr (LOAD) { q_ld<GN, SN, 0>(c, nxt); q_ld<GN, SN, 1>(c, nxt); } SBAR();
  q_mfma<1>(acc, cur); if constexpr (LOAD) { q_ld<GN, SN, 2>(c, nxt); q_ld<GN, SN, 3>(c, nxt); } if constexpr (DMAG >= 0) k1_dma_piece<1, DMAG, 0>(c); SBAR();
  q_mfma<2>(acc, cur); if constexpr (LOAD) { q_ld<GN, SN, 4>(c, nxt); } if constexpr (DMAG >= 0) k1_dma_piece<1, DMAG, 2>(c); SBAR();
  q_mfma<3>(acc, cur); if constexpr (LOAD) { q_ld<GN, SN, 5>(c, nxt); } if constexpr (DMAG >= 0) k1_dma_piece<1, DMAG, 3>(c); SBAR();
}
template <int G> DI void k1_step_q(K1C& c, f32x16 (&acc)[2][2], FragQ& fa, FragQ& fb) {
  wait_bar<waitN<1>(G)>(); SBAR();
  if constexpr (G == 0) {
    q_ld<G, 0, 0>(c, fa); q_ld<G, 0, 1>(c, fa); q_ld<G, 0, 2>(c, fa); q_ld<G, 0, 3>(c, fa); q_ld<G, 0, 4>(c, fa); q_ld<G, 0, 5>(c, fa); SBAR();
  }
  q_phase<G, 1, G, true>(c, acc, fa, fb);
  if constexpr (G == 13) asm volatile("global_load_dword %0, %1, off sc1" : "=v"(c.flagv) : "v"(c.flagp) : "memory");
  if constexpr (G == 16) PIN(c.flagv);
  q_phase<G + 1, 0, -1, (G < 17)>(c, acc, fb, fa);
}
template <int G> struct QLoop { static DI void run(K1C& c, f32x16 (&acc)[2][2], FragQ& fa, FragQ& fb) { k1_step_q<G>(c, acc, fa, fb); if constexpr (G < 17) QLoop<G + 1>::run(c, acc, fa, fb); } };

constexpr int relK(int i) { return ((i >> 2) * 8 + ((i >> 1) & 1) * 2 + (i & 1)) * 1024; }
constexpr int relV(int i) { return (((i >> 2) >> 1) * 16 + (2 * ((i >> 2) & 1) + (i & 1)) * 4 + ((i >> 1) & 1)) * 1024; }
template <bool ISV> DI void k1_kv(K1C& c, bf16x8 (&kvp)[16]) {
  f32x16 acc[4][2];
#pragma unroll
  for (int r = 0; r < 4; ++r) { acc[r][0] = f32x16{}; acc[r][1] = f32x16{}; }
  FragKV fa, fb;
  KVLoop<0, ISV>::run(c, acc, fa, fb);
#pragma unroll
  for (int r = 0; r < 4; ++r)
#pragma unroll
    for (int ct = 0; ct < 2; ++ct)
#pragma unroll
      for (int g = 0; g < 2; ++g) kvp[r * 4 + ct * 2 + g] = pack8n(acc[r][ct], g);
  SBAR();
}

__global__ __launch_bounds__(512, 2) void k0_aproj(K1P p) {
  extern __shared__ __attribute__((aligned(16))) char lds[];
  const int tid = threadIdx.x, lane = tid & 63, l32 = lane & 31, hi = lane >> 5;
  const int b = blockIdx.x, xcd = b & 7, ii = b >> 3;
  struct { unsigned lds0; } c; c.lds0 = (unsigned)(size_t)(lds_ptr)lds;
  const int w = rfl(tid >> 6);
  const int blk = (lane >> 4) & 1, q4 = (lane & 15) >> 2, p4 = lane & 3;
  if (tid == 0) { *(unsigned*)(p.ws + WS_FLAG + b * 128) = 0u; if (b < 192) *(unsigned*)(p.ws + WS_CNT + b * 128) = 0u; }
  if (ii < 20) {
    const int rg = 4 * xcd + ii / 5, cg = ii % 5;
    const __amdgpu_buffer_rsrc_t sAX = mksrd((const char*)p.ax + (size_t)rg * 64 * 1152, 64 * 1152);
    const __amdgpu_buffer_rsrc_t sAQ = mksrd((const char*)p.awq + cg * 256, 576 * 1152 - cg * 256);
    constexpr int ASL = 73728, AWO = 24576;
    const unsigned xv = (unsigned)((lane >> 3) * 1152 + (((lane & 7) ^ ((4 * (w & 1) + (lane >> 4)) & 7)) * 16));
    const unsigned wv = (unsigned)(((lane & 31) >> 2) * 1152 + (32 * (lane >> 5) + 8 * (lane & 3)) * 2);
    auto issue = [&](int th) {
      const unsigned base = c.lds0 + (th & 1) * ASL;
#pragma unroll
      for (int kc = 0; kc < 3; ++kc) dma16(rfl(base + (kc * 8 + w) * 1024), xv, sAX, rfl((unsigned)(w * 8 * 1152 + (th * 3 + kc) * 128)));
#pragma unroll
      for (int i = 0; i < 6; ++i) {
        const int g = 3 * w + (i >> 1), pr = i & 1;
        dma16(rfl(base + AWO + (g * 2 + pr) * 1024), wv, sAQ, rfl((unsigned)((th * 192 + g * 8) * 1152 + pr * 128)));
      }
    };
    const int rt = w >> 2, ct = w & 3, tile = 4 * cg + ct;
    f32x16 acc = f32x16{};
    issue(0); issue(1);
    for (int th = 0; th < 3; ++th) {
      if (th < 2) asm volatile("s_waitcnt vmcnt(9) lgkmcnt(0)\n\ts_barrier" ::: "memory");
      else asm volatile("s_waitcnt vmcnt(0) lgkmcnt(0)\n\ts_barrier" ::: "memory");
      lds_cptr xp = (lds_cptr)lds + (th & 1) * ASL + rt * 4096 + 128 * l32;
      lds_cptr wp = (lds_cptr)lds + (th & 1) * ASL + AWO + ct * 512 + 2048 * hi + 64 * q4 + 32 * blk + 8 * p4;
      bf16x8 xf[4]; s16x4 wl[4], wh[4];
#define ALD(k) do { xf[(k) & 3] = lds_rd16(xp + ((k) >> 2) * 8192 + 16 * ((2 * ((k) & 3) + hi) ^ ((l32 >> 1) & 7))); wl[(k) & 3] = lds_tr(wp + (k) * 4096); wh[(k) & 3] = lds_tr(wp + (k) * 4096 + 256); } while (0)
      if (tile < 18) {
        ALD(0); ALD(1); ALD(2);
#pragma unroll
        for (int k = 0; k < 12; ++k) {
          if (k + 3 < 12) ALD(k + 3);
          acc = MFMA(cat(wl[k & 3], wh[k & 3]), xf[k & 3], acc);
          SBAR();
        }
      }
      if (th == 0) { asm volatile("s_waitcnt lgkmcnt(0)\n\ts_barrier" ::: "memory"); issue(2); }
    }
    const int row = (rg * 2 + rt) * 32 + l32;
    if (tile < 18) {
#pragma unroll
      for (int g = 0; g < 4; ++g) {
        u32x2 v = {cvtpk(acc[4 * g] * CA, acc[4 * g + 1] * CA), cvtpk(acc[4 * g + 2] * CA, acc[4 * g + 3] * CA)};
        *(u32x2*)(p.ws + WS_QA + (size_t)row * 1152 + (tile * 32 + 8 * g + 4 * hi) * 2) = v;
      }
    }
  } else if (ii < 28) {
    const int r32 = 8 * xcd + (ii - 20);
    const __amdgpu_buffer_rsrc_t sAX = mksrd((const char*)p.ax + (size_t)r32 * 32 * 1152, 32 * 1152);
    for (int pc = w; pc < 36; pc += 8) {
      const int kc = pc >> 2, rq = pc & 3, r = lane >> 3, row = 8 * rq + r, ch = (lane & 7) ^ ((row >> 1) & 7);
      dma16(rfl(c.lds0 + pc * 1024), (unsigned)(row * 1152 + ch * 16), sAX, rfl((unsigned)(kc * 128)));
    }
    {
      const unsigned* gk = (const unsigned*)p.awk; const unsigned* gv = (const unsigned*)p.awv;
      unsigned vk[11], vv[11];
#pragma unroll
      for (int it = 0; it < 11; ++it) { const int i = tid + it * 512; const int ic = i < 5184 ? i : 0; vk[it] = gk[ic]; vv[it] = gv[ic]; }
#pragma unroll
      for (int it = 0; it < 11; ++it) {
        const int i = tid + it * 512;
        if (i < 5184) { const int r = i / 9, d = i - r * 9; char* dst = lds + 36864 + (r >> 3) * 1024 + (r & 7) * 64 + d * 4; *(unsigned*)dst = vk[it]; *(unsigned*)(dst + 512) = vv[it]; }
      }
    }
    asm volatile("s_waitcnt vmcnt(0) lgkmcnt(0)\n\ts_barrier" ::: "memory");
    const int isv = w >> 2, kq = w & 3;
    f32x16 acc = f32x16{};
    lds_cptr xp = (lds_cptr)lds + 128 * l32;
    lds_cptr wp = (lds_cptr)lds + 36864 + isv * 512 + 1024 * hi + 64 * q4 + 32 * blk + 8 * p4;
#pragma unroll
    for (int k9 = 0; k9 < 9; ++k9) {
      const int k = kq * 9 + k9;
      const bf16x8 xf = lds_rd16(xp + (k >> 2) * 4096 + 16 * ((2 * (k & 3) + hi) ^ ((l32 >> 1) & 7)));
      const bf16x8 wf = cat(lds_tr(wp + k * 2048), lds_tr(wp + k * 2048 + 256));
      if (isv) acc = MFMA(xf, wf, acc); else acc = MFMA(wf, xf, acc);
    }
    asm volatile("s_waitcnt lgkmcnt(0)\n\ts_barrier" ::: "memory");
    float* red = (float*)lds + w * 1024;
#pragma unroll
    for (int q = 0; q < 4; ++q) *(f32x4*)(red + q * 256 + lane * 4) = f32x4{acc[4 * q], acc[4 * q + 1], acc[4 * q + 2], acc[4 * q + 3]};
    __syncthreads();
    if (kq == 0) {
#pragma unroll
      for (int o = 1; o < 4; ++o)
#pragma unroll
        for (int q = 0; q < 4; ++q) { const f32x4 y = *(const f32x4*)(red + o * 1024 + q * 256 + lane * 4); acc[4 * q] += y[0]; acc[4 * q + 1] += y[1]; acc[4 * q + 2] += y[2]; acc[4 * q + 3] += y[3]; }
      const int row = r32 * 32 + l32;
      char* hb = p.ws + WS_KA + (size_t)(r32 >> 5) * AHB; const int rl = r32 & 31;
      if (!isv) {
        char* k0 = hb + rl * 1024;
        const unsigned w0 = cvtpk(acc[0], acc[1]), w1 = cvtpk(acc[2], acc[3]), w2 = cvtpk(acc[4], acc[5]), w3 = cvtpk(acc[6], acc[7]), w4 = cvtpk(acc[8], acc[9]);
        *(u32x2*)(k0 + (l32) * 16 + 8 * hi) = u32x2{w0, w1};
        *(u32x2*)(k0 + (l32 + 32) * 16 + 8 * hi) = u32x2{w2, w3};
        if (hi == 0) *(u32x4*)(hb + 32768 + (size_t)(rl * 32 + l32) * 16) = u32x4{w4, 0x00003F80u, 0u, 0u};
        auto sq = [](unsigned x) { const float a = __uint_as_float(x << 16), bq = __uint_as_float(x & 0xFFFF0000u); return a * a + bq * bq; };
        float s2 = sq(w0) + sq(w1) + sq(w2) + sq(w3) + (hi == 0 ? sq(w4) : 0.f);
        s2 = hsum(s2);
#pragma unroll
        for (int o = 16; o >= 1; o >>= 1) s2 = fmaxf(s2, __shfl_xor(s2, o));
        if (lane == 0) *(float*)(p.ws + WS_KMAX + r32 * 4) = s2;
      } else {
#pragma unroll
        for (int g = 0; g < 2; ++g) {
          bf16x8 v = pack8n(acc, g);
          if (l32 == 18) v = bf16x8{0x3F80, 0x3F80, 0x3F80, 0x3F80, 0x3F80, 0x3F80, 0x3F80, 0x3F80};
          if (l32 < 19) *(bf16x8*)(hb + 49152 + ((size_t)((2 * rl + g) * 2 + hi) * 19 + l32) * 16) = v;
        }
      }
    }
  }

}

constexpr float LTHR = 7.9228162514264338e28f;
#define LTHR_V LTHR
struct AttB {
  f32x16 o[4]; bf16x8 qf[8]; f32x16 negm; float m, l;
  f32x16 s0, s1;
  unsigned pw[16];
  bf16x8 fr[4];
  float ps;
};
DI void cmask1(f32x16& s, int key0, int row) {
  const int d = row - key0;
#pragma unroll
  for (int r = 0; r < 16; ++r) { const int c = (r & 3) + 8 * (r >> 2); s[r] = (c > d) ? -INFINITY : s[r]; }
}
DI float max16(const f32x16& s) { float a = fmaxf(fmaxf(s[0], s[1]), s[2]);
#pragma unroll
  for (int r = 3; r < 15; r += 2) a = fmaxf(fmaxf(a, s[r]), s[r + 1]);
  return fmaxf(a, s[15]); }
DI f32x16 splat16(float x) { f32x16 v;
#pragma unroll
  for (int r = 0; r < 16; ++r) v[r] = x;
  return v; }
#define PKB(w) __builtin_bit_cast(bf16x8, w)
#define EXP2(x) __builtin_amdgcn_exp2f(x)
DI bf16x8 pfrag(const AttB& a, int ks) { u32x4 w = {a.pw[4 * ks], a.pw[4 * ks + 1], a.pw[4 * ks + 2], a.pw[4 * ks + 3]}; return __builtin_bit_cast(bf16x8, w); }

template <bool HASCUR> DI void rare_fix(AttB& a, __amdgpu_buffer_rsrc_t sK, __amdgpu_buffer_rsrc_t sQ, unsigned voff, unsigned kso, unsigned qso, int key0, int row, bool mask) {
  float tm = -INFINITY;
  f32x16 t;
#pragma unroll 1
  for (int kb = 0; kb < 2; ++kb) {
    t = f32x16{};
#pragma unroll 2
    for (int ds = 0; ds < 8; ++ds) t = MFMA(bld16s(sK, voff, kso + kb * 8192 + ds * 1024), bld16(sQ, voff, qso + ds * 1024), t);
    if (mask) cmask1(t, key0 + 32 * kb, row);
    tm = fmaxf(tm, max16(t));
  }
  tm = hmax(tm);
  const float mn = fmaxf(a.m, tm), dl = mn - a.m, f = EXP2(-dl);
#pragma unroll
  for (int db = 0; db < 4; ++db)
#pragma unroll
    for (int r = 0; r < 16; ++r) a.o[db][r] *= f;
  float ps = 0.f;
#pragma unroll 1
  for (int kb = 0; kb < 2; ++kb) {
    t = f32x16{};
#pragma unroll 2
    for (int ds = 0; ds < 8; ++ds) t = MFMA(bld16s(sK, voff, kso + kb * 8192 + ds * 1024), bld16(sQ, voff, qso + ds * 1024), t);
    if (mask) cmask1(t, key0 + 32 * kb, row);
#pragma unroll
    for (int r = 0; r < 16; ++r) { t[r] = EXP2(t[r] - mn); ps += t[r]; }
    if (kb == 0) {
#pragma unroll
      for (int k = 0; k < 8; ++k) a.pw[k] = cvtpk(t[2 * k], t[2 * k + 1]);
    } else {
#pragma unroll
      for (int k = 0; k < 8; ++k) a.pw[8 + k] = cvtpk(t[2 * k], t[2 * k + 1]);
    }
  }
  a.l *= f; a.ps = ps;
  if (HASCUR) {
#pragma unroll
    for (int r = 0; r < 16; ++r) a.s0[r] -= dl;
  }
  a.m = mn; a.negm = splat16(-mn); PIN(a.negm);
}

struct StepCtx {
  lds_cptr kp;
  lds_cptr kpn;
  lds_cptr vp;
  unsigned m0k, m0v, sok, sov; bool dk, dv;
  unsigned voff;
};
constexpr int M_STEADY = 0, M_FIRST = 1, M_SWITCH = 2, M_DRAIN = 3;
template <int I, int MODE> DI void gapA(AttB& a, const StepCtx& c, __amdgpu_buffer_rsrc_t sK, __amdgpu_buffer_rsrc_t sV) {
  constexpr bool PREV = MODE != M_FIRST, CUR = MODE != M_DRAIN, NEW = MODE == M_FIRST || MODE == M_SWITCH;
  constexpr int ds = I & 7;
  if constexpr (CUR) {
    if constexpr (I < 8) { if constexpr (ds == 0) a.s0 = NEW ? MFMA(a.fr[I & 3], a.qf[0], f32x16{}) : MFMA(a.fr[I & 3], a.qf[0], a.negm); else a.s0 = MFMA(a.fr[I & 3], a.qf[ds], a.s0); }
    else { if constexpr (ds == 0) a.s1 = NEW ? MFMA(a.fr[I & 3], a.qf[0], f32x16{}) : MFMA(a.fr[I & 3], a.qf[0], a.negm); else a.s1 = MFMA(a.fr[I & 3], a.qf[ds], a.s1); }
  }
  if constexpr (I + 3 < 16) { if constexpr (CUR) a.fr[(I + 3) & 3] = lds_rd16(c.kp + (I + 3) * 1024); }
  else if constexpr (PREV) a.fr[(I + 3) & 3] = lds_rd16(c.vp + (I + 3 - 16) * 1024);
  if constexpr (I < 8) {
    if constexpr (PREV) {
      a.s1[8 + I] = EXP2(a.s1[8 + I]);
      a.ps += a.s1[7 + I];
      if constexpr (I < 4) { a.pw[8 + I] = cvtpk(a.s1[2 * I], a.s1[2 * I + 1]); PIN(a.pw[8 + I]); }
      else if constexpr (I != 6) { constexpr int k = I == 7 ? 6 : I; a.pw[8 + k] = cvtpk(a.s1[2 * k], a.s1[2 * k + 1]); PIN(a.pw[8 + k]); }
    }
    if constexpr (I & 1) { if (MODE != M_DRAIN || c.dv) dma16os<(I >> 1) * 1024>(c.m0v, c.voff, sV, c.sov); }
  } else {
    constexpr int i = I - 8;
    if constexpr (MODE == M_STEADY) {
      a.s0[i] = EXP2(a.s0[i]);
      if constexpr (i >= 1) a.ps += a.s0[i - 1];
    }
    if constexpr (i & 1) { if (MODE != M_DRAIN || c.dk) dma16os<(i >> 1) * 1024>(c.m0k, c.voff, sK, c.sok); }
  }
  PIN(a.ps);
  SBAR();
}
template <int I, int MODE> DI void gapB(AttB& a, const StepCtx& c, float& mx0, float& mx1) {
  constexpr bool PREV = MODE != M_FIRST, NEW = MODE == M_FIRST || MODE == M_SWITCH;
  constexpr int ks = I >> 2, db = I & 3;
  if constexpr (PREV) {
    a.o[db] = MFMA(a.fr[I & 3], pfrag(a, ks), a.o[db]);
    if constexpr (I + 3 < 16) a.fr[(I + 3) & 3] = lds_rd16(c.vp + (I + 3) * 1024);
  }
  if constexpr (I + 3 >= 16) a.fr[(I + 3) & 3] = lds_rd16(c.kpn + (I + 3 - 16) * 1024);
  if constexpr (MODE == M_STEADY) {
    if constexpr (I < 8) {
      a.s0[8 + I] = EXP2(a.s0[8 + I]);
      a.ps += a.s0[7 + I];
      if constexpr (I >= 4) { constexpr int k = I - 4; a.pw[k] = cvtpk(a.s0[2 * k], a.s0[2 * k + 1]); PIN(a.pw[k]); }
    } else {
      constexpr int i = I - 8;
      a.s1[i] = EXP2(a.s1[i]);
      if constexpr (i == 0) a.ps += a.s0[15]; else a.ps += a.s1[i - 1];
      if constexpr (I < 12) { constexpr int k = I - 4; a.pw[k] = cvtpk(a.s0[2 * k], a.s0[2 * k + 1]); PIN(a.pw[k]); }
    }
  } else if constexpr (NEW) {
    if constexpr (I < 4) {
      constexpr int r = 4 * I;
      if constexpr (I == 0) { mx0 = fmaxf(fmaxf(a.s0[0], a.s0[1]), a.s0[2]); mx1 = fmaxf(fmaxf(a.s1[0], a.s1[1]), a.s1[2]); mx0 = fmaxf(mx0, a.s0[3]); mx1 = fmaxf(mx1, a.s1[3]); }
      else { mx0 = fmaxf(fmaxf(mx0, a.s0[r]), a.s0[r + 1]); mx1 = fmaxf(fmaxf(mx1, a.s1[r]), a.s1[r + 1]); mx0 = fmaxf(fmaxf(mx0, a.s0[r + 2]), a.s0[r + 3]); mx1 = fmaxf(fmaxf(mx1, a.s1[r + 2]), a.s1[r + 3]); }
      PIN(mx0); PIN(mx1);
    } else {
      if constexpr (I == 4) { mx0 = fmaxf(hmax(fmaxf(mx0, mx1)), -1e30f); a.m = mx0; a.ps = 0.f; }
      if constexpr (I < 12) {
        constexpr int i = 2 * (I - 4);
        a.s0[i] = EXP2(a.s0[i] - mx0); a.s0[i + 1] = EXP2(a.s0[i + 1] - mx0);
        if constexpr (i >= 2) { a.ps += a.s0[i - 2]; a.ps += a.s0[i - 1]; }
      } else {
        constexpr int i = 2 * (I - 12);
        if constexpr (I == 12) { a.ps += a.s0[14]; a.ps += a.s0[15]; }
        a.s1[i] = EXP2(a.s1[i] - mx0); a.s1[i + 1] = EXP2(a.s1[i + 1] - mx0);
        a.s1[8 + i] -= mx0; a.s1[9 + i] -= mx0;
        if constexpr (i >= 2) { a.ps += a.s1[i - 2]; a.ps += a.s1[i - 1]; }
      }
      if constexpr (I >= 8) { constexpr int k = I - 8; if constexpr (2 * k + 1 < 2 * (I - 4)) { a.pw[k] = cvtpk(a.s0[2 * k], a.s0[2 * k + 1]); PIN(a.pw[k]); } }
    }
  }
  PIN(a.ps);
  SBAR();
}
template <int O, int MODE, int... Is> DI void phaseA_(AttB& a, const StepCtx& c, __amdgpu_buffer_rsrc_t sK, __amdgpu_buffer_rsrc_t sV, std::integer_sequence<int, Is...>) { (gapA<O + Is, MODE>(a, c, sK, sV), ...); }
template <int MODE, int... Is> DI void phaseB_(AttB& a, const StepCtx& c, float& mx0, float& mx1, std::integer_sequence<int, Is...>) { (gapB<Is, MODE>(a, c, mx0, mx1), ...); }

struct K2P { char* ws; };
struct AttEnv {
  int grp, wq, lane, hi, l32, n1, L, head; char* lds; unsigned lds0; char* ws;
  __amdgpu_buffer_rsrc_t sK, sV, sQ, sC;
};
DI int tile_of(const AttEnv& e, int x) { return e.grp == 0 ? x : (x < e.n1 ? x : 17 + (x - e.n1)); }
DI void step_top(const AttEnv& e, int s, StepCtx& c) {
  asm volatile("s_waitcnt vmcnt(0)\n\ts_barrier" ::: "memory");
  SBAR();
  const unsigned gb = e.grp * 81920;
  const int k3 = s % 3, k3n = (s + 1) % 3, k3d = (s + 2) % 3;
  c.kp = (lds_cptr)e.lds + gb + k3 * 16384 + e.lane * 16;
  c.kpn = (lds_cptr)e.lds + gb + k3n * 16384 + e.lane * 16;
  c.vp = (lds_cptr)e.lds + gb + 49152 + ((s + 1) & 1) * 16384 + e.lane * 16;
  c.dk = (s + 2 < 17); c.dv = (s < 17);
  c.m0k = rfl(e.lds0 + gb + k3d * 16384 + e.wq * 4096); c.m0v = rfl(e.lds0 + gb + 49152 + (s & 1) * 16384 + e.wq * 4096);
  c.sok = rfl((unsigned)(tile_of(e, s + 2) * 16384 + e.wq * 4096)); c.sov = rfl((unsigned)(tile_of(e, s) * 16384 + e.wq * 4096));
  c.voff = e.lane * 16;
}
DI void dma_all(const AttEnv& e, const StepCtx& c) {
  if (c.dk) {
#pragma unroll
    for (int i = 0; i < 4; ++i) dma16s(c.m0k + i * 1024, c.voff, e.sK, c.sok + i * 1024); }
  if (c.dv) {
#pragma unroll
    for (int i = 0; i < 4; ++i) dma16s(c.m0v + i * 1024, c.voff, e.sV, c.sov + i * 1024); }
}
template <bool HASCUR> DI void check_prev(AttB& a, const AttEnv& e, int qb, int prev_t) {
  if (__builtin_expect(__any(!(a.l + a.ps <= LTHR_V)), 0)) {
    const int rmin = qb * 128 + e.wq * 32;
    rare_fix<HASCUR>(a, e.sK, e.sQ, e.lane * 16, rfl((unsigned)(prev_t * 16384)), rfl((unsigned)(((qb == e.L ? 0 : 4) + e.wq) * 8192)),
                     prev_t * 64 + 4 * e.hi, rmin + e.l32, prev_t * 64 + 63 > rmin);
  }
  a.l += a.ps; a.ps = 0.f;
}
DI void store_block(AttB& a, const AttEnv& e, int qb) {
  const float inv = 1.f / hsum(a.l);
#pragma unroll
  for (int db = 0; db < 4; ++db)
#pragma unroll
    for (int g = 0; g < 2; ++g)
      bst16(pack8(a.o[db], g, inv), e.sC, e.lane * 16, rfl((unsigned)(((qb * 4 + e.wq) * 256 + db * 2 + g) * 1024)));
}
template <int MODE> DI void att_step(AttB& a, const AttEnv& e, int s, int qb_prev, int qb, bool loadq = false) {
  constexpr bool PREV = MODE != M_FIRST, CUR = MODE != M_DRAIN, NEW = MODE == M_FIRST || MODE == M_SWITCH;
  StepCtx c; step_top(e, s, c);
  if constexpr (NEW) asm volatile("" : "+v"(a.qf[0]), "+v"(a.qf[1]), "+v"(a.qf[2]), "+v"(a.qf[3]), "+v"(a.qf[4]), "+v"(a.qf[5]), "+v"(a.qf[6]), "+v"(a.qf[7]));
  const int t = tile_of(e, s), rmin = qb * 128 + e.wq * 32;
  const bool mask = t * 64 + 63 > rmin;
  if constexpr (MODE == M_FIRST) {
#pragma unroll
    for (int J = 0; J < 3; ++J) a.fr[J] = lds_rd16(c.kp + J * 1024);
  }
  phaseA_<0, MODE>(a, c, e.sK, e.sV, std::make_integer_sequence<int, 8>{});
  if constexpr (PREV) {
    a.ps += a.s1[15]; a.pw[15] = cvtpk(a.s1[14], a.s1[15]);
    check_prev<MODE == M_STEADY>(a, e, qb_prev, tile_of(e, s - 1));
  }
  if constexpr (MODE == M_STEADY) { if (mask) cmask1(a.s0, t * 64 + 4 * e.hi, rmin + e.l32); }
  SBAR();
  phaseA_<8, MODE>(a, c, e.sK, e.sV, std::make_integer_sequence<int, 8>{});
  if constexpr (CUR) {
    if (mask) { if constexpr (NEW) cmask1(a.s0, t * 64 + 4 * e.hi, rmin + e.l32); cmask1(a.s1, t * 64 + 32 + 4 * e.hi, rmin + e.l32); }
  }
  if constexpr (MODE == M_FIRST || MODE == M_STEADY) {
    if (loadq) {
#pragma unroll
      for (int ds = 0; ds < 8; ++ds)
        asm volatile("buffer_load_dwordx4 %0, %1, %2, %3 offen" : "=v"(a.qf[ds]) : "v"(e.lane * 16), "s"(e.sQ), "s"(rfl((unsigned)(e.wq * 8192 + ds * 1024))) : "memory");
    }
  }
  SBAR();
  float mx0 = 0.f, mx1 = 0.f; const float lold = a.l;
  phaseB_<MODE>(a, c, mx0, mx1, std::make_integer_sequence<int, 16>{});
  if constexpr (MODE == M_FIRST || MODE == M_STEADY) {
    if (loadq) asm volatile("s_waitcnt vmcnt(0)" : "+v"(a.qf[0]), "+v"(a.qf[1]), "+v"(a.qf[2]), "+v"(a.qf[3]), "+v"(a.qf[4]), "+v"(a.qf[5]), "+v"(a.qf[6]), "+v"(a.qf[7]) :: "memory");
  }
  if constexpr (NEW) {
    a.ps += a.s1[6];
    if constexpr (MODE == M_SWITCH) { a.l = lold; store_block(a, e, qb_prev); }
#pragma unroll
    for (int db = 0; db < 4; ++db) a.o[db] = f32x16{};
    a.l = 0.f; a.negm = splat16(-a.m); PIN(a.negm);
  }
}

struct AttA { f32x16 o; bf16x8 qf0, qf1; };
constexpr int AH_L = 65536;
#define E4(sc, i) do { sc[i] = EXP2(sc[i]); sc[i + 1] = EXP2(sc[i + 1]); sc[i + 2] = EXP2(sc[i + 2]); sc[i + 3] = EXP2(sc[i + 3]); } while (0)
#define C2(pw, j, sc, i) do { pw[j] = cvtpk(sc[i], sc[i + 1]); pw[j + 1] = cvtpk(sc[i + 2], sc[i + 3]); } while (0)
struct KVSt { __amdgpu_buffer_rsrc_t srd; unsigned voff; bool isv; };
template <bool ST> DI void a_half(AttA& A, char* lds, int lane, const bf16x8 (&kvp)[16], const KVSt& ks) {
  const int l32 = lane & 31, hi = lane >> 5;
  const int vrow = l32 < 19 ? l32 : (((l32 >= 20 && l32 <= 27)) ? 0 : 4);
  lds_cptr k0p = (lds_cptr)lds + AH_L + lane * 16;
  lds_cptr k1p = (lds_cptr)lds + AH_L + 32768 + l32 * 16;
  lds_cptr vp = (lds_cptr)lds + AH_L + 49152 + (hi * 19 + vrow) * 16;
  f32x16 sA, sB; u32x4 pa0, pa1, pb0, pb1;
  bf16x8 k0, k1, v0, v1, w0, w1;
#define LDK(kt) do { k0 = lds_rd16(k0p + (kt) * 1024); k1 = lds_rd16(k1p + (kt) * 512); } while (0)
#define ASTEP(SC, SN, PP0, PP1, PN0, PN1, kt, VC0, VC1, VN0, VN1, DOQK, DOPV, DOLDK) do { \
    if (DOQK) { SN = MFMA(k0, A.qf0, f32x16{}); } SBAR(); VN0 = lds_rd16(vp + (2 * (kt)) * 608); VN1 = lds_rd16(vp + (2 * (kt) + 1) * 608); E4(SC, 0); PIN(SC); SBAR(); \
    if (DOPV) { A.o = MFMA(VC0, PKB(PP0), A.o); } SBAR(); E4(SC, 4); C2(PN0, 0, SC, 0); PIN(SC); PIN(PN0); SBAR(); \
    if (DOQK) { SN = MFMA(k1, A.qf1, SN); } SBAR(); if (DOLDK) LDK((kt) + 2); E4(SC, 8); C2(PN0, 2, SC, 4); PIN(SC); PIN(PN0); SBAR(); \
    if (DOPV) { A.o = MFMA(VC1, PKB(PP1), A.o); } SBAR(); E4(SC, 12); C2(PN1, 0, SC, 8); C2(PN1, 2, SC, 12); PIN(SC); PIN(PN1); SBAR(); } while (0)
  pa0 = u32x4{0,0,0,0}; pa1 = pa0; pb0 = pa0; pb1 = pa0; v0 = bf16x8{}; v1 = v0; w0 = v0; w1 = v0;
  LDK(0); sA = MFMA(k0, A.qf0, f32x16{}); sA = MFMA(k1, A.qf1, sA); LDK(1);
  ASTEP(sA, sB, pb0, pb1, pa0, pa1, 0, v0, v1, w0, w1, true, false, true);
#define KVST(i) do { if (ST) { st16_sc1(kvp[i], ks.voff, ks.srd, ks.isv ? (unsigned)relV(i) : (unsigned)relK(i)); SBAR(); } } while (0)
  if constexpr (ST) {
    KVST(0);
#define APAIR(kt, i) ASTEP(sB, sA, pa0, pa1, pb0, pb1, kt, w0, w1, v0, v1, true, true, true); KVST(i); ASTEP(sA, sB, pb0, pb1, pa0, pa1, (kt) + 1, v0, v1, w0, w1, true, true, true)
    APAIR(1, 1); APAIR(3, 2); APAIR(5, 3); APAIR(7, 4); APAIR(9, 5); APAIR(11, 6); APAIR(13, 7); APAIR(15, 8); APAIR(17, 9); APAIR(19, 10); APAIR(21, 11); APAIR(23, 12); APAIR(25, 13); APAIR(27, 14);
    KVST(15);
#undef APAIR
  } else {
    for (int kt = 1; kt < 29; kt += 2) {
      ASTEP(sB, sA, pa0, pa1, pb0, pb1, kt, w0, w1, v0, v1, true, true, true);
      ASTEP(sA, sB, pb0, pb1, pa0, pa1, kt + 1, v0, v1, w0, w1, true, true, true);
    }
  }
  ASTEP(sB, sA, pa0, pa1, pb0, pb1, 29, w0, w1, v0, v1, true, true, true);
  ASTEP(sA, sB, pb0, pb1, pa0, pa1, 30, v0, v1, w0, w1, true, true, false);
  ASTEP(sB, sA, pa0, pa1, pb0, pb1, 31, w0, w1, v0, v1, false, true, false);
  A.o = MFMA(v0, PKB(pb0), A.o); A.o = MFMA(v1, PKB(pb1), A.o);
#undef LDK
#undef ASTEP
#undef KVST
}
DI void a_dma(unsigned lds0, int w, unsigned voff, char* ws, int hf) {
  const __amdgpu_buffer_rsrc_t sr = mksrd(ws + WS_KA + (size_t)hf * AHB, AHB);
#pragma unroll
  for (int i = 0; i < 11; ++i) { const int pc = w + 8 * i; dma16(rfl(lds0 + AH_L + (pc < 86 ? pc : 85) * 1024), voff, sr, rfl((unsigned)((pc < 86 ? pc : 85) * 1024))); }
}
DI void a_finish(AttA& A, char* ws, int lane, int qrow, int ah) {
  const int l32 = lane & 31, hi = lane >> 5;
  f32x16 o = A.o;
  float l = o[10];
  { auto rr = __builtin_amdgcn_permlane32_swap(__float_as_uint(l), __float_as_uint(l), false, false); l = __uint_as_float(rr[0]); }
  if (__builtin_expect(__any(!(l >= 8.6736174e-19f)), 0)) {
    float m = -1e30f; o = f32x16{};
    bf16x8 qz = A.qf1; qz[2] = 0;
    const int vrow = l32 < 19 ? l32 : 0;
    for (int kt = 0; kt < 64; ++kt) {
      const char* hb = ws + WS_KA + (size_t)(kt >> 5) * AHB; const int kl = kt & 31;
      const bf16x8 k0 = *(const bf16x8*)(hb + kl * 1024 + lane * 16), k1 = *(const bf16x8*)(hb + 32768 + (kl * 32 + l32) * 16);
      const bf16x8 v0 = *(const bf16x8*)(hb + 49152 + ((2 * kl) * 38 + hi * 19 + vrow) * 16), v1 = *(const bf16x8*)(hb + 49152 + ((2 * kl + 1) * 38 + hi * 19 + vrow) * 16);
      f32x16 sc = MFMA(k0, A.qf0, f32x16{});
      sc = MFMA(k1, qz, sc);
      const float mn = fmaxf(m, hmax(max16(sc))), f = EXP2(m - mn);
      m = mn;
#pragma unroll
      for (int r = 0; r < 16; ++r) { sc[r] = EXP2(sc[r] - mn); o[r] *= f; }
      o = MFMA(v0, pack8n(sc, 0), o);
      o = MFMA(v1, pack8n(sc, 1), o);
    }
    l = o[10];
    auto rr = __builtin_amdgcn_permlane32_swap(__float_as_uint(l), __float_as_uint(l), false, false); l = __uint_as_float(rr[0]);
  }
  const float inv = 1.f / l;
  char* ob = ws + WS_OA + (size_t)(qrow >> 5) * 36 * 1024 + l32 * 16;
  auto st2 = [&](int d, float x, float y) { const int c = ah * 18 + d; *(unsigned*)(ob + (c >> 4) * 1024 + ((c >> 2) & 1) * 512 + (((c >> 3) & 1) * 4 + (c & 3)) * 2) = cvtpk(x * inv, y * inv); };
  st2(4 * hi, o[0], o[1]); st2(4 * hi + 2, o[2], o[3]); st2(8 + 4 * hi, o[4], o[5]); st2(10 + 4 * hi, o[6], o[7]);
  if (hi == 0) st2(16, o[8], o[9]);
}

__global__ __launch_bounds__(512, 2) void kb_main(K1P p) {
  extern __shared__ __attribute__((aligned(16))) char lds[];
  const int tid = threadIdx.x, lane = tid & 63, l32 = lane & 31, hi = lane >> 5;
  const int b = blockIdx.x, xcd = b & 7, ii = b >> 3, head = xcd * 4 + (ii & 3), j = ii >> 2;
  K1C c;
  c.lds0 = (unsigned)(size_t)(lds_ptr)lds; c.w = rfl(tid >> 6);
  const int w = c.w;
  {
    const int r = lane >> 3, chp = lane & 7, row = 8 * w + r, ch = chp ^ ((4 * (w & 1) + (r >> 1)) & 7);
    c.xoffC = (unsigned)(row * 1024 + ch * 16); c.xoffX = (unsigned)(row * 1152 + ch * 16);
    const int sub = lane >> 5, rr = (lane & 31) >> 2, c4 = lane & 3;
    c.woff = (unsigned)((8 * (w >> 1) + rr) * 8192 + (32 * (2 * (w & 1) + sub) + 8 * c4) * 2);
  }
  c.sC = mksrd((const char*)p.cx + (size_t)j * 256 * 1024, 256 * 1024);
  c.sX = mksrd((const char*)p.bx, 2048 * 1152);
  c.rowL = (unsigned)((15 - j) * 128 * 1152); c.rowS = (unsigned)(j * 128 * 1152);
  c.sUK = mksrd((const char*)p.wuk + head * 256, 512 * 8192 - head * 256);
  c.sUV = mksrd((const char*)p.wuv + head * 256, 512 * 8192 - head * 256);
  c.sWQ = mksrd((const char*)p.wq + head * 256, 576 * 8192 - head * 256);
  const int blk = (lane >> 4) & 1, q4 = (lane & 15) >> 2, p4 = lane & 3;
  const unsigned wlane = 2048 * hi + 64 * q4 + 32 * blk + 8 * p4;
  {
    const int wr = w >> 2, wc = w & 3;
#pragma unroll
    for (int s = 0; s < 4; ++s) c.xb[s] = (lds_cptr)lds + (wr * 4) * 4096 + 128 * l32 + 16 * ((2 * s + hi) ^ ((l32 >> 1) & 7));
    c.wb = (lds_cptr)lds + WRING + (wc >> 1) * 8192 + (2 * (wc & 1)) * 512 + wlane;
  }
  STAMP(p.ws, 0, 0);
  const unsigned lds0 = c.lds0, voff = lane * 16;
  AttA A;
  const int aqrow = j * 256 + w * 32 + l32;
  unsigned wv[7] = {0, 0, 0, 0, 0, 0, 0};
  {
    a_dma(lds0, w, voff, p.ws, 0);
    const char* qp = p.ws + WS_QA + (size_t)aqrow * 1152 + head * 36;
    u32x4 q0; { const unsigned* qq = (const unsigned*)(qp + 16 * hi); q0 = u32x4{qq[0], qq[1], qq[2], qq[3]}; }
    const unsigned q16 = *(const unsigned*)(qp + 32);
    float km = *(const float*)(p.ws + WS_KMAX + lane * 4);
#pragma unroll
    for (int o = 32; o >= 1; o >>= 1) km = fmaxf(km, __shfl_xor(km, o));
    float qs = 0.f;
#pragma unroll
    for (int e = 0; e < 4; ++e) { const float x = __uint_as_float(q0[e] << 16), y = __uint_as_float(q0[e] & 0xFFFF0000u); qs += x * x + y * y; }
    if (hi == 0) { const float x = __uint_as_float(q16 << 16), y = __uint_as_float(q16 & 0xFFFF0000u); qs += x * x + y * y; }
    qs = hsum(qs);
    const float bound = sqrtf(qs * km) * 1.0009765625f + 1e-30f;
    const unsigned bb = (__float_as_uint(bound) + 0xFFFFu) >> 16;
    A.qf0 = __builtin_bit_cast(bf16x8, q0);
    A.qf1 = __builtin_bit_cast(bf16x8, hi == 0 ? u32x4{q16, bb | 0x8000u, 0u, 0u} : u32x4{0u, 0u, 0u, 0u});
    A.o = f32x16{};
    PIN(A.qf0); PIN(A.qf1);
    k1_issueX<0, 0>(c); k1_issueX<0, 1>(c);
    asm volatile("s_waitcnt vmcnt(8)\n\ts_barrier" ::: "memory");
    STAMP(p.ws, 0, 6);
    if (WARM) {
      const char* src[7] = {(const char*)p.cx, (const char*)p.wuk, (const char*)p.wuv, (const char*)p.bx, (const char*)p.wq, (const char*)p.wo, (const char*)p.awo};
      const unsigned nline[7] = {2048u * 1024 / 128, 512u * 8192 / 128, 512u * 8192 / 128, 2048u * 1152 / 128, 576u * 8192 / 128, 4096u * 1152 / 128, 576u * 1152 / 128};
#pragma unroll
      for (int a7 = 0; a7 < 7; ++a7) {
        const unsigned i = (unsigned)(b * 192 + tid);
        if (tid < 192 && i < nline[a7]) wv[a7] = *(const unsigned*)(src[a7] + (size_t)i * 128);
      }
    }
    unsigned wv7 = 0;
    if (WARM && tid >= 192 && tid < 192 + 22) { const unsigned i = (unsigned)(ii * 22 + tid - 192); if (i < 688) wv7 = *(const unsigned*)(p.ws + WS_KA + AHB + (size_t)i * 128); }
    { bf16x8 dummy[16]; KVSt ks0; a_half<false>(A, lds, lane, dummy, ks0); }
    wv[0] ^= wv7;
    if ((wv[0] ^ wv[1] ^ wv[2] ^ wv[3] ^ wv[4] ^ wv[5] ^ wv[6]) == 0x9E3779B9u) *(unsigned*)(p.ws + WS_FLAG + 256 * 128 + 64) = 1u;
    STAMP(p.ws, 0, 7);
  }
  asm volatile("s_waitcnt lgkmcnt(0)\n\ts_barrier" ::: "memory");
  k1_issueW<0, 0>(c); k1_issueW<0, 1>(c); k1_issueW<0, 2>(c);
  {
    bf16x8 kvp[16];
    if ((w & 2) == 0) k1_kv<false>(c, kvp); else k1_kv<true>(c, kvp);
    STAMP(p.ws, 0, 1);
    asm volatile("s_waitcnt lgkmcnt(0)\n\ts_barrier" ::: "memory");
    a_dma(lds0, w, voff, p.ws, 1);
    k1_issueX<1, 0>(c); k1_issueX<1, 1>(c);
    const int wr = w >> 2, wc1 = w & 1;
    KVSt ks; ks.isv = (w & 2) != 0; ks.voff = voff;
    const size_t base = ks.isv ? WS_VF + ((size_t)(head * 32 + 4 * j + 2 * wr) * 16 + 2 * wc1) * 1024 : WS_KF + ((size_t)(head * 64 + 8 * j + 4 * wr) * 8 + 4 * wc1) * 1024;
    ks.srd = mksrd(p.ws + base, 65536);
    asm volatile("s_waitcnt vmcnt(8)\n\ts_barrier" ::: "memory");
    STAMP(p.ws, 0, 8);
    a_half<true>(A, lds, lane, kvp, ks);
  }
  a_finish(A, p.ws, lane, aqrow, head);
  STAMP(p.ws, 0, 5);
  asm volatile("s_waitcnt vmcnt(0) lgkmcnt(0)\n\ts_barrier" ::: "memory");
  if (tid == 0) __hip_atomic_store((__attribute__((address_space(1))) unsigned*)(p.ws + WS_FLAG + (head * 8 + j) * 128), 1u, __ATOMIC_RELAXED, __HIP_MEMORY_SCOPE_AGENT);
  k1_issueW<1, 0>(c); k1_issueW<1, 1>(c); k1_issueW<1, 2>(c);
  AttB a;
  const int grp = w >> 2, wq = w & 3, pp = j;
  {
    const int wr4 = w >> 1, wc2 = w & 1;
#pragma unroll
    for (int s = 0; s < 4; ++s) c.xb[s] = (lds_cptr)lds + (wr4 * 2) * 4096 + 128 * l32 + 16 * ((2 * s + hi) ^ ((l32 >> 1) & 7));
    c.wb = (lds_cptr)lds + WRING + (2 * wc2) * 512 + wlane;
    f32x16 acc[2][2];
    acc[0][0] = f32x16{}; acc[0][1] = f32x16{}; acc[1][0] = f32x16{}; acc[1][1] = f32x16{};
    FragQ fa, fb;
    c.flagp = p.ws + WS_FLAG + (head * 8 + (lane & 7)) * 128; c.flagv = 0;
    QLoop<0>::run(c, acc, fa, fb);
    STAMP(p.ws, 0, 2);
    asm volatile("s_waitcnt lgkmcnt(0)\n\ts_barrier" ::: "memory");
    if (w == 0) {
      if (!__all(c.flagv == 1u)) {
        const __attribute__((address_space(1))) unsigned* fl = (const __attribute__((address_space(1))) unsigned*)c.flagp;
        for (unsigned spin = 0; spin < (1u << 20); ++spin) {
          const unsigned v = __hip_atomic_load(fl, __ATOMIC_RELAXED, __HIP_MEMORY_SCOPE_AGENT);
          if (__all(v == 1u)) break;
          __builtin_amdgcn_s_sleep(2);
        }
      }
      __builtin_amdgcn_fence(__ATOMIC_ACQUIRE, "wavefront");
    }
    const __amdgpu_buffer_rsrc_t sQLw = mksrd(p.ws + WS_QF + (size_t)b * 65536, 65536);
#pragma unroll
    for (int r = 0; r < 2; ++r)
#pragma unroll
      for (int ct = 0; ct < 2; ++ct)
#pragma unroll
        for (int g = 0; g < 2; ++g) {
          const int fi = (2 * wr4 + r) * 8 + 2 * (2 * wc2 + ct) + g;
          const bf16x8 v = pack8(acc[r][ct], g, CB);
          *(bf16x8*)(lds + fi * 1024 + lane * 16) = v;
        }
    asm volatile("s_waitcnt lgkmcnt(0)\n\ts_barrier" ::: "memory");
#pragma unroll
    for (int ds = 0; ds < 8; ++ds) a.qf[ds] = lds_rd16((lds_cptr)lds + ((grp * 4 + wq) * 8 + ds) * 1024 + lane * 16);
    STAMP(p.ws, 0, 3);
    if (w == 0) asm volatile("s_waitcnt vmcnt(0)" ::: "memory");
    asm volatile("s_waitcnt lgkmcnt(0)\n\ts_barrier" ::: "memory");
#pragma unroll
    for (int ds = 0; ds < 8; ++ds) bst16(a.qf[ds], sQLw, voff, (unsigned)(((grp * 4 + wq) * 8 + ds) * 1024));
    STAMP(p.ws, 0, 4);
  }
  const int L = 15 - pp, n1 = 2 * pp + 2;
  STAMP(p.ws, 0, 9);
  {
  AttEnv e; e.grp = grp; e.wq = wq; e.lane = lane; e.hi = hi; e.l32 = l32; e.n1 = n1; e.L = L; e.head = head; e.lds = lds; e.lds0 = lds0; e.ws = p.ws;
  e.sK = mksrd(p.ws + WS_KF + (size_t)head * S * 256, S * 256);
  e.sV = mksrd(p.ws + WS_VF + (size_t)head * S * 256, S * 256);
  e.sQ = mksrd(p.ws + WS_QF + (size_t)b * 65536, 65536);
  e.sC = mksrd(p.ws + WS_CF + (size_t)head * 8192, 64 * 256 * 1024 - head * 8192);
  int qb = grp == 0 ? L : pp;
  {
    const unsigned gb = grp * 81920;
#pragma unroll
    for (int t = 0; t < 2; ++t)
#pragma unroll
      for (int i = 0; i < 4; ++i) dma16s(rfl(lds0 + gb + t * 16384 + wq * 4096 + i * 1024), voff, e.sK, rfl((unsigned)(t * 16384 + wq * 4096 + i * 1024)));
  }
  if (grp == BPRIO_G) __builtin_amdgcn_s_setprio(BPRIO);
  {
    const int send0 = grp == 0 ? 17 : n1;
    const int rmin0 = qb * 128 + wq * 32;
    int nt0 = ((rmin0 + 31) >> 6) + 1; if (nt0 > send0) nt0 = send0;
    int nt1 = 0;
    if (grp == 1) { const int rminL = L * 128 + wq * 32; nt1 = ((rminL + 31) >> 6) - 17 + 1; if (nt1 > 17 - n1) nt1 = 17 - n1; if (nt1 < 0) nt1 = 0; }
    a.l = 0.f; a.ps = 0.f; a.m = -1e30f;
#pragma unroll
    for (int db = 0; db < 4; ++db) a.o[db] = f32x16{};
    int s = 0; bool carry = false; int qprev = qb;
    const int nseg = grp == 0 ? 1 : 2;
#pragma unroll 1
    for (int seg = 0; seg < nseg; ++seg) {
      const int nt = seg == 0 ? nt0 : nt1;
      const int send = seg == 0 ? send0 : 17;
      const bool shortblk = grp == 1 && seg == 0;
      if (nt > 0) {
        const int slast = s + nt;
        if (carry) att_step<M_SWITCH>(a, e, s, qprev, qb); else att_step<M_FIRST>(a, e, s, qb, qb, shortblk && nt == 1);
        for (++s; s < slast; ++s) att_step<M_STEADY>(a, e, s, qb, qb, shortblk && s == slast - 1);
        carry = shortblk && s == send && nt1 > 0;
        if (!carry) {
          att_step<M_DRAIN>(a, e, s, qb, qb); ++s;
          if (shortblk) {
            store_block(a, e, qb);
#pragma unroll
            for (int db = 0; db < 4; ++db) a.o[db] = f32x16{};
            a.l = 0.f; a.ps = 0.f; a.m = -1e30f;
          }
        }
      }
      for (; s < send; ++s) { StepCtx sc; step_top(e, s, sc); dma_all(e, sc); }
      qprev = qb; qb = L;
    }
    for (; s < 18; ++s) { StepCtx sc; step_top(e, s, sc); dma_all(e, sc); }
  }
  STAMP(p.ws, 0, 10);
  asm volatile("s_waitcnt vmcnt(0) lgkmcnt(0)\n\ts_barrier" ::: "memory");
  {
    const float lt = hsum(a.l);
    char* xb = lds + w * 8192;
    float* st = (float*)(lds + 65536) + w * 128;
#pragma unroll
    for (int i = 0; i < 2; ++i)
#pragma unroll
      for (int q = 0; q < 4; ++q) {
        const f32x16& o = grp == 0 ? a.o[2 + i] : a.o[i];
        *(f32x4*)(xb + (i * 4 + q) * 1024 + lane * 16) = f32x4{o[4 * q], o[4 * q + 1], o[4 * q + 2], o[4 * q + 3]};
      }
    st[lane] = a.m; st[64 + lane] = lt;
    __syncthreads();
    const int ow = w ^ 4;
    const char* yb = lds + ow * 8192; const float* so = (const float*)(lds + 65536) + ow * 128;
    const float m2 = so[lane], l2 = so[64 + lane];
    const float mn = fmaxf(a.m, m2), f1 = EXP2(a.m - mn), f2 = EXP2(m2 - mn);
    const float inv = 1.f / (lt * f1 + l2 * f2);
    const float g1 = f1 * inv, g2 = f2 * inv;
#pragma unroll
    for (int i = 0; i < 2; ++i) {
      f32x16 o = grp == 0 ? a.o[i] : a.o[2 + i];
#pragma unroll
      for (int q = 0; q < 4; ++q) { const f32x4 y = *(const f32x4*)(yb + (i * 4 + q) * 1024 + lane * 16);
#pragma unroll
        for (int e = 0; e < 4; ++e) o[4 * q + e] = o[4 * q + e] * g1 + y[e] * g2; }
      const int db = (grp == 0 ? 0 : 2) + i;
#pragma unroll
      for (int g = 0; g < 2; ++g)
        bst16(pack8n(o, g), e.sC, voff, rfl((unsigned)(((L * 4 + wq) * 256 + db * 2 + g) * 1024)));
    }
  }
  }
  STAMP(p.ws, 0, 11);
}

struct K3P { char* ws; const void* wo; const void* awo; void* out; };
constexpr int K3_WS = 24576;
struct K3C {
  unsigned lds0, w, xvoff, wvoff;
  __amdgpu_buffer_rsrc_t sX, sW;
  lds_cptr wb;
};
DI void xload(bf16x8& d, unsigned voff, __amdgpu_buffer_rsrc_t srd, unsigned soff) {
  asm volatile("buffer_load_dwordx4 %0, %1, %2, %3 offen" : "=v"(d) : "v"(voff), "s"(srd), "s"(soff) : "memory");
}
template <int I> DI void k3_dmaW(const K3C& c, int st) {
  dma16(c.lds0 + (st & 3) * K3_WS + (c.w * 3 + I) * 1024, c.wvoff + I * 128, c.sW, (unsigned)((st * 64 + c.w * 8) * 1152));
}
struct K3F { s16x4 wl[3], wh[3]; };
DI void k3_stage(const K3C& c, f32x16 (&acc)[6], bf16x8& x, K3F& fa, K3F& fb, int st, unsigned xso) {
  const int so = (st & 3) * K3_WS, sn = ((st + 1) & 3) * K3_WS;
#pragma unroll
  for (int m = 0; m < 3; ++m) {
    acc[m] = MFMA(cat(fa.wl[m], fa.wh[m]), x, acc[m]);
    lds_cptr p = c.wb + so + (3 + m) * 512;
    fb.wl[m] = lds_tr(p); fb.wh[m] = lds_tr(p + 3072);
    if (m == 0) k3_dmaW<0>(c, st + 3);
    if (m == 2) k3_dmaW<1>(c, st + 3);
    SBAR();
  }
#pragma unroll
  for (int m = 0; m < 3; ++m) {
    acc[3 + m] = MFMA(cat(fb.wl[m], fb.wh[m]), x, acc[3 + m]);
    lds_cptr p = c.wb + sn + m * 512;
    fa.wl[m] = lds_tr(p); fa.wh[m] = lds_tr(p + 3072);
    if (m == 1) k3_dmaW<2>(c, st + 3);
    SBAR();
  }
  xload(x, c.xvoff, c.sX, xso);
  SBAR();
}
__global__ __launch_bounds__(512, 2) void k3_out(K3P p) {
  extern __shared__ __attribute__((aligned(16))) char lds[];
  const int tid = threadIdx.x, lane = tid & 63, l32 = lane & 31, hi = lane >> 5;
  const int b = blockIdx.x;
  const bool isB = b < 192;
  K3C c;
  c.lds0 = (unsigned)(size_t)(lds_ptr)lds; c.w = rfl(tid >> 6);
  const int rt = c.w & 1, kq = c.w >> 1;
  const int blk = (lane >> 4) & 1, q4 = (lane & 15) >> 2, p4 = lane & 3;
  const int nst = isB ? 32 : 9;
  c.xvoff = lane * 16;
  { const int sub = lane >> 5, rr = (lane & 31) >> 2, c4 = lane & 3; c.wvoff = (unsigned)(rr * 1152 + (32 * sub + 8 * c4) * 2); }
  c.wb = (lds_cptr)lds + (2 * kq) * 3072 + 32 * blk + 8 * p4 + (4 * hi + q4) * 64;
  STAMP(p.ws, 2, 0);
  const int ntile = isB ? 1 : ((b - 192) < 32 ? 2 : 1);
#pragma unroll 1
  for (int it = 0; it < ntile; ++it) {
    int t, sk = 0;
    if (isB) { const int x = b & 7, i = b >> 3; t = x * 12 + (i >> 1); sk = i & 1; } else t = (b - 192) + 64 * it;
    const int rb = t / 3, cb = t - rb * 3;
    c.sW = isB ? mksrd((const char*)p.wo + (size_t)sk * 2048 * 1152 + cb * 384, 2048 * 1152 - cb * 384) : mksrd((const char*)p.awo + cb * 384, 576 * 1152 - cb * 384);
    {
      const char* xb = isB ? p.ws + WS_CF + ((size_t)(rb * 2 + rt) * 256 + sk * 128) * 1024 : p.ws + WS_OA + (size_t)(rb * 2 + rt) * 36 * 1024;
      c.sX = mksrd(xb + kq * 1024, nst * 4096 - kq * 1024);
    }
    f32x16 acc[6];
#pragma unroll
    for (int ct = 0; ct < 6; ++ct) acc[ct] = f32x16{};
    bf16x8 x0, x1, x2;
    if (it) asm volatile("s_waitcnt lgkmcnt(0)\n\ts_barrier" ::: "memory");
    k3_dmaW<0>(c, 0); k3_dmaW<1>(c, 0); k3_dmaW<2>(c, 0);
    xload(x0, c.xvoff, c.sX, 0);
    k3_dmaW<0>(c, 1); k3_dmaW<1>(c, 1); k3_dmaW<2>(c, 1);
    xload(x1, c.xvoff, c.sX, 4096);
    k3_dmaW<0>(c, 2); k3_dmaW<1>(c, 2); k3_dmaW<2>(c, 2);
    (void)x2;
    K3F fa, fb;
#define K3_WAIT(N, X) asm volatile("s_waitcnt vmcnt(" #N ")\n\ts_barrier" : "+v"(X) :: "memory")
    K3_WAIT(4, x0); SBAR();
    STAMP(p.ws, 2, 1);
#pragma unroll
    for (int m = 0; m < 3; ++m) { fa.wl[m] = lds_tr(c.wb + m * 512); fa.wh[m] = lds_tr(c.wb + m * 512 + 3072); }
    SBAR();
    int st = 0;
    for (; st + 1 < nst; st += 2) {
      k3_stage(c, acc, x0, fa, fb, st, (unsigned)((st + 2) * 4096));
      K3_WAIT(4, x1); SBAR();
      k3_stage(c, acc, x1, fa, fb, st + 1, (unsigned)((st + 3) * 4096));
      K3_WAIT(4, x0); SBAR();
    }
    if (st < nst) k3_stage(c, acc, x0, fa, fb, st, (unsigned)((st + 2) * 4096));
    STAMP(p.ws, 2, 2);
    asm volatile("s_waitcnt vmcnt(0) lgkmcnt(0)\n\ts_barrier" : "+v"(x0), "+v"(x1) :: "memory");
    f32x4 h[12];
    {
      const bool up = (kq & 2) != 0;
      char* mine = lds + c.w * 12288; const char* theirs = lds + (c.w ^ 4) * 12288;
#pragma unroll
      for (int c3 = 0; c3 < 3; ++c3)
#pragma unroll
        for (int g = 0; g < 4; ++g) {
          const f32x16& give = up ? acc[c3] : acc[3 + c3];
          *(f32x4*)(mine + (c3 * 4 + g) * 1024 + lane * 16) = f32x4{give[4 * g], give[4 * g + 1], give[4 * g + 2], give[4 * g + 3]};
        }
      __syncthreads();
#pragma unroll
      for (int c3 = 0; c3 < 3; ++c3)
#pragma unroll
        for (int g = 0; g < 4; ++g) {
          const f32x16& keep = up ? acc[3 + c3] : acc[c3];
          h[c3 * 4 + g] = f32x4{keep[4 * g], keep[4 * g + 1], keep[4 * g + 2], keep[4 * g + 3]} + *(const f32x4*)(theirs + (c3 * 4 + g) * 1024 + lane * 16);
        }
      __syncthreads();
    }
    f32x4 q[6];
    {
      const bool up = (kq & 1) != 0;
      char* mine = lds + c.w * 6144; const char* theirs = lds + (c.w ^ 2) * 6144;
#pragma unroll
      for (int i = 0; i < 6; ++i) *(f32x4*)(mine + i * 1024 + lane * 16) = up ? h[i] : h[6 + i];
      __syncthreads();
#pragma unroll
      for (int i = 0; i < 6; ++i) q[i] = (up ? h[6 + i] : h[i]) + *(const f32x4*)(theirs + i * 1024 + lane * 16);
    }
    const int it0 = (kq & 2) * 6 + (kq & 1) * 6;
    auto store_out = [&](int coloff) {
      const int row = rb * 64 + rt * 32 + l32;
#pragma unroll
      for (int i = 0; i < 6; ++i) { const int item = it0 + i, ct = item >> 2, g = item & 3;
        *(u32x2*)((char*)p.out + (size_t)row * 2304 + coloff + (cb * 192 + ct * 32 + 8 * g + 4 * hi) * 2) = u32x2{cvtpk(q[i][0], q[i][1]), cvtpk(q[i][2], q[i][3])}; } };
    if (!isB) store_out(0);
    else {
      char* slab = p.ws + WS_KF + (size_t)(t * 2) * 12288;
      if (rt != sk) {
        const __amdgpu_buffer_rsrc_t sP = mksrd(slab + sk * 12288 + kq * 3072, 3072);
#pragma unroll
        for (int i = 0; i < 3; ++i)
          __builtin_amdgcn_raw_buffer_store_b128(u32x4{cvtpk(q[2 * i][0], q[2 * i][1]), cvtpk(q[2 * i][2], q[2 * i][3]), cvtpk(q[2 * i + 1][0], q[2 * i + 1][1]), cvtpk(q[2 * i + 1][2], q[2 * i + 1][3])}, sP, lane * 16 + i * 1024, 0, 16);
        asm volatile("s_waitcnt vmcnt(0)" ::: "memory");
        if (lane == 0) __hip_atomic_fetch_add((__attribute__((address_space(1))) unsigned*)(p.ws + WS_CNT + (t * 2 + sk) * 128), 1u, __ATOMIC_RELAXED, __HIP_MEMORY_SCOPE_AGENT);
      } else {
        const __attribute__((address_space(1))) unsigned* fl = (const __attribute__((address_space(1))) unsigned*)(p.ws + WS_CNT + (t * 2 + (sk ^ 1)) * 128);
        for (unsigned spin = 0; spin < (1u << 20); ++spin) {
          if (__hip_atomic_load(fl, __ATOMIC_RELAXED, __HIP_MEMORY_SCOPE_AGENT) >= 4u) break;
          __builtin_amdgcn_s_sleep(1);
        }
        __builtin_amdgcn_fence(__ATOMIC_ACQUIRE, "wavefront");
        const __amdgpu_buffer_rsrc_t sP = mksrd(slab + (sk ^ 1) * 12288 + kq * 3072, 3072);
#pragma unroll
        for (int i = 0; i < 3; ++i) {
          const u32x4 y = __builtin_bit_cast(u32x4, __builtin_amdgcn_raw_buffer_load_b128(sP, lane * 16 + i * 1024, 0, 16));
          q[2 * i][0] += __uint_as_float(y[0] << 16); q[2 * i][1] += __uint_as_float(y[0] & 0xFFFF0000u); q[2 * i][2] += __uint_as_float(y[1] << 16); q[2 * i][3] += __uint_as_float(y[1] & 0xFFFF0000u);
          q[2 * i + 1][0] += __uint_as_float(y[2] << 16); q[2 * i + 1][1] += __uint_as_float(y[2] & 0xFFFF0000u); q[2 * i + 1][2] += __uint_as_float(y[3] << 16); q[2 * i + 1][3] += __uint_as_float(y[3] & 0xFFFF0000u);
        }
        store_out(1152);
      }
    }
  }
  STAMP(p.ws, 2, 3);
}

extern "C" void kernel_launch(void* const* d_in, const int* in_sizes, int n_in, void* d_out, int out_size, void* d_ws, size_t ws_size, hipStream_t stream) {
  static bool attr = false;
  if (!attr) {
    hipFuncSetAttribute((const void*)k0_aproj, hipFuncAttributeMaxDynamicSharedMemorySize, 147456);
    hipFuncSetAttribute((const void*)kb_main, hipFuncAttributeMaxDynamicSharedMemorySize, 163840);
    hipFuncSetAttribute((const void*)k3_out, hipFuncAttributeMaxDynamicSharedMemorySize, 4 * K3_WS);
    attr = true;
  }
  K1P p1; p1.cx = d_in[6]; p1.bx = d_in[5]; p1.wuk = d_in[8]; p1.wuv = d_in[9]; p1.wq = d_in[7];
  p1.ax = d_in[0]; p1.awq = d_in[1]; p1.awk = d_in[2]; p1.awv = d_in[3]; p1.wo = d_in[10]; p1.awo = d_in[4]; p1.ws = (char*)d_ws;
  k0_aproj<<<256, 512, 147456, stream>>>(p1);
  kb_main<<<256, 512, 163840, stream>>>(p1);
  K3P p3; p3.ws = (char*)d_ws; p3.wo = d_in[10]; p3.awo = d_in[4]; p3.out = d_out;
  k3_out<<<256, 512, 4 * K3_WS, stream>>>(p3);
}
```

```cpp
#include <hip/hip_runtime.h>
#include <hip/hip_bf16.h>
#include <cstdio>
#include <cstdint>
#include <utility>

#define PUBMODE 0
#define KV_ACQ 0
#define BPRIO 1
#define BPRIO_G 1
#define PPRIO 2
#define WARM 1
#define ST_AUX 0
#define DI __device__ __forceinline__
typedef short bf16x8 __attribute__((ext_vector_type(8)));
typedef short s16x4 __attribute__((ext_vector_type(4)));
typedef float f32x16 __attribute__((ext_vector_type(16)));
typedef float f32x4 __attribute__((ext_vector_type(4)));
typedef float f32x2 __attribute__((ext_vector_type(2)));
typedef __bf16 bf16x2_t __attribute__((ext_vector_type(2)));
typedef unsigned u32x4 __attribute__((ext_vector_type(4)));
typedef unsigned u32x2 __attribute__((ext_vector_type(2)));
typedef __attribute__((address_space(3))) const char* lds_cptr;
typedef __attribute__((address_space(3))) char* lds_ptr;
#define SBAR() __builtin_amdgcn_sched_barrier(0)
#define PIN(x) asm volatile("" : "+v"(x))
#define MFMA(a, b, c) __builtin_amdgcn_mfma_f32_32x32x16_bf16(a, b, c, 0, 0, 0)
#define SRD_W3 0x00020000

DI unsigned rfl(unsigned x) { return __builtin_amdgcn_readfirstlane(x); }
DI unsigned cvtpk(float lo, float hi) { f32x2 v = {lo, hi}; bf16x2_t b = __builtin_convertvector(v, bf16x2_t); return __builtin_bit_cast(unsigned, b); }
DI void dma16(unsigned m0, unsigned voff, __amdgpu_buffer_rsrc_t srd, unsigned soff) {
  asm volatile("s_mov_b32 m0, %0\n\ts_nop 0\n\tbuffer_load_dwordx4 %1, %2, %3 offen lds" :: "s"(m0), "v"(voff), "s"(srd), "s"(soff) : "m0", "memory");
}
template <int OFF> DI void dma16o(unsigned m0, unsigned voff, __amdgpu_buffer_rsrc_t srd, unsigned soff) {
  asm volatile("s_mov_b32 m0, %0\n\ts_nop 0\n\tbuffer_load_dwordx4 %1, %2, %3 offen offset:%4 lds" :: "s"(m0), "v"(voff), "s"(srd), "s"(soff), "i"(OFF) : "m0", "memory");
}
DI void dma16s(unsigned m0, unsigned voff, __amdgpu_buffer_rsrc_t srd, unsigned soff) {
  asm volatile("s_mov_b32 m0, %0\n\ts_nop 0\n\tbuffer_load_dwordx4 %1, %2, %3 offen sc1 lds" :: "s"(m0), "v"(voff), "s"(srd), "s"(soff) : "m0", "memory");
}
template <int OFF> DI void dma16os(unsigned m0, unsigned voff, __amdgpu_buffer_rsrc_t srd, unsigned soff) {
  asm volatile("s_mov_b32 m0, %0\n\ts_nop 0\n\tbuffer_load_dwordx4 %1, %2, %3 offen offset:%4 sc1 lds" :: "s"(m0), "v"(voff), "s"(srd), "s"(soff), "i"(OFF) : "m0", "memory");
}
DI bf16x8 bld16s(__amdgpu_buffer_rsrc_t srd, unsigned voff, unsigned soff) { return __builtin_bit_cast(bf16x8, __builtin_amdgcn_raw_buffer_load_b128(srd, voff, soff, 16)); }
DI void dma4(unsigned m0, unsigned voff, __amdgpu_buffer_rsrc_t srd, unsigned soff) {
  asm volatile("s_mov_b32 m0, %0\n\ts_nop 0\n\tbuffer_load_dword %1, %2, %3 offen lds" :: "s"(m0), "v"(voff), "s"(srd), "s"(soff) : "m0", "memory");
}
DI __amdgpu_buffer_rsrc_t mksrd(const void* p, unsigned bytes) { return __builtin_amdgcn_make_buffer_rsrc((void*)p, (short)0, (int)bytes, SRD_W3); }
DI bf16x8 bld16(__amdgpu_buffer_rsrc_t srd, unsigned voff, unsigned soff) { return __builtin_bit_cast(bf16x8, __builtin_amdgcn_raw_buffer_load_b128(srd, voff, soff, 0)); }
DI void bst16(bf16x8 v, __amdgpu_buffer_rsrc_t srd, unsigned voff, unsigned soff) { __builtin_amdgcn_raw_buffer_store_b128(__builtin_bit_cast(u32x4, v), srd, voff + soff, 0, ST_AUX); }
template <int N> DI void wait_bar() { asm volatile("s_waitcnt vmcnt(%0)\n\ts_barrier" :: "i"(N) : "memory"); }
DI bf16x8 lds_rd16(lds_cptr p) { return *(const __attribute__((address_space(3))) bf16x8*)p; }
DI s16x4 lds_tr(lds_cptr p) { return __builtin_bit_cast(s16x4, __builtin_amdgcn_ds_read_tr16_b64_v4i16((__attribute__((address_space(3))) s16x4*)p)); }
DI bf16x8 cat(s16x4 a, s16x4 b) { return __builtin_shufflevector(a, b, 0, 1, 2, 3, 4, 5, 6, 7); }
DI bf16x8 pack8(const f32x16& x, int g, float sc) {
  u32x4 w;
#pragma unroll
  for (int e = 0; e < 4; ++e) w[e] = cvtpk(x[8 * g + 2 * e] * sc, x[8 * g + 2 * e + 1] * sc);
  return __builtin_bit_cast(bf16x8, w);
}
DI bf16x8 pack8n(const f32x16& x, int g) {
  u32x4 w;
#pragma unroll
  for (int e = 0; e < 4; ++e) w[e] = cvtpk(x[8 * g + 2 * e], x[8 * g + 2 * e + 1]);
  return __builtin_bit_cast(bf16x8, w);
}

#define DBG_S 1
#define STAMP(ws, k, i)
DI float hmax(float m) { auto rr = __builtin_amdgcn_permlane32_swap(__float_as_uint(m), __float_as_uint(m), false, false); return fmaxf(__uint_as_float(rr[0]), __uint_as_float(rr[1])); }
DI float hsum(float m) { auto rr = __builtin_amdgcn_permlane32_swap(__float_as_uint(m), __float_as_uint(m), false, false); return __uint_as_float(rr[0]) + __uint_as_float(rr[1]); }
constexpr int S = 2048, H = 32, DB = 128, MB = 576, DC = 512, DA = 576, DHA = 18;
constexpr float CB = 0.08838834764831845f * 1.4426950408889634f;
constexpr float CA = 0.23570226039551584f * 1.4426950408889634f;
constexpr size_t WS_KF = 0;
constexpr size_t WS_VF = WS_KF + (size_t)H * S * DB * 2;
constexpr size_t WS_QF = WS_VF + (size_t)H * S * DB * 2;
constexpr size_t WS_CF = WS_QF + (size_t)H * S * DB * 2;
constexpr size_t WS_QA = WS_CF + (size_t)H * S * DB * 2;
constexpr int ATB = 3072;
constexpr size_t WS_KA = WS_QA + (size_t)S * DA * 2 + 256;
constexpr size_t WS_KMAX = WS_KA + 64 * ATB;
constexpr size_t WS_OA = WS_KMAX + 1024;
constexpr size_t WS_PART = WS_OA + (size_t)S * DA * 2;
constexpr size_t WS_FLAG = WS_PART + (size_t)4 * S * 576 * 4;
constexpr size_t WS_CNT = WS_FLAG + 256 * 128 + 1024;
constexpr size_t WS_END = WS_CNT + 192 * 128;

struct K1P {
  const void *cx, *bx, *wuk, *wuv, *wq;
  const void *ax, *awq, *awk, *awv;
  const void *wo, *awo;
  char* ws;
};
constexpr int XSLOT = 32768, WRING = 98304, WSLOT = 8192, ARING = 131072, NPSTEP = 50;
struct PG {
  lds_cptr xb[4];
  lds_cptr wb;
  unsigned lds0, aw;
  unsigned xoC[2], xoX[2], woff, voff;
  __amdgpu_buffer_rsrc_t sC, sX, sUK, sUV, sWQ, sA;
  unsigned rowL, rowS;
};
constexpr int TAc(int g) { return (g * 64) / NPSTEP; }
template <int G, int I> DI void g_piece(const PG& c) {
  if constexpr (I < 2) {
    constexpr int gw = G + 3, pass = gw < 16 ? 0 : (gw < 32 ? 1 : 2), lg = gw - (pass == 0 ? 0 : (pass == 1 ? 16 : 32));
    const unsigned m0 = c.lds0 + WRING + (gw & 3) * WSLOT + c.aw * 2048 + I * 1024, so = (unsigned)(lg * 32 * 8192) + c.aw * 65536;
    dma16(m0, c.woff + I * 128, pass == 0 ? c.sUK : (pass == 1 ? c.sUV : c.sWQ), so);
  } else if constexpr (I < 6) {
    constexpr int i = I - 2, T = (G >> 1) + 2, half = G & 1;
    const unsigned m0 = c.lds0 + (T % 3) * XSLOT + (16 * half + i) * 1024 + c.aw * 4096;
    if constexpr (T < 16) dma16(m0, c.xoC[i & 1], c.sC, (unsigned)((128 * half + 8 * i) * 1024 + (T & 7) * 128) + c.aw * 32768);
    else dma16(m0, c.xoX[i & 1], c.sX, (half ? c.rowS : c.rowL) + (unsigned)(8 * i * 1152 + (T - 16) * 128) + (T > 24 ? 0x40000000u : 0u) + c.aw * (32 * 1152));
  } else {
    constexpr int f0 = TAc(G + 4), nf = (TAc(G + 5) - f0) * 3;
    int q = 2 * c.aw + (I - 6); if (q >= nf) q = q - nf < nf ? q - nf : 0;
    const int kt = f0 + q / 3, part = q % 3;
    dma16(rfl(c.lds0 + ARING + (kt % 10) * ATB + part * 1024), c.voff, c.sA, rfl((unsigned)(kt * ATB + part * 1024)));
  }
}
DI void st16_sc1(bf16x8 v, unsigned voff, __amdgpu_buffer_rsrc_t srd, unsigned soff) {
  asm volatile("buffer_store_dwordx4 %0, %1, %2, %3 offen sc1\n\ts_nop 1" :: "v"(v), "v"(voff), "s"(srd), "s"(soff) : "memory");
}
struct FragKV { bf16x8 x[4]; s16x4 wl[2], wh[2]; };
template <int G, int SS, int I> DI void kv_ld(const PG& c, FragKV& f) {
  constexpr int T = G >> 1;
  lds_cptr xp = c.xb[2 * (G & 1) + SS] + (T % 3) * XSLOT;
  lds_cptr wp = c.wb + (G & 3) * WSLOT + SS * 4096;
  if constexpr (I == 0) f.x[0] = lds_rd16(xp);
  else if constexpr (I == 1) f.wl[0] = lds_tr(wp);
  else if constexpr (I == 2) f.wh[0] = lds_tr(wp + 256);
  else if constexpr (I == 3) f.wl[1] = lds_tr(wp + 512);
  else if constexpr (I == 4) f.wh[1] = lds_tr(wp + 768);
  else f.x[I - 4] = lds_rd16(xp + (I - 4) * 4096);
}
template <bool ISV, int M> DI void kv_mfma(f32x16 (&acc)[4][2], const FragKV& f) {
  constexpr int r = M >> 1, ct = M & 1;
  const bf16x8 wf = cat(f.wl[ct], f.wh[ct]);
  acc[r][ct] = ISV ? MFMA(f.x[r], wf, acc[r][ct]) : MFMA(wf, f.x[r], acc[r][ct]);
}
struct KVSt { __amdgpu_buffer_rsrc_t srd; unsigned voff; };
constexpr int relK(int i) { return ((i >> 2) * 8 + ((i >> 1) & 1) * 2 + (i & 1)) * 1024; }
constexpr int relV(int i) { return (((i >> 2) >> 1) * 16 + (2 * ((i >> 2) & 1) + (i & 1)) * 4 + ((i >> 1) & 1)) * 1024; }
template <bool ISV, int GN, int SN, bool LOAD, int STI, bool PV, int DG, int DB, int M> DI void kv_gap(const PG& c, f32x16 (&acc)[4][2], const FragKV& cur, FragKV& nxt, const bf16x8 (&kvp)[16], const KVSt& ks) {
  kv_mfma<ISV, M>(acc, cur);
  if constexpr (LOAD) kv_ld<GN, SN, M>(c, nxt);
  if constexpr ((M & 1) && DB + (M >> 1) < 8) g_piece<DG, DB + (M >> 1)>(c);
  if constexpr (STI >= 0 && M == 6) st16_sc1(kvp[STI], ks.voff, ks.srd, (unsigned)(PV ? relV(STI) : relK(STI)));
  SBAR();
}
template <bool ISV, int GN, int SN, bool LOAD, int STI, bool PV, int DG, int DB> DI void kv_phase(const PG& c, f32x16 (&acc)[4][2], const FragKV& cur, FragKV& nxt, const bf16x8 (&kvp)[16], const KVSt& ks) {
  kv_gap<ISV, GN, SN, LOAD, STI, PV, DG, DB, 0>(c, acc, cur, nxt, kvp, ks); kv_gap<ISV, GN, SN, LOAD, STI, PV, DG, DB, 1>(c, acc, cur, nxt, kvp, ks);
  kv_gap<ISV, GN, SN, LOAD, STI, PV, DG, DB, 2>(c, acc, cur, nxt, kvp, ks); kv_gap<ISV, GN, SN, LOAD, STI, PV, DG, DB, 3>(c, acc, cur, nxt, kvp, ks);
  kv_gap<ISV, GN, SN, LOAD, STI, PV, DG, DB, 4>(c, acc, cur, nxt, kvp, ks); kv_gap<ISV, GN, SN, LOAD, STI, PV, DG, DB, 5>(c, acc, cur, nxt, kvp, ks);
  kv_gap<ISV, GN, SN, LOAD, STI, PV, DG, DB, 6>(c, acc, cur, nxt, kvp, ks); kv_gap<ISV, GN, SN, LOAD, STI, PV, DG, DB, 7>(c, acc, cur, nxt, kvp, ks);
}
constexpr int nSt(int g) { return (g >= 16 && g < 32) ? 1 : ((g >= 32 && g < 40) ? 2 : 0); }
constexpr int FLAGG = 43;
template <int PASS, int G> DI void g_step(const PG& c, f32x16 (&acc)[4][2], FragKV& fa, FragKV& fb, const bf16x8 (&kvp)[16], const KVSt& ks, char* flag) {
  constexpr int G0 = PASS == 0 ? 0 : (PASS == 1 ? 16 : 32), LG = G - G0;
  if constexpr (G == 0) wait_bar<12>(); else if constexpr (G == 1) wait_bar<8>(); else wait_bar<nSt(G - 2) + 8 + nSt(G - 1)>();
  SBAR();
  if constexpr (G == FLAGG) { if (threadIdx.x == 256) __hip_atomic_store((__attribute__((address_space(1))) unsigned*)flag, 1u, __ATOMIC_RELAXED, __HIP_MEMORY_SCOPE_AGENT); }
  if constexpr (G == 0) {
    kv_ld<0, 0, 0>(c, fa); kv_ld<0, 0, 1>(c, fa); kv_ld<0, 0, 2>(c, fa); kv_ld<0, 0, 3>(c, fa);
    kv_ld<0, 0, 4>(c, fa); kv_ld<0, 0, 5>(c, fa); kv_ld<0, 0, 6>(c, fa); kv_ld<0, 0, 7>(c, fa); SBAR();
  }
  constexpr int STA = PASS == 1 ? LG : ((PASS == 2 && LG < 8) ? 2 * LG : -1);
  constexpr int STB = (PASS == 2 && LG < 8) ? 2 * LG + 1 : -1;
  kv_phase<PASS == 1, G, 1, true, -1, PASS == 2, G, 0>(c, acc, fa, fb, kvp, ks);
  kv_phase<PASS == 1, G + 1, 0, (G + 1 < NPSTEP), -1, PASS == 2, G, 4>(c, acc, fb, fa, kvp, ks);
  if constexpr (STA >= 0) st16_sc1(kvp[STA], ks.voff, ks.srd, (unsigned)(PASS == 2 ? relV(STA) : relK(STA)));
  if constexpr (STB >= 0) st16_sc1(kvp[STB], ks.voff, ks.srd, (unsigned)relV(STB));
  SBAR();
}
template <int PASS, int G, int GEND> struct GLoop { static DI void run(const PG& c, f32x16 (&acc)[4][2], FragKV& fa, FragKV& fb, const bf16x8 (&kvp)[16], const KVSt& ks, char* flag) {
  g_step<PASS, G>(c, acc, fa, fb, kvp, ks, flag); if constexpr (G + 1 < GEND) GLoop<PASS, G + 1, GEND>::run(c, acc, fa, fb, kvp, ks, flag); } };

__global__ __launch_bounds__(512, 2) void k0_aproj(K1P p) {
  extern __shared__ __attribute__((aligned(16))) char lds[];
  const int tid = threadIdx.x, lane = tid & 63, l32 = lane & 31, hi = lane >> 5;
  const int b = blockIdx.x, xcd = b & 7, ii = b >> 3;
  struct { unsigned lds0; } c; c.lds0 = (unsigned)(size_t)(lds_ptr)lds;
  const int w = rfl(tid >> 6);
  const int blk = (lane >> 4) & 1, q4 = (lane & 15) >> 2, p4 = lane & 3;
  if (tid == 0) { *(unsigned*)(p.ws + WS_FLAG + b * 128) = 0u; if (b < 192) *(unsigned*)(p.ws + WS_CNT + b * 128) = 0u; }
  if (ii >= 28) {
    const int t = (b & 7) + 8 * (ii - 28);
    unsigned v = 0;
    { const int i = t * 512 + tid; if (i < 2048 * 2) v ^= *(const unsigned*)((const char*)p.cx + (size_t)(i >> 1) * 1024 + (i & 1) * 128); }
    { const int i = t * 512 + tid; if (i < 96 * 64) v ^= *(const unsigned*)((const char*)p.wuk + (size_t)i * 128); }
    if (v == 0x9E3779B9u) *(unsigned*)(p.ws + WS_FLAG + 256 * 128 + 64) = v;
  }
  if (ii < 20) {
    const int rg = 4 * xcd + ii / 5, cg = ii % 5;
    const __amdgpu_buffer_rsrc_t sAX = mksrd((const char*)p.ax + (size_t)rg * 64 * 1152, 64 * 1152);
    const __amdgpu_buffer_rsrc_t sAQ = mksrd((const char*)p.awq + cg * 256, 576 * 1152 - cg * 256);
    constexpr int ASL = 73728, AWO = 24576;
    const unsigned xv = (unsigned)((lane >> 3) * 1152 + (((lane & 7) ^ ((4 * (w & 1) + (lane >> 4)) & 7)) * 16));
    const unsigned wv = (unsigned)(((lane & 31) >> 2) * 1152 + (32 * (lane >> 5) + 8 * (lane & 3)) * 2);
    auto issue = [&](int th) {
      const unsigned base = c.lds0 + (th & 1) * ASL;
#pragma unroll
      for (int kc = 0; kc < 3; ++kc) dma16(rfl(base + (kc * 8 + w) * 1024), xv, sAX, rfl((unsigned)(w * 8 * 1152 + (th * 3 + kc) * 128)));
#pragma unroll
      for (int i = 0; i < 6; ++i) {
        const int g = 3 * w + (i >> 1), pr = i & 1;
        dma16(rfl(base + AWO + (g * 2 + pr) * 1024), wv, sAQ, rfl((unsigned)((th * 192 + g * 8) * 1152 + pr * 128)));
      }
    };
    const int rt = w >> 2, ct = w & 3, tile = 4 * cg + ct;
    f32x16 acc = f32x16{};
    issue(0); issue(1);
    for (int th = 0; th < 3; ++th) {
      if (th < 2) asm volatile("s_waitcnt vmcnt(9) lgkmcnt(0)\n\ts_barrier" ::: "memory");
      else asm volatile("s_waitcnt vmcnt(0) lgkmcnt(0)\n\ts_barrier" ::: "memory");
      lds_cptr xp = (lds_cptr)lds + (th & 1) * ASL + rt * 4096 + 128 * l32;
      lds_cptr wp = (lds_cptr)lds + (th & 1) * ASL + AWO + ct * 512 + 2048 * hi + 64 * q4 + 32 * blk + 8 * p4;
      bf16x8 xf[4]; s16x4 wl[4], wh[4];
#define ALD(k) do { xf[(k) & 3] = lds_rd16(xp + ((k) >> 2) * 8192 + 16 * ((2 * ((k) & 3) + hi) ^ ((l32 >> 1) & 7))); wl[(k) & 3] = lds_tr(wp + (k) * 4096); wh[(k) & 3] = lds_tr(wp + (k) * 4096 + 256); } while (0)
      if (tile < 18) {
        ALD(0); ALD(1); ALD(2);
#pragma unroll
        for (int k = 0; k < 12; ++k) {
          if (k + 3 < 12) ALD(k + 3);
          acc = MFMA(cat(wl[k & 3], wh[k & 3]), xf[k & 3], acc);
          SBAR();
        }
      }
      if (th == 0) { asm volatile("s_waitcnt lgkmcnt(0)\n\ts_barrier" ::: "memory"); issue(2); }
    }
    const int row = (rg * 2 + rt) * 32 + l32;
    if (tile < 18) {
#pragma unroll
      for (int g = 0; g < 4; ++g) {
        u32x2 v = {cvtpk(acc[4 * g] * CA, acc[4 * g + 1] * CA), cvtpk(acc[4 * g + 2] * CA, acc[4 * g + 3] * CA)};
        *(u32x2*)(p.ws + WS_QA + (size_t)row * 1152 + (tile * 32 + 8 * g + 4 * hi) * 2) = v;
      }
    }
  } else if (ii < 28) {
    const int r32 = 8 * xcd + (ii - 20);
    const __amdgpu_buffer_rsrc_t sAX = mksrd((const char*)p.ax + (size_t)r32 * 32 * 1152, 32 * 1152);
    for (int pc = w; pc < 36; pc += 8) {
      const int kc = pc >> 2, rq = pc & 3, r = lane >> 3, row = 8 * rq + r, ch = (lane & 7) ^ ((row >> 1) & 7);
      dma16(rfl(c.lds0 + pc * 1024), (unsigned)(row * 1152 + ch * 16), sAX, rfl((unsigned)(kc * 128)));
    }
    {
      const unsigned* gk = (const unsigned*)p.awk; const unsigned* gv = (const unsigned*)p.awv;
      unsigned vk[11], vv[11];
#pragma unroll
      for (int it = 0; it < 11; ++it) { const int i = tid + it * 512; const int ic = i < 5184 ? i : 0; vk[it] = gk[ic]; vv[it] = gv[ic]; }
#pragma unroll
      for (int it = 0; it < 11; ++it) {
        const int i = tid + it * 512;
        if (i < 5184) { const int r = i / 9, d = i - r * 9; char* dst = lds + 36864 + (r >> 3) * 1024 + (r & 7) * 64 + d * 4; *(unsigned*)dst = vk[it]; *(unsigned*)(dst + 512) = vv[it]; }
      }
    }
    asm volatile("s_waitcnt vmcnt(0) lgkmcnt(0)\n\ts_barrier" ::: "memory");
    const int isv = w >> 2, kq = w & 3;
    f32x16 acc = f32x16{};
    lds_cptr xp = (lds_cptr)lds + 128 * l32;
    lds_cptr wp = (lds_cptr)lds + 36864 + isv * 512 + 1024 * hi + 64 * q4 + 32 * blk + 8 * p4;
#pragma unroll
    for (int k9 = 0; k9 < 9; ++k9) {
      const int k = kq * 9 + k9;
      const bf16x8 xf = lds_rd16(xp + (k >> 2) * 4096 + 16 * ((2 * (k & 3) + hi) ^ ((l32 >> 1) & 7)));
      const bf16x8 wf = cat(lds_tr(wp + k * 2048), lds_tr(wp + k * 2048 + 256));
      if (isv) acc = MFMA(xf, wf, acc); else acc = MFMA(wf, xf, acc);
    }
    asm volatile("s_waitcnt lgkmcnt(0)\n\ts_barrier" ::: "memory");
    float* red = (float*)lds + w * 1024;
#pragma unroll
    for (int q = 0; q < 4; ++q) *(f32x4*)(red + q * 256 + lane * 4) = f32x4{acc[4 * q], acc[4 * q + 1], acc[4 * q + 2], acc[4 * q + 3]};
    __syncthreads();
    if (kq == 0) {
#pragma unroll
      for (int o = 1; o < 4; ++o)
#pragma unroll
        for (int q = 0; q < 4; ++q) { const f32x4 y = *(const f32x4*)(red + o * 1024 + q * 256 + lane * 4); acc[4 * q] += y[0]; acc[4 * q + 1] += y[1]; acc[4 * q + 2] += y[2]; acc[4 * q + 3] += y[3]; }
      const int row = r32 * 32 + l32;
      char* hb = p.ws + WS_KA + (size_t)r32 * ATB;
      if (!isv) {
        char* k0 = hb;
        const unsigned w0 = cvtpk(acc[0], acc[1]), w1 = cvtpk(acc[2], acc[3]), w2 = cvtpk(acc[4], acc[5]), w3 = cvtpk(acc[6], acc[7]), w4 = cvtpk(acc[8], acc[9]);
        *(u32x2*)(k0 + (l32) * 16 + 8 * hi) = u32x2{w0, w1};
        *(u32x2*)(k0 + (l32 + 32) * 16 + 8 * hi) = u32x2{w2, w3};
        if (hi == 0) *(u32x4*)(hb + 1024 + l32 * 16) = u32x4{w4, 0x00003F80u, 0u, 0u};
        auto sq = [](unsigned x) { const float a = __uint_as_float(x << 16), bq = __uint_as_float(x & 0xFFFF0000u); return a * a + bq * bq; };
        float s2 = sq(w0) + sq(w1) + sq(w2) + sq(w3) + (hi == 0 ? sq(w4) : 0.f);
        s2 = hsum(s2);
#pragma unroll
        for (int o = 16; o >= 1; o >>= 1) s2 = fmaxf(s2, __shfl_xor(s2, o));
        if (lane == 0) *(float*)(p.ws + WS_KMAX + r32 * 4) = s2;
      } else {
#pragma unroll
        for (int g = 0; g < 2; ++g) {
          bf16x8 v = pack8n(acc, g);
          if (l32 == 18) v = bf16x8{0x3F80, 0x3F80, 0x3F80, 0x3F80, 0x3F80, 0x3F80, 0x3F80, 0x3F80};
          if (l32 < 19) *(bf16x8*)(hb + 1536 + ((g * 2 + hi) * 19 + l32) * 16) = v;
        }
      }
    }
  }

}

constexpr float LTHR = 7.9228162514264338e28f;
#define LTHR_V LTHR
struct AttB {
  f32x16 o[4]; bf16x8 qf[8]; f32x16 negm; float m, l;
  f32x16 s0, s1;
  unsigned pw[16];
  bf16x8 fr[4];
  float ps;
};
DI void cmask1(f32x16& s, int key0, int row) {
  const int d = row - key0;
#pragma unroll
  for (int r = 0; r < 16; ++r) { const int c = (r & 3) + 8 * (r >> 2); s[r] = (c > d) ? -INFINITY : s[r]; }
}
DI float max16(const f32x16& s) { float a = fmaxf(fmaxf(s[0], s[1]), s[2]);
#pragma unroll
  for (int r = 3; r < 15; r += 2) a = fmaxf(fmaxf(a, s[r]), s[r + 1]);
  return fmaxf(a, s[15]); }
DI f32x16 splat16(float x) { f32x16 v;
#pragma unroll
  for (int r = 0; r < 16; ++r) v[r] = x;
  return v; }
#define PKB(w) __builtin_bit_cast(bf16x8, w)
#define EXP2(x) __builtin_amdgcn_exp2f(x)
DI bf16x8 pfrag(const AttB& a, int ks) { u32x4 w = {a.pw[4 * ks], a.pw[4 * ks + 1], a.pw[4 * ks + 2], a.pw[4 * ks + 3]}; return __builtin_bit_cast(bf16x8, w); }

template <bool HASCUR> DI void rare_fix(AttB& a, __amdgpu_buffer_rsrc_t sK, __amdgpu_buffer_rsrc_t sQ, unsigned voff, unsigned kso, unsigned qso, int key0, int row, bool mask) {
  float tm = -INFINITY;
  f32x16 t;
#pragma unroll 1
  for (int kb = 0; kb < 2; ++kb) {
    t = f32x16{};
#pragma unroll 2
    for (int ds = 0; ds < 8; ++ds) t = MFMA(bld16s(sK, voff, kso + kb * 8192 + ds * 1024), bld16(sQ, voff, qso + ds * 1024), t);
    if (mask) cmask1(t, key0 + 32 * kb, row);
    tm = fmaxf(tm, max16(t));
  }
  tm = hmax(tm);
  const float mn = fmaxf(a.m, tm), dl = mn - a.m, f = EXP2(-dl);
#pragma unroll
  for (int db = 0; db < 4; ++db)
#pragma unroll
    for (int r = 0; r < 16; ++r) a.o[db][r] *= f;
  float ps = 0.f;
#pragma unroll 1
  for (int kb = 0; kb < 2; ++kb) {
    t = f32x16{};
#pragma unroll 2
    for (int ds = 0; ds < 8; ++ds) t = MFMA(bld16s(sK, voff, kso + kb * 8192 + ds * 1024), bld16(sQ, voff, qso + ds * 1024), t);
    if (mask) cmask1(t, key0 + 32 * kb, row);
#pragma unroll
    for (int r = 0; r < 16; ++r) { t[r] = EXP2(t[r] - mn); ps += t[r]; }
    if (kb == 0) {
#pragma unroll
      for (int k = 0; k < 8; ++k) a.pw[k] = cvtpk(t[2 * k], t[2 * k + 1]);
    } else {
#pragma unroll
      for (int k = 0; k < 8; ++k) a.pw[8 + k] = cvtpk(t[2 * k], t[2 * k + 1]);
    }
  }
  a.l *= f; a.ps = ps;
  if (HASCUR) {
#pragma unroll
    for (int r = 0; r < 16; ++r) a.s0[r] -= dl;
  }
  a.m = mn; a.negm = splat16(-mn); PIN(a.negm);
}

struct StepCtx {
  lds_cptr kp;
  lds_cptr kpn;
  lds_cptr vp;
  unsigned m0k, m0v, sok, sov; bool dk, dv;
  unsigned voff;
};
constexpr int M_STEADY = 0, M_FIRST = 1, M_SWITCH = 2, M_DRAIN = 3;
template <int I, int MODE> DI void gapA(AttB& a, const StepCtx& c, __amdgpu_buffer_rsrc_t sK, __amdgpu_buffer_rsrc_t sV) {
  constexpr bool PREV = MODE != M_FIRST, CUR = MODE != M_DRAIN, NEW = MODE == M_FIRST || MODE == M_SWITCH;
  constexpr int ds = I & 7;
  if constexpr (CUR) {
    if constexpr (I < 8) { if constexpr (ds == 0) a.s0 = NEW ? MFMA(a.fr[I & 3], a.qf[0], f32x16{}) : MFMA(a.fr[I & 3], a.qf[0], a.negm); else a.s0 = MFMA(a.fr[I & 3], a.qf[ds], a.s0); }
    else { if constexpr (ds == 0) a.s1 = NEW ? MFMA(a.fr[I & 3], a.qf[0], f32x16{}) : MFMA(a.fr[I & 3], a.qf[0], a.negm); else a.s1 = MFMA(a.fr[I & 3], a.qf[ds], a.s1); }
  }
  if constexpr (I + 3 < 16) { if constexpr (CUR) a.fr[(I + 3) & 3] = lds_rd16(c.kp + (I + 3) * 1024); }
  else if constexpr (PREV) a.fr[(I + 3) & 3] = lds_rd16(c.vp + (I + 3 - 16) * 1024);
  if constexpr (I < 8) {
    if constexpr (PREV) {
      a.s1[8 + I] = EXP2(a.s1[8 + I]);
      a.ps += a.s1[7 + I];
      if constexpr (I < 4) { a.pw[8 + I] = cvtpk(a.s1[2 * I], a.s1[2 * I + 1]); PIN(a.pw[8 + I]); }
      else if constexpr (I != 6) { constexpr int k = I == 7 ? 6 : I; a.pw[8 + k] = cvtpk(a.s1[2 * k], a.s1[2 * k + 1]); PIN(a.pw[8 + k]); }
    }
    if constexpr (I & 1) { if (MODE != M_DRAIN || c.dv) dma16os<(I >> 1) * 1024>(c.m0v, c.voff, sV, c.sov); }
  } else {
    constexpr int i = I - 8;
    if constexpr (MODE == M_STEADY) {
      a.s0[i] = EXP2(a.s0[i]);
      if constexpr (i >= 1) a.ps += a.s0[i - 1];
    }
    if constexpr (i & 1) { if (MODE != M_DRAIN || c.dk) dma16os<(i >> 1) * 1024>(c.m0k, c.voff, sK, c.sok); }
  }
  PIN(a.ps);
  SBAR();
}
template <int I, int MODE> DI void gapB(AttB& a, const StepCtx& c, float& mx0, float& mx1) {
  constexpr bool PREV = MODE != M_FIRST, NEW = MODE == M_FIRST || MODE == M_SWITCH;
  constexpr int ks = I >> 2, db = I & 3;
  if constexpr (PREV) {
    a.o[db] = MFMA(a.fr[I & 3], pfrag(a, ks), a.o[db]);
    if constexpr (I + 3 < 16) a.fr[(I + 3) & 3] = lds_rd16(c.vp + (I + 3) * 1024);
  }
  if constexpr (I + 3 >= 16) a.fr[(I + 3) & 3] = lds_rd16(c.kpn + (I + 3 - 16) * 1024);
  if constexpr (MODE == M_STEADY) {
    if constexpr (I < 8) {
      a.s0[8 + I] = EXP2(a.s0[8 + I]);
      a.ps += a.s0[7 + I];
      if constexpr (I >= 4) { constexpr int k = I - 4; a.pw[k] = cvtpk(a.s0[2 * k], a.s0[2 * k + 1]); PIN(a.pw[k]); }
    } else {
      constexpr int i = I - 8;
      a.s1[i] = EXP2(a.s1[i]);
      if constexpr (i == 0) a.ps += a.s0[15]; else a.ps += a.s1[i - 1];
      if constexpr (I < 12) { constexpr int k = I - 4; a.pw[k] = cvtpk(a.s0[2 * k], a.s0[2 * k + 1]); PIN(a.pw[k]); }
    }
  } else if constexpr (NEW) {
    if constexpr (I < 4) {
      constexpr int r = 4 * I;
      if constexpr (I == 0) { mx0 = fmaxf(fmaxf(a.s0[0], a.s0[1]), a.s0[2]); mx1 = fmaxf(fmaxf(a.s1[0], a.s1[1]), a.s1[2]); mx0 = fmaxf(mx0, a.s0[3]); mx1 = fmaxf(mx1, a.s1[3]); }
      else { mx0 = fmaxf(fmaxf(mx0, a.s0[r]), a.s0[r + 1]); mx1 = fmaxf(fmaxf(mx1, a.s1[r]), a.s1[r + 1]); mx0 = fmaxf(fmaxf(mx0, a.s0[r + 2]), a.s0[r + 3]); mx1 = fmaxf(fmaxf(mx1, a.s1[r + 2]), a.s1[r + 3]); }
      PIN(mx0); PIN(mx1);
    } else {
      if constexpr (I == 4) { mx0 = fmaxf(hmax(fmaxf(mx0, mx1)), -1e30f); a.m = mx0; a.ps = 0.f; }
      if constexpr (I < 12) {
        constexpr int i = 2 * (I - 4);
        a.s0[i] = EXP2(a.s0[i] - mx0); a.s0[i + 1] = EXP2(a.s0[i + 1] - mx0);
        if constexpr (i >= 2) { a.ps += a.s0[i - 2]; a.ps += a.s0[i - 1]; }
      } else {
        constexpr int i = 2 * (I - 12);
        if constexpr (I == 12) { a.ps += a.s0[14]; a.ps += a.s0[15]; }
        a.s1[i] = EXP2(a.s1[i] - mx0); a.s1[i + 1] = EXP2(a.s1[i + 1] - mx0);
        a.s1[8 + i] -= mx0; a.s1[9 + i] -= mx0;
        if constexpr (i >= 2) { a.ps += a.s1[i - 2]; a.ps += a.s1[i - 1]; }
      }
      if constexpr (I >= 8) { constexpr int k = I - 8; if constexpr (2 * k + 1 < 2 * (I - 4)) { a.pw[k] = cvtpk(a.s0[2 * k], a.s0[2 * k + 1]); PIN(a.pw[k]); } }
    }
  }
  PIN(a.ps);
  SBAR();
}
template <int O, int MODE, int... Is> DI void phaseA_(AttB& a, const StepCtx& c, __amdgpu_buffer_rsrc_t sK, __amdgpu_buffer_rsrc_t sV, std::integer_sequence<int, Is...>) { (gapA<O + Is, MODE>(a, c, sK, sV), ...); }
template <int MODE, int... Is> DI void phaseB_(AttB& a, const StepCtx& c, float& mx0, float& mx1, std::integer_sequence<int, Is...>) { (gapB<Is, MODE>(a, c, mx0, mx1), ...); }

struct K2P { char* ws; };
struct AttEnv {
  int grp, wq, lane, hi, l32, n1, L, head; char* lds; unsigned lds0; char* ws;
  __amdgpu_buffer_rsrc_t sK, sV, sQ, sC;
};
DI int tile_of(const AttEnv& e, int x) { return e.grp == 0 ? x : (x < e.n1 ? x : 17 + (x - e.n1)); }
DI void step_top(const AttEnv& e, int s, StepCtx& c) {
  asm volatile("s_waitcnt vmcnt(0)\n\ts_barrier" ::: "memory");
  SBAR();
  const unsigned gb = e.grp * 81920;
  const int k3 = s % 3, k3n = (s + 1) % 3, k3d = (s + 2) % 3;
  c.kp = (lds_cptr)e.lds + gb + k3 * 16384 + e.lane * 16;
  c.kpn = (lds_cptr)e.lds + gb + k3n * 16384 + e.lane * 16;
  c.vp = (lds_cptr)e.lds + gb + 49152 + ((s + 1) & 1) * 16384 + e.lane * 16;
  c.dk = (s + 2 < 17); c.dv = (s < 17);
  c.m0k = rfl(e.lds0 + gb + k3d * 16384 + e.wq * 4096); c.m0v = rfl(e.lds0 + gb + 49152 + (s & 1) * 16384 + e.wq * 4096);
  c.sok = rfl((unsigned)(tile_of(e, s + 2) * 16384 + e.wq * 4096)); c.sov = rfl((unsigned)(tile_of(e, s) * 16384 + e.wq * 4096));
  c.voff = e.lane * 16;
}
DI void dma_all(const AttEnv& e, const StepCtx& c) {
  if (c.dk) {
#pragma unroll
    for (int i = 0; i < 4; ++i) dma16s(c.m0k + i * 1024, c.voff, e.sK, c.sok + i * 1024); }
  if (c.dv) {
#pragma unroll
    for (int i = 0; i < 4; ++i) dma16s(c.m0v + i * 1024, c.voff, e.sV, c.sov + i * 1024); }
}
template <bool HASCUR> DI void check_prev(AttB& a, const AttEnv& e, int qb, int prev_t) {
  if (__builtin_expect(__any(!(a.l + a.ps <= LTHR_V)), 0)) {
    const int rmin = qb * 128 + e.wq * 32;
    rare_fix<HASCUR>(a, e.sK, e.sQ, e.lane * 16, rfl((unsigned)(prev_t * 16384)), rfl((unsigned)(((qb == e.L ? 0 : 4) + e.wq) * 8192)),
                     prev_t * 64 + 4 * e.hi, rmin + e.l32, prev_t * 64 + 63 > rmin);
  }
  a.l += a.ps; a.ps = 0.f;
}
DI void store_block(AttB& a, const AttEnv& e, int qb) {
  const float inv = 1.f / hsum(a.l);
#pragma unroll
  for (int db = 0; db < 4; ++db)
#pragma unroll
    for (int g = 0; g < 2; ++g)
      bst16(pack8(a.o[db], g, inv), e.sC, e.lane * 16, rfl((unsigned)(((qb * 4 + e.wq) * 256 + db * 2 + g) * 1024)));
}
template <int MODE> DI void att_step(AttB& a, const AttEnv& e, int s, int qb_prev, int qb, bool loadq = false) {
  constexpr bool PREV = MODE != M_FIRST, CUR = MODE != M_DRAIN, NEW = MODE == M_FIRST || MODE == M_SWITCH;
  StepCtx c; step_top(e, s, c);
  if constexpr (NEW) asm volatile("" : "+v"(a.qf[0]), "+v"(a.qf[1]), "+v"(a.qf[2]), "+v"(a.qf[3]), "+v"(a.qf[4]), "+v"(a.qf[5]), "+v"(a.qf[6]), "+v"(a.qf[7]));
  const int t = tile_of(e, s), rmin = qb * 128 + e.wq * 32;
  const bool mask = t * 64 + 63 > rmin;
  if constexpr (MODE == M_FIRST) {
#pragma unroll
    for (int J = 0; J < 3; ++J) a.fr[J] = lds_rd16(c.kp + J * 1024);
  }
  phaseA_<0, MODE>(a, c, e.sK, e.sV, std::make_integer_sequence<int, 8>{});
  if constexpr (PREV) {
    a.ps += a.s1[15]; a.pw[15] = cvtpk(a.s1[14], a.s1[15]);
    check_prev<MODE == M_STEADY>(a, e, qb_prev, tile_of(e, s - 1));
  }
  if constexpr (MODE == M_STEADY) { if (mask) cmask1(a.s0, t * 64 + 4 * e.hi, rmin + e.l32); }
  SBAR();
  phaseA_<8, MODE>(a, c, e.sK, e.sV, std::make_integer_sequence<int, 8>{});
  if constexpr (CUR) {
    if (mask) { if constexpr (NEW) cmask1(a.s0, t * 64 + 4 * e.hi, rmin + e.l32); cmask1(a.s1, t * 64 + 32 + 4 * e.hi, rmin + e.l32); }
  }
  if constexpr (MODE == M_FIRST || MODE == M_STEADY) {
    if (loadq) {
#pragma unroll
      for (int ds = 0; ds < 8; ++ds)
        asm volatile("buffer_load_dwordx4 %0, %1, %2, %3 offen" : "=v"(a.qf[ds]) : "v"(e.lane * 16), "s"(e.sQ), "s"(rfl((unsigned)(e.wq * 8192 + ds * 1024))) : "memory");
    }
  }
  SBAR();
  float mx0 = 0.f, mx1 = 0.f; const float lold = a.l;
  phaseB_<MODE>(a, c, mx0, mx1, std::make_integer_sequence<int, 16>{});
  if constexpr (MODE == M_FIRST || MODE == M_STEADY) {
    if (loadq) asm volatile("s_waitcnt vmcnt(0)" : "+v"(a.qf[0]), "+v"(a.qf[1]), "+v"(a.qf[2]), "+v"(a.qf[3]), "+v"(a.qf[4]), "+v"(a.qf[5]), "+v"(a.qf[6]), "+v"(a.qf[7]) :: "memory");
  }
  if constexpr (NEW) {
    a.ps += a.s1[6];
    if constexpr (MODE == M_SWITCH) { a.l = lold; store_block(a, e, qb_prev); }
#pragma unroll
    for (int db = 0; db < 4; ++db) a.o[db] = f32x16{};
    a.l = 0.f; a.negm = splat16(-a.m); PIN(a.negm);
  }
}

struct AttA { f32x16 o; bf16x8 qf0, qf1; };
#define E4(sc, i) do { sc[i] = EXP2(sc[i]); sc[i + 1] = EXP2(sc[i + 1]); sc[i + 2] = EXP2(sc[i + 2]); sc[i + 3] = EXP2(sc[i + 3]); } while (0)
#define C2(pw, j, sc, i) do { pw[j] = cvtpk(sc[i], sc[i + 1]); pw[j + 1] = cvtpk(sc[i + 2], sc[i + 3]); } while (0)
DI int TA(int g) { return (g * 64) / NPSTEP; }
DI void a_loop(AttA& A, AttA& B, char* lds, int lane) {
  const int l32 = lane & 31, hi = lane >> 5;
  const int vrow = l32 < 19 ? l32 : (((l32 >= 20 && l32 <= 27)) ? 0 : 4);
  lds_cptr k0p = (lds_cptr)lds + ARING + lane * 16;
  lds_cptr k1p = (lds_cptr)lds + ARING + 1024 + l32 * 16;
  lds_cptr vp = (lds_cptr)lds + ARING + 1536 + (hi * 19 + vrow) * 16;
  f32x16 sA, sB, tA, tB; u32x4 pa0, pa1, pb0, pb1, qa0, qa1, qb0, qb1;
  bf16x8 k0, k1, v0, v1, w0, w1;
  int g = 0;
#define SLOT(kt) (((kt) % 10) * ATB)
#define LDK(kt) do { const int so_ = SLOT(kt); k0 = lds_rd16(k0p + so_); k1 = lds_rd16(k1p + so_); } while (0)
#define TOPCHK(kt) do { if (g < NPSTEP && TA(g) == (kt)) { asm volatile("s_barrier" ::: "memory"); SBAR(); ++g; } } while (0)
#define DMP(i) do { } while (0)
#define ASTEP(SC, SN, TC, TN, PP0, PP1, PN0, PN1, QP0, QP1, QN0, QN1, kt, VC0, VC1, VN0, VN1, DOQK, DOPV, DOLDK) do { \
    TOPCHK(kt); \
    const int sv_ = SLOT(kt); \
    if (DOQK) { SN = MFMA(k0, A.qf0, f32x16{}); } SBAR(); VN0 = lds_rd16(vp + sv_); VN1 = lds_rd16(vp + sv_ + 608); E4(SC, 0); PIN(SC); DMP(0); SBAR(); \
    if (DOQK) { TN = MFMA(k0, B.qf0, f32x16{}); } SBAR(); E4(TC, 0); PIN(TC); DMP(1); SBAR(); \
    if (DOPV) { A.o = MFMA(VC0, PKB(PP0), A.o); } SBAR(); E4(SC, 4); C2(PN0, 0, SC, 0); PIN(SC); PIN(PN0); DMP(2); SBAR(); \
    if (DOPV) { B.o = MFMA(VC0, PKB(QP0), B.o); } SBAR(); E4(TC, 4); C2(QN0, 0, TC, 0); PIN(TC); PIN(QN0); DMP(3); SBAR(); \
    if (DOQK) { SN = MFMA(k1, A.qf1, SN); } SBAR(); E4(SC, 8); C2(PN0, 2, SC, 4); PIN(SC); PIN(PN0); DMP(4); SBAR(); \
    if (DOQK) { TN = MFMA(k1, B.qf1, TN); } SBAR(); if (DOLDK) LDK((kt) + 2); E4(TC, 8); C2(QN0, 2, TC, 4); PIN(TC); PIN(QN0); DMP(5); SBAR(); \
    if (DOPV) { A.o = MFMA(VC1, PKB(PP1), A.o); } SBAR(); E4(SC, 12); C2(PN1, 0, SC, 8); C2(PN1, 2, SC, 12); PIN(SC); PIN(PN1); DMP(6); SBAR(); \
    if (DOPV) { B.o = MFMA(VC1, PKB(QP1), B.o); } SBAR(); E4(TC, 12); C2(QN1, 0, TC, 8); C2(QN1, 2, TC, 12); PIN(TC); PIN(QN1); DMP(7); SBAR(); } while (0)
  pa0 = u32x4{0,0,0,0}; pa1 = pa0; pb0 = pa0; pb1 = pa0; qa0 = pa0; qa1 = pa0; qb0 = pa0; qb1 = pa0; v0 = bf16x8{}; v1 = v0; w0 = v0; w1 = v0;
  asm volatile("s_barrier" ::: "memory"); SBAR();
  LDK(0); sA = MFMA(k0, A.qf0, f32x16{}); tA = MFMA(k0, B.qf0, f32x16{}); sA = MFMA(k1, A.qf1, sA); tA = MFMA(k1, B.qf1, tA); LDK(1);
  g = 1;
#define TOPCHK0(kt) do { } while (0)
  {
#pragma push_macro("TOPCHK")
#undef TOPCHK
#define TOPCHK(kt) TOPCHK0(kt)
    ASTEP(sA, sB, tA, tB, pb0, pb1, pa0, pa1, qb0, qb1, qa0, qa1, 0, v0, v1, w0, w1, true, false, true);
#pragma pop_macro("TOPCHK")
  }
#pragma unroll 1
  for (int kt = 1; kt < 61; kt += 2) {
    ASTEP(sB, sA, tB, tA, pa0, pa1, pb0, pb1, qa0, qa1, qb0, qb1, kt, w0, w1, v0, v1, true, true, true);
    ASTEP(sA, sB, tA, tB, pb0, pb1, pa0, pa1, qb0, qb1, qa0, qa1, kt + 1, v0, v1, w0, w1, true, true, true);
  }
  ASTEP(sB, sA, tB, tA, pa0, pa1, pb0, pb1, qa0, qa1, qb0, qb1, 61, w0, w1, v0, v1, true, true, true);
  ASTEP(sA, sB, tA, tB, pb0, pb1, pa0, pa1, qb0, qb1, qa0, qa1, 62, v0, v1, w0, w1, true, true, false);
  ASTEP(sB, sA, tB, tA, pa0, pa1, pb0, pb1, qa0, qa1, qb0, qb1, 63, w0, w1, v0, v1, false, true, false);
  A.o = MFMA(v0, PKB(pb0), A.o); B.o = MFMA(v0, PKB(qb0), B.o); A.o = MFMA(v1, PKB(pb1), A.o); B.o = MFMA(v1, PKB(qb1), B.o);
#undef LDK
#undef ASTEP
#undef TOPCHK
#undef TOPCHK0
#undef DMP
#undef SLOT
}
DI void a_initq(AttA& A, const char* ws, int qrow, int ah, int lane, float km) {
  const int hi = lane >> 5;
  const char* qp = ws + WS_QA + (size_t)qrow * 1152 + ah * 36;
  u32x4 q0; { const unsigned* qq = (const unsigned*)(qp + 16 * hi); q0 = u32x4{qq[0], qq[1], qq[2], qq[3]}; }
  const unsigned q16 = *(const unsigned*)(qp + 32);
  float qs = 0.f;
#pragma unroll
  for (int e = 0; e < 4; ++e) { const float x = __uint_as_float(q0[e] << 16), y = __uint_as_float(q0[e] & 0xFFFF0000u); qs += x * x + y * y; }
  if (hi == 0) { const float x = __uint_as_float(q16 << 16), y = __uint_as_float(q16 & 0xFFFF0000u); qs += x * x + y * y; }
  qs = hsum(qs);
  const float bound = sqrtf(qs * km) * 1.0009765625f + 1e-30f;
  const unsigned bb = (__float_as_uint(bound) + 0xFFFFu) >> 16;
  A.qf0 = __builtin_bit_cast(bf16x8, q0);
  A.qf1 = __builtin_bit_cast(bf16x8, hi == 0 ? u32x4{q16, bb | 0x8000u, 0u, 0u} : u32x4{0u, 0u, 0u, 0u});
  A.o = f32x16{};
}
DI void a_finish(AttA& A, char* ws, int lane, int qrow, int ah) {
  const int l32 = lane & 31, hi = lane >> 5;
  f32x16 o = A.o;
  float l = o[10];
  { auto rr = __builtin_amdgcn_permlane32_swap(__float_as_uint(l), __float_as_uint(l), false, false); l = __uint_as_float(rr[0]); }
  if (__builtin_expect(__any(!(l >= 8.6736174e-19f)), 0)) {
    float m = -1e30f; o = f32x16{};
    bf16x8 qz = A.qf1; qz[2] = 0;
    const int vrow = l32 < 19 ? l32 : 0;
    for (int kt = 0; kt < 64; ++kt) {
      const char* hb = ws + WS_KA + (size_t)kt * ATB;
      const bf16x8 k0 = *(const bf16x8*)(hb + lane * 16), k1 = *(const bf16x8*)(hb + 1024 + l32 * 16);
      const bf16x8 v0 = *(const bf16x8*)(hb + 1536 + (hi * 19 + vrow) * 16), v1 = *(const bf16x8*)(hb + 1536 + 608 + (hi * 19 + vrow) * 16);
      f32x16 sc = MFMA(k0, A.qf0, f32x16{});
      sc = MFMA(k1, qz, sc);
      const float mn = fmaxf(m, hmax(max16(sc))), f = EXP2(m - mn);
      m = mn;
#pragma unroll
      for (int r = 0; r < 16; ++r) { sc[r] = EXP2(sc[r] - mn); o[r] *= f; }
      o = MFMA(v0, pack8n(sc, 0), o);
      o = MFMA(v1, pack8n(sc, 1), o);
    }
    l = o[10];
    auto rr = __builtin_amdgcn_permlane32_swap(__float_as_uint(l), __float_as_uint(l), false, false); l = __uint_as_float(rr[0]);
  }
  const float inv = 1.f / l;
  char* ob = ws + WS_OA + (size_t)(qrow >> 5) * 36 * 1024 + l32 * 16;
  auto st2 = [&](int d, float x, float y) { const int c = ah * 18 + d; *(unsigned*)(ob + (c >> 4) * 1024 + ((c >> 2) & 1) * 512 + (((c >> 3) & 1) * 4 + (c & 3)) * 2) = cvtpk(x * inv, y * inv); };
  st2(4 * hi, o[0], o[1]); st2(4 * hi + 2, o[2], o[3]); st2(8 + 4 * hi, o[4], o[5]); st2(10 + 4 * hi, o[6], o[7]);
  if (hi == 0) st2(16, o[8], o[9]);
}

__global__ __launch_bounds__(512, 2) void kb_main(K1P p) {
  extern __shared__ __attribute__((aligned(16))) char lds[];
  const int tid = threadIdx.x, lane = tid & 63, l32 = lane & 31, hi = lane >> 5;
  const int b = blockIdx.x, xcd = b & 7, ii = b >> 3, head = xcd * 4 + (ii & 3), j = ii >> 2;
  const int w = rfl(tid >> 6);
  const unsigned lds0 = (unsigned)(size_t)(lds_ptr)lds, voff = lane * 16;
  const int grp = w >> 2, wq = w & 3, pp = j;
  char* flagw = p.ws + WS_FLAG + (head * 8 + j) * 128;
  STAMP(p.ws, 0, 0);
  AttB a;
  if (grp == 0) {
    AttA A, B;
    float km = *(const float*)(p.ws + WS_KMAX + lane * 4);
#pragma unroll
    for (int o = 32; o >= 1; o >>= 1) km = fmaxf(km, __shfl_xor(km, o));
    const int aqrow = j * 256 + wq * 64 + l32;
    a_initq(A, p.ws, aqrow, head, lane, km); a_initq(B, p.ws, aqrow + 32, head, lane, km);
    PIN(A.qf0); PIN(A.qf1); PIN(B.qf0); PIN(B.qf1);
    unsigned wv[7] = {0, 0, 0, 0, 0, 0, 0};
    if (WARM) {
      const char* src[7] = {(const char*)p.cx, (const char*)p.wuk, (const char*)p.wuv, (const char*)p.bx, (const char*)p.wq, (const char*)p.wo, (const char*)p.awo};
      const unsigned nline[7] = {2048u * 1024 / 128, 512u * 8192 / 128, 512u * 8192 / 128, 2048u * 1152 / 128, 576u * 8192 / 128, 4096u * 1152 / 128, 576u * 1152 / 128};
#pragma unroll
      for (int a7 = 0; a7 < 7; ++a7) {
        const unsigned i = (unsigned)(b * 192 + tid);
        if (tid < 192 && i < nline[a7]) wv[a7] = *(const unsigned*)(src[a7] + (size_t)i * 128);
      }
    }
    STAMP(p.ws, 0, 6);
    a_loop(A, B, lds, lane);
    if ((wv[0] ^ wv[1] ^ wv[2] ^ wv[3] ^ wv[4] ^ wv[5] ^ wv[6]) == 0x9E3779B9u) *(unsigned*)(p.ws + WS_FLAG + 256 * 128 + 64) = 1u;

    STAMP(p.ws, 0, 7);
    a_finish(A, p.ws, lane, aqrow, head); a_finish(B, p.ws, lane, aqrow + 32, head);
    STAMP(p.ws, 0, 8);
    if (w == 0) {
      const __attribute__((address_space(1))) unsigned* fl = (const __attribute__((address_space(1))) unsigned*)(p.ws + WS_FLAG + (head * 8 + (lane & 7)) * 128);
      for (unsigned spin = 0; spin < (1u << 20); ++spin) {
        const unsigned v = __hip_atomic_load(fl, __ATOMIC_RELAXED, __HIP_MEMORY_SCOPE_AGENT);
        if (__all(v == 1u)) break;
        __builtin_amdgcn_s_sleep(2);
      }
      __builtin_amdgcn_fence(__ATOMIC_ACQUIRE, "wavefront");
    }
    asm volatile("s_waitcnt vmcnt(0) lgkmcnt(0)\n\ts_barrier" ::: "memory");
  } else {
    __builtin_amdgcn_s_setprio(PPRIO);
    PG c;
    const int gr = wq >> 1, gc = wq & 1;
    c.lds0 = lds0; c.aw = wq; c.voff = voff;
    { const int r = lane >> 3, chp = lane & 7;
#pragma unroll
      for (int par = 0; par < 2; ++par) { const int ch = chp ^ ((4 * par + (r >> 1)) & 7); c.xoC[par] = (unsigned)(r * 1024 + ch * 16); c.xoX[par] = (unsigned)(r * 1152 + ch * 16); }
      c.woff = (unsigned)(((lane & 31) >> 2) * 8192 + (32 * (lane >> 5) + 8 * (lane & 3)) * 2); }
    c.sC = mksrd((const char*)p.cx + (size_t)j * 256 * 1024, 256 * 1024);
    c.sX = mksrd((const char*)p.bx, 2048 * 1152);
    c.rowL = (unsigned)((15 - j) * 128 * 1152); c.rowS = (unsigned)(j * 128 * 1152);
    c.sUK = mksrd((const char*)p.wuk + head * 256, 512 * 8192 - head * 256);
    c.sUV = mksrd((const char*)p.wuv + head * 256, 512 * 8192 - head * 256);
    c.sWQ = mksrd((const char*)p.wq + head * 256, 576 * 8192 - head * 256);
    c.sA = mksrd(p.ws + WS_KA, 64 * ATB);
    {
      auto da = [&](int q) { if (q > 14) q = 14; dma16(rfl(lds0 + ARING + (q / 3) * ATB + (q % 3) * 1024), voff, c.sA, rfl((unsigned)((q / 3) * ATB + (q % 3) * 1024))); };
      auto dw = [&](int st) { const unsigned m0 = rfl(lds0 + WRING + st * WSLOT + wq * 2048), so = rfl((unsigned)((st * 32 + wq * 8) * 8192)); dma16(m0, c.woff, c.sUK, so); dma16(m0 + 1024, c.woff + 128, c.sUK, so); };
      auto dx = [&](int T) {
#pragma unroll
        for (int half = 0; half < 2; ++half)
#pragma unroll
          for (int i = 0; i < 4; ++i)
            dma16(rfl(lds0 + T * XSLOT + (16 * half + 4 * wq + i) * 1024), c.xoC[i & 1], c.sC, rfl((unsigned)((128 * half + 32 * wq + 8 * i) * 1024 + T * 128))); };
      { const int q0 = 3 * wq; da(q0 < 9 ? q0 : 0); da(q0 + 1 < 9 ? q0 + 1 : 1); da(q0 + 2 < 9 ? q0 + 2 : 2); }
      dw(0); dx(0); dw(1); dw(2); dx(1);
      da(9 + 2 * wq); da(10 + 2 * wq);
    }
    const int blk = (lane >> 4) & 1, q4 = (lane & 15) >> 2, p4 = lane & 3;
#pragma unroll
    for (int s = 0; s < 4; ++s) c.xb[s] = (lds_cptr)lds + (gr * 4) * 4096 + 128 * l32 + 16 * ((2 * s + hi) ^ ((l32 >> 1) & 7));
    c.wb = (lds_cptr)lds + WRING + (2 * gc) * 512 + 2048 * hi + 64 * q4 + 32 * blk + 8 * p4;
    f32x16 acc[4][2]; FragKV fa, fb; bf16x8 kvp[16]; KVSt ks; ks.voff = voff;
#define ZACC() do { _Pragma("unroll") for (int r = 0; r < 4; ++r) { acc[r][0] = f32x16{}; acc[r][1] = f32x16{}; } } while (0)
#define PACKKV() do { _Pragma("unroll") for (int r = 0; r < 4; ++r) _Pragma("unroll") for (int ct = 0; ct < 2; ++ct) _Pragma("unroll") for (int g8 = 0; g8 < 2; ++g8) kvp[r * 4 + ct * 2 + g8] = pack8n(acc[r][ct], g8); SBAR(); } while (0)
#pragma unroll
    for (int i = 0; i < 16; ++i) kvp[i] = bf16x8{};
    ks.srd = mksrd(p.ws + WS_KF + ((size_t)(head * 64 + 8 * j + 4 * gr) * 8 + 4 * gc) * 1024, 65536);
    ZACC();
    GLoop<0, 0, 2>::run(c, acc, fa, fb, kvp, ks, flagw);
    GLoop<0, 2, 16>::run(c, acc, fa, fb, kvp, ks, flagw);
    PACKKV(); ZACC();
    STAMP(p.ws, 0, 1);
    GLoop<1, 16, 32>::run(c, acc, fa, fb, kvp, ks, flagw);
    PACKKV(); ZACC();
    ks.srd = mksrd(p.ws + WS_VF + ((size_t)(head * 32 + 4 * j + 2 * gr) * 16 + 2 * gc) * 1024, 65536);
    STAMP(p.ws, 0, 5);
    GLoop<2, 32, 50>::run(c, acc, fa, fb, kvp, ks, flagw);
    __builtin_amdgcn_s_setprio(0);
    STAMP(p.ws, 0, 2);
    asm volatile("s_waitcnt vmcnt(0) lgkmcnt(0)\n\ts_barrier" ::: "memory");
#pragma unroll
    for (int r = 0; r < 4; ++r)
#pragma unroll
      for (int ct = 0; ct < 2; ++ct)
#pragma unroll
        for (int g8 = 0; g8 < 2; ++g8) {
          const int fi = (4 * gr + r) * 8 + 2 * (2 * gc + ct) + g8;
          *(bf16x8*)(lds + fi * 1024 + lane * 16) = pack8(acc[r][ct], g8, CB);
        }
  }
  {
    const __amdgpu_buffer_rsrc_t sQLw = mksrd(p.ws + WS_QF + (size_t)b * 65536, 65536);
    asm volatile("s_waitcnt lgkmcnt(0)\n\ts_barrier" ::: "memory");
#pragma unroll
    for (int ds = 0; ds < 8; ++ds) a.qf[ds] = lds_rd16((lds_cptr)lds + ((grp * 4 + wq) * 8 + ds) * 1024 + lane * 16);
    STAMP(p.ws, 0, 3);
    if (w == 0) asm volatile("s_waitcnt vmcnt(0)" ::: "memory");
    asm volatile("s_waitcnt lgkmcnt(0)\n\ts_barrier" ::: "memory");
#pragma unroll
    for (int ds = 0; ds < 8; ++ds) bst16(a.qf[ds], sQLw, voff, (unsigned)(((grp * 4 + wq) * 8 + ds) * 1024));
    STAMP(p.ws, 0, 4);
  }
  const int L = 15 - pp, n1 = 2 * pp + 2;
  STAMP(p.ws, 0, 9);
  {
  AttEnv e; e.grp = grp; e.wq = wq; e.lane = lane; e.hi = hi; e.l32 = l32; e.n1 = n1; e.L = L; e.head = head; e.lds = lds; e.lds0 = lds0; e.ws = p.ws;
  e.sK = mksrd(p.ws + WS_KF + (size_t)head * S * 256, S * 256);
  e.sV = mksrd(p.ws + WS_VF + (size_t)head * S * 256, S * 256);
  e.sQ = mksrd(p.ws + WS_QF + (size_t)b * 65536, 65536);
  e.sC = mksrd(p.ws + WS_CF + (size_t)head * 8192, 64 * 256 * 1024 - head * 8192);
  int qb = grp == 0 ? L : pp;
  {
    const unsigned gb = grp * 81920;
#pragma unroll
    for (int t = 0; t < 2; ++t)
#pragma unroll
      for (int i = 0; i < 4; ++i) dma16s(rfl(lds0 + gb + t * 16384 + wq * 4096 + i * 1024), voff, e.sK, rfl((unsigned)(t * 16384 + wq * 4096 + i * 1024)));
  }
  if (grp == BPRIO_G) __builtin_amdgcn_s_setprio(BPRIO);
  {
    const int send0 = grp == 0 ? 17 : n1;
    const int rmin0 = qb * 128 + wq * 32;
    int nt0 = ((rmin0 + 31) >> 6) + 1; if (nt0 > send0) nt0 = send0;
    int nt1 = 0;
    if (grp == 1) { const int rminL = L * 128 + wq * 32; nt1 = ((rminL + 31) >> 6) - 17 + 1; if (nt1 > 17 - n1) nt1 = 17 - n1; if (nt1 < 0) nt1 = 0; }
    a.l = 0.f; a.ps = 0.f; a.m = -1e30f;
#pragma unroll
    for (int db = 0; db < 4; ++db) a.o[db] = f32x16{};
    int s = 0; bool carry = false; int qprev = qb;
    const int nseg = grp == 0 ? 1 : 2;
#pragma unroll 1
    for (int seg = 0; seg < nseg; ++seg) {
      const int nt = seg == 0 ? nt0 : nt1;
      const int send = seg == 0 ? send0 : 17;
      const bool shortblk = grp == 1 && seg == 0;
      if (nt > 0) {
        const int slast = s + nt;
        if (carry) att_step<M_SWITCH>(a, e, s, qprev, qb); else att_step<M_FIRST>(a, e, s, qb, qb, shortblk && nt == 1);
        for (++s; s < slast; ++s) att_step<M_STEADY>(a, e, s, qb, qb, shortblk && s == slast - 1);
        carry = shortblk && s == send && nt1 > 0;
        if (!carry) {
          att_step<M_DRAIN>(a, e, s, qb, qb); ++s;
          if (shortblk) {
            store_block(a, e, qb);
#pragma unroll
            for (int db = 0; db < 4; ++db) a.o[db] = f32x16{};
            a.l = 0.f; a.ps = 0.f; a.m = -1e30f;
          }
        }
      }
      for (; s < send; ++s) { StepCtx sc; step_top(e, s, sc); dma_all(e, sc); }
      qprev = qb; qb = L;
    }
    for (; s < 18; ++s) { StepCtx sc; step_top(e, s, sc); dma_all(e, sc); }
  }
  STAMP(p.ws, 0, 10);
  asm volatile("s_waitcnt vmcnt(0) lgkmcnt(0)\n\ts_barrier" ::: "memory");
  {
    const float lt = hsum(a.l);
    char* xb = lds + w * 8192;
    float* st = (float*)(lds + 65536) + w * 128;
#pragma unroll
    for (int i = 0; i < 2; ++i)
#pragma unroll
      for (int q = 0; q < 4; ++q) {
        const f32x16& o = grp == 0 ? a.o[2 + i] : a.o[i];
        *(f32x4*)(xb + (i * 4 + q) * 1024 + lane * 16) = f32x4{o[4 * q], o[4 * q + 1], o[4 * q + 2], o[4 * q + 3]};
      }
    st[lane] = a.m; st[64 + lane] = lt;
    __syncthreads();
    const int ow = w ^ 4;
    const char* yb = lds + ow * 8192; const float* so = (const float*)(lds + 65536) + ow * 128;
    const float m2 = so[lane], l2 = so[64 + lane];
    const float mn = fmaxf(a.m, m2), f1 = EXP2(a.m - mn), f2 = EXP2(m2 - mn);
    const float inv = 1.f / (lt * f1 + l2 * f2);
    const float g1 = f1 * inv, g2 = f2 * inv;
#pragma unroll
    for (int i = 0; i < 2; ++i) {
      f32x16 o = grp == 0 ? a.o[i] : a.o[2 + i];
#pragma unroll
      for (int q = 0; q < 4; ++q) { const f32x4 y = *(const f32x4*)(yb + (i * 4 + q) * 1024 + lane * 16);
#pragma unroll
        for (int e = 0; e < 4; ++e) o[4 * q + e] = o[4 * q + e] * g1 + y[e] * g2; }
      const int db = (grp == 0 ? 0 : 2) + i;
#pragma unroll
      for (int g = 0; g < 2; ++g)
        bst16(pack8n(o, g), e.sC, voff, rfl((unsigned)(((L * 4 + wq) * 256 + db * 2 + g) * 1024)));
    }
  }
  }
  STAMP(p.ws, 0, 11);
}

struct K3P { char* ws; const void* wo; const void* awo; void* out; };
constexpr int K3_WS = 24576;
struct K3C {
  unsigned lds0, w, xvoff, wvoff;
  __amdgpu_buffer_rsrc_t sX, sW;
  lds_cptr wb;
};
DI void xload(bf16x8& d, unsigned voff, __amdgpu_buffer_rsrc_t srd, unsigned soff) {
  asm volatile("buffer_load_dwordx4 %0, %1, %2, %3 offen" : "=v"(d) : "v"(voff), "s"(srd), "s"(soff) : "memory");
}
template <int I> DI void k3_dmaW(const K3C& c, int st) {
  dma16(c.lds0 + (st & 3) * K3_WS + (c.w * 3 + I) * 1024, c.wvoff + I * 128, c.sW, (unsigned)((st * 64 + c.w * 8) * 1152));
}
struct K3F { s16x4 wl[3], wh[3]; };
DI void k3_stage(const K3C& c, f32x16 (&acc)[6], bf16x8& x, K3F& fa, K3F& fb, int st, unsigned xso) {
  const int so = (st & 3) * K3_WS, sn = ((st + 1) & 3) * K3_WS;
#pragma unroll
  for (int m = 0; m < 3; ++m) {
    acc[m] = MFMA(cat(fa.wl[m], fa.wh[m]), x, acc[m]);
    lds_cptr p = c.wb + so + (3 + m) * 512;
    fb.wl[m] = lds_tr(p); fb.wh[m] = lds_tr(p + 3072);
    if (m == 0) k3_dmaW<0>(c, st + 3);
    if (m == 2) k3_dmaW<1>(c, st + 3);
    SBAR();
  }
#pragma unroll
  for (int m = 0; m < 3; ++m) {
    acc[3 + m] = MFMA(cat(fb.wl[m], fb.wh[m]), x, acc[3 + m]);
    lds_cptr p = c.wb + sn + m * 512;
    fa.wl[m] = lds_tr(p); fa.wh[m] = lds_tr(p + 3072);
    if (m == 1) k3_dmaW<2>(c, st + 3);
    SBAR();
  }
  xload(x, c.xvoff, c.sX, xso);
  SBAR();
}
__global__ __launch_bounds__(512, 2) void k3_out(K3P p) {
  extern __shared__ __attribute__((aligned(16))) char lds[];
  const int tid = threadIdx.x, lane = tid & 63, l32 = lane & 31, hi = lane >> 5;
  const int b = blockIdx.x;
  const bool isB = b < 192;
  K3C c;
  c.lds0 = (unsigned)(size_t)(lds_ptr)lds; c.w = rfl(tid >> 6);
  const int rt = c.w & 1, kq = c.w >> 1;
  const int blk = (lane >> 4) & 1, q4 = (lane & 15) >> 2, p4 = lane & 3;
  const int nst = isB ? 32 : 9;
  c.xvoff = lane * 16;
  { const int sub = lane >> 5, rr = (lane & 31) >> 2, c4 = lane & 3; c.wvoff = (unsigned)(rr * 1152 + (32 * sub + 8 * c4) * 2); }
  c.wb = (lds_cptr)lds + (2 * kq) * 3072 + 32 * blk + 8 * p4 + (4 * hi + q4) * 64;
  STAMP(p.ws, 2, 0);
  const int ntile = isB ? 1 : ((b - 192) < 32 ? 2 : 1);
#pragma unroll 1
  for (int it = 0; it < ntile; ++it) {
    int t, sk = 0;
    if (isB) { const int x = b & 7, i = b >> 3; t = x * 12 + (i >> 1); sk = i & 1; } else t = (b - 192) + 64 * it;
    const int rb = t / 3, cb = t - rb * 3;
    c.sW = isB ? mksrd((const char*)p.wo + (size_t)sk * 2048 * 1152 + cb * 384, 2048 * 1152 - cb * 384) : mksrd((const char*)p.awo + cb * 384, 576 * 1152 - cb * 384);
    {
      const char* xb = isB ? p.ws + WS_CF + ((size_t)(rb * 2 + rt) * 256 + sk * 128) * 1024 : p.ws + WS_OA + (size_t)(rb * 2 + rt) * 36 * 1024;
      c.sX = mksrd(xb + kq * 1024, nst * 4096 - kq * 1024);
    }
    f32x16 acc[6];
#pragma unroll
    for (int ct = 0; ct < 6; ++ct) acc[ct] = f32x16{};
    bf16x8 x0, x1, x2;
    if (it) asm volatile("s_waitcnt lgkmcnt(0)\n\ts_barrier" ::: "memory");
    k3_dmaW<0>(c, 0); k3_dmaW<1>(c, 0); k3_dmaW<2>(c, 0);
    xload(x0, c.xvoff, c.sX, 0);
    k3_dmaW<0>(c, 1); k3_dmaW<1>(c, 1); k3_dmaW<2>(c, 1);
    xload(x1, c.xvoff, c.sX, 4096);
    k3_dmaW<0>(c, 2); k3_dmaW<1>(c, 2); k3_dmaW<2>(c, 2);
    (void)x2;
    K3F fa, fb;
#define K3_WAIT(N, X) asm volatile("s_waitcnt vmcnt(" #N ")\n\ts_barrier" : "+v"(X) :: "memory")
    K3_WAIT(4, x0); SBAR();
    STAMP(p.ws, 2, 1);
#pragma unroll
    for (int m = 0; m < 3; ++m) { fa.wl[m] = lds_tr(c.wb + m * 512); fa.wh[m] = lds_tr(c.wb + m * 512 + 3072); }
    SBAR();
    int st = 0;
    for (; st + 1 < nst; st += 2) {
      k3_stage(c, acc, x0, fa, fb, st, (unsigned)((st + 2) * 4096));
      K3_WAIT(4, x1); SBAR();
      k3_stage(c, acc, x1, fa, fb, st + 1, (unsigned)((st + 3) * 4096));
      K3_WAIT(4, x0); SBAR();
    }
    if (st < nst) k3_stage(c, acc, x0, fa, fb, st, (unsigned)((st + 2) * 4096));
    STAMP(p.ws, 2, 2);
    asm volatile("s_waitcnt vmcnt(0) lgkmcnt(0)\n\ts_barrier" : "+v"(x0), "+v"(x1) :: "memory");
    f32x4 h[12];
    {
      const bool up = (kq & 2) != 0;
      char* mine = lds + c.w * 12288; const char* theirs = lds + (c.w ^ 4) * 12288;
#pragma unroll
      for (int c3 = 0; c3 < 3; ++c3)
#pragma unroll
        for (int g = 0; g < 4; ++g) {
          const f32x16& give = up ? acc[c3] : acc[3 + c3];
          *(f32x4*)(mine + (c3 * 4 + g) * 1024 + lane * 16) = f32x4{give[4 * g], give[4 * g + 1], give[4 * g + 2], give[4 * g + 3]};
        }
      __syncthreads();
#pragma unroll
      for (int c3 = 0; c3 < 3; ++c3)
#pragma unroll
        for (int g = 0; g < 4; ++g) {
          const f32x16& keep = up ? acc[3 + c3] : acc[c3];
          h[c3 * 4 + g] = f32x4{keep[4 * g], keep[4 * g + 1], keep[4 * g + 2], keep[4 * g + 3]} + *(const f32x4*)(theirs + (c3 * 4 + g) * 1024 + lane * 16);
        }
      __syncthreads();
    }
    f32x4 q[6];
    {
      const bool up = (kq & 1) != 0;
      char* mine = lds + c.w * 6144; const char* theirs = lds + (c.w ^ 2) * 6144;
#pragma unroll
      for (int i = 0; i < 6; ++i) *(f32x4*)(mine + i * 1024 + lane * 16) = up ? h[i] : h[6 + i];
      __syncthreads();
#pragma unroll
      for (int i = 0; i < 6; ++i) q[i] = (up ? h[6 + i] : h[i]) + *(const f32x4*)(theirs + i * 1024 + lane * 16);
    }
    const int it0 = (kq & 2) * 6 + (kq & 1) * 6;
    auto store_out = [&](int coloff) {
      const int row = rb * 64 + rt * 32 + l32;
#pragma unroll
      for (int i = 0; i < 6; ++i) { const int item = it0 + i, ct = item >> 2, g = item & 3;
        *(u32x2*)((char*)p.out + (size_t)row * 2304 + coloff + (cb * 192 + ct * 32 + 8 * g + 4 * hi) * 2) = u32x2{cvtpk(q[i][0], q[i][1]), cvtpk(q[i][2], q[i][3])}; } };
    if (!isB) store_out(0);
    else {
      char* slab = p.ws + WS_KF + (size_t)(t * 2) * 12288;
      if (rt != sk) {
        const __amdgpu_buffer_rsrc_t sP = mksrd(slab + sk * 12288 + kq * 3072, 3072);
#pragma unroll
        for (int i = 0; i < 3; ++i)
          __builtin_amdgcn_raw_buffer_store_b128(u32x4{cvtpk(q[2 * i][0], q[2 * i][1]), cvtpk(q[2 * i][2], q[2 * i][3]), cvtpk(q[2 * i + 1][0], q[2 * i + 1][1]), cvtpk(q[2 * i + 1][2], q[2 * i + 1][3])}, sP, lane * 16 + i * 1024, 0, 16);
        asm volatile("s_waitcnt vmcnt(0)" ::: "memory");
        if (lane == 0) __hip_atomic_fetch_add((__attribute__((address_space(1))) unsigned*)(p.ws + WS_CNT + (t * 2 + sk) * 128), 1u, __ATOMIC_RELAXED, __HIP_MEMORY_SCOPE_AGENT);
      } else {
        const __attribute__((address_space(1))) unsigned* fl = (const __attribute__((address_space(1))) unsigned*)(p.ws + WS_CNT + (t * 2 + (sk ^ 1)) * 128);
        for (unsigned spin = 0; spin < (1u << 20); ++spin) {
          if (__hip_atomic_load(fl, __ATOMIC_RELAXED, __HIP_MEMORY_SCOPE_AGENT) >= 4u) break;
          __builtin_amdgcn_s_sleep(1);
        }
        __builtin_amdgcn_fence(__ATOMIC_ACQUIRE, "wavefront");
        const __amdgpu_buffer_rsrc_t sP = mksrd(slab + (sk ^ 1) * 12288 + kq * 3072, 3072);
#pragma unroll
        for (int i = 0; i < 3; ++i) {
          const u32x4 y = __builtin_bit_cast(u32x4, __builtin_amdgcn_raw_buffer_load_b128(sP, lane * 16 + i * 1024, 0, 16));
          q[2 * i][0] += __uint_as_float(y[0] << 16); q[2 * i][1] += __uint_as_float(y[0] & 0xFFFF0000u); q[2 * i][2] += __uint_as_float(y[1] << 16); q[2 * i][3] += __uint_as_float(y[1] & 0xFFFF0000u);
          q[2 * i + 1][0] += __uint_as_float(y[2] << 16); q[2 * i + 1][1] += __uint_as_float(y[2] & 0xFFFF0000u); q[2 * i + 1][2] += __uint_as_float(y[3] << 16); q[2 * i + 1][3] += __uint_as_float(y[3] & 0xFFFF0000u);
        }
        store_out(1152);
      }
    }
  }
  STAMP(p.ws, 2, 3);
}

extern "C" void kernel_launch(void* const* d_in, const int* in_sizes, int n_in, void* d_out, int out_size, void* d_ws, size_t ws_size, hipStream_t stream) {
  static bool attr = false;
  if (!attr) {
    hipFuncSetAttribute((const void*)k0_aproj, hipFuncAttributeMaxDynamicSharedMemorySize, 147456);
    hipFuncSetAttribute((const void*)kb_main, hipFuncAttributeMaxDynamicSharedMemorySize, 163840);
    hipFuncSetAttribute((const void*)k3_out, hipFuncAttributeMaxDynamicSharedMemorySize, 4 * K3_WS);
    attr = true;
  }
  K1P p1; p1.cx = d_in[6]; p1.bx = d_in[5]; p1.wuk = d_in[8]; p1.wuv = d_in[9]; p1.wq = d_in[7];
  p1.ax = d_in[0]; p1.awq = d_in[1]; p1.awk = d_in[2]; p1.awv = d_in[3]; p1.wo = d_in[10]; p1.awo = d_in[4]; p1.ws = (char*)d_ws;
  k0_aproj<<<256, 512, 147456, stream>>>(p1);
  kb_main<<<256, 512, 163840, stream>>>(p1);
  K3P p3; p3.ws = (char*)d_ws; p3.wo = d_in[10]; p3.awo = d_in[4]; p3.out = d_out;
  k3_out<<<256, 512, 4 * K3_WS, stream>>>(p3);
}
```

```cpp
#include <hip/hip_runtime.h>
#include <hip/hip_bf16.h>
#include <cstdio>
#include <cstdint>
#include <utility>

#define PUBMODE 0
#define KV_ACQ 0
#define BPRIO 1
#define BPRIO_G 1
#define PPRIO 2
#define WARM 1
#define ST_AUX 0
#define DI __device__ __forceinline__
typedef short bf16x8 __attribute__((ext_vector_type(8)));
typedef short s16x4 __attribute__((ext_vector_type(4)));
typedef float f32x16 __attribute__((ext_vector_type(16)));
typedef float f32x4 __attribute__((ext_vector_type(4)));
typedef float f32x2 __attribute__((ext_vector_type(2)));
typedef __bf16 bf16x2_t __attribute__((ext_vector_type(2)));
typedef unsigned u32x4 __attribute__((ext_vector_type(4)));
typedef unsigned u32x2 __attribute__((ext_vector_type(2)));
typedef __attribute__((address_space(3))) const char* lds_cptr;
typedef __attribute__((address_space(3))) char* lds_ptr;
#define SBAR() __builtin_amdgcn_sched_barrier(0)
#define PIN(x) asm volatile("" : "+v"(x))
#define MFMA(a, b, c) __builtin_amdgcn_mfma_f32_32x32x16_bf16(a, b, c, 0, 0, 0)
#define SRD_W3 0x00020000

DI unsigned rfl(unsigned x) { return __builtin_amdgcn_readfirstlane(x); }
DI unsigned cvtpk(float lo, float hi) { f32x2 v = {lo, hi}; bf16x2_t b = __builtin_convertvector(v, bf16x2_t); return __builtin_bit_cast(unsigned, b); }
DI void dma16(unsigned m0, unsigned voff, __amdgpu_buffer_rsrc_t srd, unsigned soff) {
  asm volatile("s_mov_b32 m0, %0\n\ts_nop 0\n\tbuffer_load_dwordx4 %1, %2, %3 offen lds" :: "s"(m0), "v"(voff), "s"(srd), "s"(soff) : "m0", "memory");
}
template <int OFF> DI void dma16o(unsigned m0, unsigned voff, __amdgpu_buffer_rsrc_t srd, unsigned soff) {
  asm volatile("s_mov_b32 m0, %0\n\ts_nop 0\n\tbuffer_load_dwordx4 %1, %2, %3 offen offset:%4 lds" :: "s"(m0), "v"(voff), "s"(srd), "s"(soff), "i"(OFF) : "m0", "memory");
}
DI void dma16s(unsigned m0, unsigned voff, __amdgpu_buffer_rsrc_t srd, unsigned soff) {
  asm volatile("s_mov_b32 m0, %0\n\ts_nop 0\n\tbuffer_load_dwordx4 %1, %2, %3 offen sc1 lds" :: "s"(m0), "v"(voff), "s"(srd), "s"(soff) : "m0", "memory");
}
template <int OFF> DI void dma16os(unsigned m0, unsigned voff, __amdgpu_buffer_rsrc_t srd, unsigned soff) {
  asm volatile("s_mov_b32 m0, %0\n\ts_nop 0\n\tbuffer_load_dwordx4 %1, %2, %3 offen offset:%4 sc1 lds" :: "s"(m0), "v"(voff), "s"(srd), "s"(soff), "i"(OFF) : "m0", "memory");
}
DI bf16x8 bld16s(__amdgpu_buffer_rsrc_t srd, unsigned voff, unsigned soff) { return __builtin_bit_cast(bf16x8, __builtin_amdgcn_raw_buffer_load_b128(srd, voff, soff, 16)); }
DI void dma4(unsigned m0, unsigned voff, __amdgpu_buffer_rsrc_t srd, unsigned soff) {
  asm volatile("s_mov_b32 m0, %0\n\ts_nop 0\n\tbuffer_load_dword %1, %2, %3 offen lds" :: "s"(m0), "v"(voff), "s"(srd), "s"(soff) : "m0", "memory");
}
DI __amdgpu_buffer_rsrc_t mksrd(const void* p, unsigned bytes) { return __builtin_amdgcn_make_buffer_rsrc((void*)p, (short)0, (int)bytes, SRD_W3); }
DI bf16x8 bld16(__amdgpu_buffer_rsrc_t srd, unsigned voff, unsigned soff) { return __builtin_bit_cast(bf16x8, __builtin_amdgcn_raw_buffer_load_b128(srd, voff, soff, 0)); }
DI void bst16(bf16x8 v, __amdgpu_buffer_rsrc_t srd, unsigned voff, unsigned soff) { __builtin_amdgcn_raw_buffer_store_b128(__builtin_bit_cast(u32x4, v), srd, voff + soff, 0, ST_AUX); }
template <int N> DI void wait_bar() { asm volatile("s_waitcnt vmcnt(%0)\n\ts_barrier" :: "i"(N) : "memory"); }
DI bf16x8 lds_rd16(lds_cptr p) { return *(const __attribute__((address_space(3))) bf16x8*)p; }
DI s16x4 lds_tr(lds_cptr p) { return __builtin_bit_cast(s16x4, __builtin_amdgcn_ds_read_tr16_b64_v4i16((__attribute__((address_space(3))) s16x4*)p)); }
DI bf16x8 cat(s16x4 a, s16x4 b) { return __builtin_shufflevector(a, b, 0, 1, 2, 3, 4, 5, 6, 7); }
DI bf16x8 pack8(const f32x16& x, int g, float sc) {
  u32x4 w;
#pragma unroll
  for (int e = 0; e < 4; ++e) w[e] = cvtpk(x[8 * g + 2 * e] * sc, x[8 * g + 2 * e + 1] * sc);
  return __builtin_bit_cast(bf16x8, w);
}
DI bf16x8 pack8n(const f32x16& x, int g) {
  u32x4 w;
#pragma unroll
  for (int e = 0; e < 4; ++e) w[e] = cvtpk(x[8 * g + 2 * e], x[8 * g + 2 * e + 1]);
  return __builtin_bit_cast(bf16x8, w);
}

DI float hmax(float m) { auto rr = __builtin_amdgcn_permlane32_swap(__float_as_uint(m), __float_as_uint(m), false, false); return fmaxf(__uint_as_float(rr[0]), __uint_as_float(rr[1])); }
DI float hsum(float m) { auto rr = __builtin_amdgcn_permlane32_swap(__float_as_uint(m), __float_as_uint(m), false, false); return __uint_as_float(rr[0]) + __uint_as_float(rr[1]); }
constexpr int S = 2048, H = 32, DB = 128, MB = 576, DC = 512, DA = 576, DHA = 18;
constexpr float CB = 0.08838834764831845f * 1.4426950408889634f;
constexpr float CA = 0.23570226039551584f * 1.4426950408889634f;
constexpr size_t WS_KF = 0;
constexpr size_t WS_VF = WS_KF + (size_t)H * S * DB * 2;
constexpr size_t WS_QF = WS_VF + (size_t)H * S * DB * 2;
constexpr size_t WS_CF = WS_QF + (size_t)H * S * DB * 2;
constexpr size_t WS_QA = WS_CF + (size_t)H * S * DB * 2;
constexpr int ATB = 3072;
constexpr size_t WS_KA = WS_QA + (size_t)S * DA * 2 + 256;
constexpr size_t WS_KMAX = WS_KA + 64 * ATB;
constexpr size_t WS_OA = WS_KMAX + 1024;
constexpr size_t WS_PART = WS_OA + (size_t)S * DA * 2;
constexpr size_t WS_FLAG = WS_PART + (size_t)4 * S * 576 * 4;
constexpr size_t WS_CNT = WS_FLAG + 256 * 128 + 1024;
constexpr size_t WS_END = WS_CNT + 192 * 128;

struct K1P {
  const void *cx, *bx, *wuk, *wuv, *wq;
  const void *ax, *awq, *awk, *awv;
  const void *wo, *awo;
  char* ws;
};
constexpr int XSLOT = 32768, WRING = 98304, WSLOT = 8192, ARING = 131072, NPSTEP = 50;
struct PG {
  lds_cptr xb[4];
  lds_cptr wb;
  unsigned lds0, aw;
  unsigned xoC[2], xoX[2], woff, voff;
  __amdgpu_buffer_rsrc_t sC, sX, sUK, sUV, sWQ, sA;
  unsigned rowL, rowS;
};
constexpr int TAc(int g) { return (g * 64) / NPSTEP; }
template <int G, int I> DI void g_piece(const PG& c) {
  if constexpr (I < 2) {
    constexpr int gw = G + 3, pass = gw < 16 ? 0 : (gw < 32 ? 1 : 2), lg = gw - (pass == 0 ? 0 : (pass == 1 ? 16 : 32));
    const unsigned m0 = c.lds0 + WRING + (gw & 3) * WSLOT + c.aw * 2048 + I * 1024, so = (unsigned)(lg * 32 * 8192) + c.aw * 65536;
    dma16(m0, c.woff + I * 128, pass == 0 ? c.sUK : (pass == 1 ? c.sUV : c.sWQ), so);
  } else if constexpr (I < 6) {
    constexpr int i = I - 2, T = (G >> 1) + 2, half = G & 1;
    const unsigned m0 = c.lds0 + (T % 3) * XSLOT + (16 * half + i) * 1024 + c.aw * 4096;
    if constexpr (T < 16) dma16(m0, c.xoC[i & 1], c.sC, (unsigned)((128 * half + 8 * i) * 1024 + (T & 7) * 128) + c.aw * 32768);
    else dma16(m0, c.xoX[i & 1], c.sX, (half ? c.rowS : c.rowL) + (unsigned)(8 * i * 1152 + (T - 16) * 128) + (T > 24 ? 0x40000000u : 0u) + c.aw * (32 * 1152));
  } else {
    constexpr int f0 = TAc(G + 4), nf = (TAc(G + 5) - f0) * 3;
    int q = 2 * c.aw + (I - 6); if (q >= nf) q = q - nf < nf ? q - nf : 0;
    const int kt = f0 + q / 3, part = q % 3;
    dma16(rfl(c.lds0 + ARING + (kt % 10) * ATB + part * 1024), c.voff, c.sA, rfl((unsigned)(kt * ATB + part * 1024)));
  }
}
DI void st16_sc1(bf16x8 v, unsigned voff, __amdgpu_buffer_rsrc_t srd, unsigned soff) {
  asm volatile("buffer_store_dwordx4 %0, %1, %2, %3 offen sc1\n\ts_nop 1" :: "v"(v), "v"(voff), "s"(srd), "s"(soff) : "memory");
}
struct FragKV { bf16x8 x[4]; s16x4 wl[2], wh[2]; };
template <int G, int SS, int I> DI void kv_ld(const PG& c, FragKV& f) {
  constexpr int T = G >> 1;
  lds_cptr xp = c.xb[2 * (G & 1) + SS] + (T % 3) * XSLOT;
  lds_cptr wp = c.wb + (G & 3) * WSLOT + SS * 4096;
  if constexpr (I == 0) f.x[0] = lds_rd16(xp);
  else if constexpr (I == 1) f.wl[0] = lds_tr(wp);
  else if constexpr (I == 2) f.wh[0] = lds_tr(wp + 256);
  else if constexpr (I == 3) f.wl[1] = lds_tr(wp + 512);
  else if constexpr (I == 4) f.wh[1] = lds_tr(wp + 768);
  else f.x[I - 4] = lds_rd16(xp + (I - 4) * 4096);
}
template <bool ISV, int M> DI void kv_mfma(f32x16 (&acc)[4][2], const FragKV& f) {
  constexpr int r = M >> 1, ct = M & 1;
  const bf16x8 wf = cat(f.wl[ct], f.wh[ct]);
  acc[r][ct] = ISV ? MFMA(f.x[r], wf, acc[r][ct]) : MFMA(wf, f.x[r], acc[r][ct]);
}
struct KVSt { __amdgpu_buffer_rsrc_t srd; unsigned voff; };
constexpr int relK(int i) { return ((i >> 2) * 8 + ((i >> 1) & 1) * 2 + (i & 1)) * 1024; }
constexpr int relV(int i) { return (((i >> 2) >> 1) * 16 + (2 * ((i >> 2) & 1) + (i & 1)) * 4 + ((i >> 1) & 1)) * 1024; }
template <bool ISV, int GN, int SN, bool LOAD, int STI, bool PV, int DG, int DB, int M> DI void kv_gap(const PG& c, f32x16 (&acc)[4][2], const FragKV& cur, FragKV& nxt, const bf16x8 (&kvp)[16], const KVSt& ks) {
  kv_mfma<ISV, M>(acc, cur);
  if constexpr (LOAD) kv_ld<GN, SN, M>(c, nxt);
  if constexpr ((M & 1) && DB + (M >> 1) < 8) g_piece<DG, DB + (M >> 1)>(c);
  if constexpr (STI >= 0 && M == 6) st16_sc1(kvp[STI], ks.voff, ks.srd, (unsigned)(PV ? relV(STI) : relK(STI)));
  SBAR();
}
template <bool ISV, int GN, int SN, bool LOAD, int STI, bool PV, int DG, int DB> DI void kv_phase(const PG& c, f32x16 (&acc)[4][2], const FragKV& cur, FragKV& nxt, const bf16x8 (&kvp)[16], const KVSt& ks) {
  kv_gap<ISV, GN, SN, LOAD, STI, PV, DG, DB, 0>(c, acc, cur, nxt, kvp, ks); kv_gap<ISV, GN, SN, LOAD, STI, PV, DG, DB, 1>(c, acc, cur, nxt, kvp, ks);
  kv_gap<ISV, GN, SN, LOAD, STI, PV, DG, DB, 2>(c, acc, cur, nxt, kvp, ks); kv_gap<ISV, GN, SN, LOAD, STI, PV, DG, DB, 3>(c, acc, cur, nxt, kvp, ks);
  kv_gap<ISV, GN, SN, LOAD, STI, PV, DG, DB, 4>(c, acc, cur, nxt, kvp, ks); kv_gap<ISV, GN, SN, LOAD, STI, PV, DG, DB, 5>(c, acc, cur, nxt, kvp, ks);
  kv_gap<ISV, GN, SN, LOAD, STI, PV, DG, DB, 6>(c, acc, cur, nxt, kvp, ks); kv_gap<ISV, GN, SN, LOAD, STI, PV, DG, DB, 7>(c, acc, cur, nxt, kvp, ks);
}
constexpr int nSt(int g) { return (g >= 16 && g < 32) ? 1 : ((g >= 32 && g < 40) ? 2 : 0); }
constexpr int FLAGG = 43;
template <int PASS, int G> DI void g_step(const PG& c, f32x16 (&acc)[4][2], FragKV& fa, FragKV& fb, const bf16x8 (&kvp)[16], const KVSt& ks, char* flag) {
  constexpr int G0 = PASS == 0 ? 0 : (PASS == 1 ? 16 : 32), LG = G - G0;
  if constexpr (G == 0) wait_bar<12>(); else if constexpr (G == 1) wait_bar<8>(); else wait_bar<nSt(G - 2) + 8 + nSt(G - 1)>();
  SBAR();
  if constexpr (G == FLAGG) { if (threadIdx.x == 256) __hip_atomic_store((__attribute__((address_space(1))) unsigned*)flag, 1u, __ATOMIC_RELAXED, __HIP_MEMORY_SCOPE_AGENT); }
  if constexpr (G == 0) {
    kv_ld<0, 0, 0>(c, fa); kv_ld<0, 0, 1>(c, fa); kv_ld<0, 0, 2>(c, fa); kv_ld<0, 0, 3>(c, fa);
    kv_ld<0, 0, 4>(c, fa); kv_ld<0, 0, 5>(c, fa); kv_ld<0, 0, 6>(c, fa); kv_ld<0, 0, 7>(c, fa); SBAR();
  }
  constexpr int STA = PASS == 1 ? LG : ((PASS == 2 && LG < 8) ? 2 * LG : -1);
  constexpr int STB = (PASS == 2 && LG < 8) ? 2 * LG + 1 : -1;
  kv_phase<PASS == 1, G, 1, true, -1, PASS == 2, G, 0>(c, acc, fa, fb, kvp, ks);
  kv_phase<PASS == 1, G + 1, 0, (G + 1 < NPSTEP), -1, PASS == 2, G, 4>(c, acc, fb, fa, kvp, ks);
  if constexpr (STA >= 0) st16_sc1(kvp[STA], ks.voff, ks.srd, (unsigned)(PASS == 2 ? relV(STA) : relK(STA)));
  if constexpr (STB >= 0) st16_sc1(kvp[STB], ks.voff, ks.srd, (unsigned)relV(STB));
  SBAR();
}
template <int PASS, int G, int GEND> struct GLoop { static DI void run(const PG& c, f32x16 (&acc)[4][2], FragKV& fa, FragKV& fb, const bf16x8 (&kvp)[16], const KVSt& ks, char* flag) {
  g_step<PASS, G>(c, acc, fa, fb, kvp, ks, flag); if constexpr (G + 1 < GEND) GLoop<PASS, G + 1, GEND>::run(c, acc, fa, fb, kvp, ks, flag); } };

__global__ __launch_bounds__(512, 2) void k0_aproj(K1P p) {
  extern __shared__ __attribute__((aligned(16))) char lds[];
  const int tid = threadIdx.x, lane = tid & 63, l32 = lane & 31, hi = lane >> 5;
  const int b = blockIdx.x, xcd = b & 7, ii = b >> 3;
  struct { unsigned lds0; } c; c.lds0 = (unsigned)(size_t)(lds_ptr)lds;
  const int w = rfl(tid >> 6);
  const int blk = (lane >> 4) & 1, q4 = (lane & 15) >> 2, p4 = lane & 3;
  if (tid == 0) { *(unsigned*)(p.ws + WS_FLAG + b * 128) = 0u; if (b < 192) *(unsigned*)(p.ws + WS_CNT + b * 128) = 0u; }
  if (ii >= 28) {
    const int t = (b & 7) + 8 * (ii - 28);
    unsigned v = 0;
    { const int i = t * 512 + tid; if (i < 2048 * 2) v ^= *(const unsigned*)((const char*)p.cx + (size_t)(i >> 1) * 1024 + (i & 1) * 128); }
    { const int i = t * 512 + tid; if (i < 96 * 64) v ^= *(const unsigned*)((const char*)p.wuk + (size_t)i * 128); }
    if (v == 0x9E3779B9u) *(unsigned*)(p.ws + WS_FLAG + 256 * 128 + 64) = v;
  }
  if (ii < 20) {
    const int rg = 4 * xcd + ii / 5, cg = ii % 5;
    const __amdgpu_buffer_rsrc_t sAX = mksrd((const char*)p.ax + (size_t)rg * 64 * 1152, 64 * 1152);
    const __amdgpu_buffer_rsrc_t sAQ = mksrd((const char*)p.awq + cg * 256, 576 * 1152 - cg * 256);
    constexpr int ASL = 73728, AWO = 24576;
    const unsigned xv = (unsigned)((lane >> 3) * 1152 + (((lane & 7) ^ ((4 * (w & 1) + (lane >> 4)) & 7)) * 16));
    const unsigned wv = (unsigned)(((lane & 31) >> 2) * 1152 + (32 * (lane >> 5) + 8 * (lane & 3)) * 2);
    auto issue = [&](int th) {
      const unsigned base = c.lds0 + (th & 1) * ASL;
#pragma unroll
      for (int kc = 0; kc < 3; ++kc) dma16(rfl(base + (kc * 8 + w) * 1024), xv, sAX, rfl((unsigned)(w * 8 * 1152 + (th * 3 + kc) * 128)));
#pragma unroll
      for (int i = 0; i < 6; ++i) {
        const int g = 3 * w + (i >> 1), pr = i & 1;
        dma16(rfl(base + AWO + (g * 2 + pr) * 1024), wv, sAQ, rfl((unsigned)((th * 192 + g * 8) * 1152 + pr * 128)));
      }
    };
    const int rt = w >> 2, ct = w & 3, tile = 4 * cg + ct;
    f32x16 acc = f32x16{};
    issue(0); issue(1);
    for (int th = 0; th < 3; ++th) {
      if (th < 2) asm volatile("s_waitcnt vmcnt(9) lgkmcnt(0)\n\ts_barrier" ::: "memory");
      else asm volatile("s_waitcnt vmcnt(0) lgkmcnt(0)\n\ts_barrier" ::: "memory");
      lds_cptr xp = (lds_cptr)lds + (th & 1) * ASL + rt * 4096 + 128 * l32;
      lds_cptr wp = (lds_cptr)lds + (th & 1) * ASL + AWO + ct * 512 + 2048 * hi + 64 * q4 + 32 * blk + 8 * p4;
      bf16x8 xf[4]; s16x4 wl[4], wh[4];
#define ALD(k) do { xf[(k) & 3] = lds_rd16(xp + ((k) >> 2) * 8192 + 16 * ((2 * ((k) & 3) + hi) ^ ((l32 >> 1) & 7))); wl[(k) & 3] = lds_tr(wp + (k) * 4096); wh[(k) & 3] = lds_tr(wp + (k) * 4096 + 256); } while (0)
      if (tile < 18) {
        ALD(0); ALD(1); ALD(2);
#pragma unroll
        for (int k = 0; k < 12; ++k) {
          if (k + 3 < 12) ALD(k + 3);
          acc = MFMA(cat(wl[k & 3], wh[k & 3]), xf[k & 3], acc);
          SBAR();
        }
      }
      if (th == 0) { asm volatile("s_waitcnt lgkmcnt(0)\n\ts_barrier" ::: "memory"); issue(2); }
    }
    const int row = (rg * 2 + rt) * 32 + l32;
    if (tile < 18) {
#pragma unroll
      for (int g = 0; g < 4; ++g) {
        u32x2 v = {cvtpk(acc[4 * g] * CA, acc[4 * g + 1] * CA), cvtpk(acc[4 * g + 2] * CA, acc[4 * g + 3] * CA)};
        *(u32x2*)(p.ws + WS_QA + (size_t)row * 1152 + (tile * 32 + 8 * g + 4 * hi) * 2) = v;
      }
    }
  } else if (ii < 28) {
    const int r32 = 8 * xcd + (ii - 20);
    const __amdgpu_buffer_rsrc_t sAX = mksrd((const char*)p.ax + (size_t)r32 * 32 * 1152, 32 * 1152);
    for (int pc = w; pc < 36; pc += 8) {
      const int kc = pc >> 2, rq = pc & 3, r = lane >> 3, row = 8 * rq + r, ch = (lane & 7) ^ ((row >> 1) & 7);
      dma16(rfl(c.lds0 + pc * 1024), (unsigned)(row * 1152 + ch * 16), sAX, rfl((unsigned)(kc * 128)));
    }
    {
      const unsigned* gk = (const unsigned*)p.awk; const unsigned* gv = (const unsigned*)p.awv;
      unsigned vk[11], vv[11];
#pragma unroll
      for (int it = 0; it < 11; ++it) { const int i = tid + it * 512; const int ic = i < 5184 ? i : 0; vk[it] = gk[ic]; vv[it] = gv[ic]; }
#pragma unroll
      for (int it = 0; it < 11; ++it) {
        const int i = tid + it * 512;
        if (i < 5184) { const int r = i / 9, d = i - r * 9; char* dst = lds + 36864 + (r >> 3) * 1024 + (r & 7) * 64 + d * 4; *(unsigned*)dst = vk[it]; *(unsigned*)(dst + 512) = vv[it]; }
      }
    }
    asm volatile("s_waitcnt vmcnt(0) lgkmcnt(0)\n\ts_barrier" ::: "memory");
    const int isv = w >> 2, kq = w & 3;
    f32x16 acc = f32x16{};
    lds_cptr xp = (lds_cptr)lds + 128 * l32;
    lds_cptr wp = (lds_cptr)lds + 36864 + isv * 512 + 1024 * hi + 64 * q4 + 32 * blk + 8 * p4;
#pragma unroll
    for (int k9 = 0; k9 < 9; ++k9) {
      const int k = kq * 9 + k9;
      const bf16x8 xf = lds_rd16(xp + (k >> 2) * 4096 + 16 * ((2 * (k & 3) + hi) ^ ((l32 >> 1) & 7)));
      const bf16x8 wf = cat(lds_tr(wp + k * 2048), lds_tr(wp + k * 2048 + 256));
      if (isv) acc = MFMA(xf, wf, acc); else acc = MFMA(wf, xf, acc);
    }
    asm volatile("s_waitcnt lgkmcnt(0)\n\ts_barrier" ::: "memory");
    float* red = (float*)lds + w * 1024;
#pragma unroll
    for (int q = 0; q < 4; ++q) *(f32x4*)(red + q * 256 + lane * 4) = f32x4{acc[4 * q], acc[4 * q + 1], acc[4 * q + 2], acc[4 * q + 3]};
    __syncthreads();
    if (kq == 0) {
#pragma unroll
      for (int o = 1; o < 4; ++o)
#pragma unroll
        for (int q = 0; q < 4; ++q) { const f32x4 y = *(const f32x4*)(red + o * 1024 + q * 256 + lane * 4); acc[4 * q] += y[0]; acc[4 * q + 1] += y[1]; acc[4 * q + 2] += y[2]; acc[4 * q + 3] += y[3]; }
      const int row = r32 * 32 + l32;
      char* hb = p.ws + WS_KA + (size_t)r32 * ATB;
      if (!isv) {
        char* k0 = hb;
        const unsigned w0 = cvtpk(acc[0], acc[1]), w1 = cvtpk(acc[2], acc[3]), w2 = cvtpk(acc[4], acc[5]), w3 = cvtpk(acc[6], acc[7]), w4 = cvtpk(acc[8], acc[9]);
        *(u32x2*)(k0 + (l32) * 16 + 8 * hi) = u32x2{w0, w1};
        *(u32x2*)(k0 + (l32 + 32) * 16 + 8 * hi) = u32x2{w2, w3};
        if (hi == 0) *(u32x4*)(hb + 1024 + l32 * 16) = u32x4{w4, 0x00003F80u, 0u, 0u};
        auto sq = [](unsigned x) { const float a = __uint_as_float(x << 16), bq = __uint_as_float(x & 0xFFFF0000u); return a * a + bq * bq; };
        float s2 = sq(w0) + sq(w1) + sq(w2) + sq(w3) + (hi == 0 ? sq(w4) : 0.f);
        s2 = hsum(s2);
#pragma unroll
        for (int o = 16; o >= 1; o >>= 1) s2 = fmaxf(s2, __shfl_xor(s2, o));
        if (lane == 0) *(float*)(p.ws + WS_KMAX + r32 * 4) = s2;
      } else {
#pragma unroll
        for (int g = 0; g < 2; ++g) {
          bf16x8 v = pack8n(acc, g);
          if (l32 == 18) v = bf16x8{0x3F80, 0x3F80, 0x3F80, 0x3F80, 0x3F80, 0x3F80, 0x3F80, 0x3F80};
          if (l32 < 19) *(bf16x8*)(hb + 1536 + ((g * 2 + hi) * 19 + l32) * 16) = v;
        }
      }
    }
  }

}

constexpr float LTHR = 7.9228162514264338e28f;
#define LTHR_V LTHR
struct AttB {
  f32x16 o[4]; bf16x8 qf[8]; f32x16 negm; float m, l;
  f32x16 s0, s1;
  unsigned pw[16];
  bf16x8 fr[4];
  float ps;
};
DI void cmask1(f32x16& s, int key0, int row) {
  const int d = row - key0;
#pragma unroll
  for (int r = 0; r < 16; ++r) { const int c = (r & 3) + 8 * (r >> 2); s[r] = (c > d) ? -INFINITY : s[r]; }
}
DI float max16(const f32x16& s) { float a = fmaxf(fmaxf(s[0], s[1]), s[2]);
#pragma unroll
  for (int r = 3; r < 15; r += 2) a = fmaxf(fmaxf(a, s[r]), s[r + 1]);
  return fmaxf(a, s[15]); }
DI f32x16 splat16(float x) { f32x16 v;
#pragma unroll
  for (int r = 0; r < 16; ++r) v[r] = x;
  return v; }
#define PKB(w) __builtin_bit_cast(bf16x8, w)
#define EXP2(x) __builtin_amdgcn_exp2f(x)
DI bf16x8 pfrag(const AttB& a, int ks) { u32x4 w = {a.pw[4 * ks], a.pw[4 * ks + 1], a.pw[4 * ks + 2], a.pw[4 * ks + 3]}; return __builtin_bit_cast(bf16x8, w); }

template <bool HASCUR> DI void rare_fix(AttB& a, __amdgpu_buffer_rsrc_t sK, __amdgpu_buffer_rsrc_t sQ, unsigned voff, unsigned kso, unsigned qso, int key0, int row, bool mask) {
  float tm = -INFINITY;
  f32x16 t;
#pragma unroll 1
  for (int kb = 0; kb < 2; ++kb) {
    t = f32x16{};
#pragma unroll 2
    for (int ds = 0; ds < 8; ++ds) t = MFMA(bld16s(sK, voff, kso + kb * 8192 + ds * 1024), bld16(sQ, voff, qso + ds * 1024), t);
    if (mask) cmask1(t, key0 + 32 * kb, row);
    tm = fmaxf(tm, max16(t));
  }
  tm = hmax(tm);
  const float mn = fmaxf(a.m, tm), dl = mn - a.m, f = EXP2(-dl);
#pragma unroll
  for (int db = 0; db < 4; ++db)
#pragma unroll
    for (int r = 0; r < 16; ++r) a.o[db][r] *= f;
  float ps = 0.f;
#pragma unroll 1
  for (int kb = 0; kb < 2; ++kb) {
    t = f32x16{};
#pragma unroll 2
    for (int ds = 0; ds < 8; ++ds) t = MFMA(bld16s(sK, voff, kso + kb * 8192 + ds * 1024), bld16(sQ, voff, qso + ds * 1024), t);
    if (mask) cmask1(t, key0 + 32 * kb, row);
#pragma unroll
    for (int r = 0; r < 16; ++r) { t[r] = EXP2(t[r] - mn); ps += t[r]; }
    if (kb == 0) {
#pragma unroll
      for (int k = 0; k < 8; ++k) a.pw[k] = cvtpk(t[2 * k], t[2 * k + 1]);
    } else {
#pragma unroll
      for (int k = 0; k < 8; ++k) a.pw[8 + k] = cvtpk(t[2 * k], t[2 * k + 1]);
    }
  }
  a.l *= f; a.ps = ps;
  if (HASCUR) {
#pragma unroll
    for (int r = 0; r < 16; ++r) a.s0[r] -= dl;
  }
  a.m = mn; a.negm = splat16(-mn); PIN(a.negm);
}

struct StepCtx {
  lds_cptr kp;
  lds_cptr kpn;
  lds_cptr vp;
  unsigned m0k, m0v, sok, sov; bool dk, dv;
  unsigned voff;
};
constexpr int M_STEADY = 0, M_FIRST = 1, M_SWITCH = 2, M_DRAIN = 3;
template <int I, int MODE> DI void gapA(AttB& a, const StepCtx& c, __amdgpu_buffer_rsrc_t sK, __amdgpu_buffer_rsrc_t sV) {
  constexpr bool PREV = MODE != M_FIRST, CUR = MODE != M_DRAIN, NEW = MODE == M_FIRST || MODE == M_SWITCH;
  constexpr int ds = I & 7;
  if constexpr (CUR) {
    if constexpr (I < 8) { if constexpr (ds == 0) a.s0 = NEW ? MFMA(a.fr[I & 3], a.qf[0], f32x16{}) : MFMA(a.fr[I & 3], a.qf[0], a.negm); else a.s0 = MFMA(a.fr[I & 3], a.qf[ds], a.s0); }
    else { if constexpr (ds == 0) a.s1 = NEW ? MFMA(a.fr[I & 3], a.qf[0], f32x16{}) : MFMA(a.fr[I & 3], a.qf[0], a.negm); else a.s1 = MFMA(a.fr[I & 3], a.qf[ds], a.s1); }
  }
  if constexpr (I + 3 < 16) { if constexpr (CUR) a.fr[(I + 3) & 3] = lds_rd16(c.kp + (I + 3) * 1024); }
  else if constexpr (PREV) a.fr[(I + 3) & 3] = lds_rd16(c.vp + (I + 3 - 16) * 1024);
  if constexpr (I < 8) {
    if constexpr (PREV) {
      a.s1[8 + I] = EXP2(a.s1[8 + I]);
      a.ps += a.s1[7 + I];
      if constexpr (I < 4) { a.pw[8 + I] = cvtpk(a.s1[2 * I], a.s1[2 * I + 1]); PIN(a.pw[8 + I]); }
      else if constexpr (I != 6) { constexpr int k = I == 7 ? 6 : I; a.pw[8 + k] = cvtpk(a.s1[2 * k], a.s1[2 * k + 1]); PIN(a.pw[8 + k]); }
    }
    if constexpr (I & 1) { if (MODE != M_DRAIN || c.dv) dma16os<(I >> 1) * 1024>(c.m0v, c.voff, sV, c.sov); }
  } else {
    constexpr int i = I - 8;
    if constexpr (MODE == M_STEADY) {
      a.s0[i] = EXP2(a.s0[i]);
      if constexpr (i >= 1) a.ps += a.s0[i - 1];
    }
    if constexpr (i & 1) { if (MODE != M_DRAIN || c.dk) dma16os<(i >> 1) * 1024>(c.m0k, c.voff, sK, c.sok); }
  }
  PIN(a.ps);
  SBAR();
}
template <int I, int MODE> DI void gapB(AttB& a, const StepCtx& c, float& mx0, float& mx1) {
  constexpr bool PREV = MODE != M_FIRST, NEW = MODE == M_FIRST || MODE == M_SWITCH;
  constexpr int ks = I >> 2, db = I & 3;
  if constexpr (PREV) {
    a.o[db] = MFMA(a.fr[I & 3], pfrag(a, ks), a.o[db]);
    if constexpr (I + 3 < 16) a.fr[(I + 3) & 3] = lds_rd16(c.vp + (I + 3) * 1024);
  }
  if constexpr (I + 3 >= 16) a.fr[(I + 3) & 3] = lds_rd16(c.kpn + (I + 3 - 16) * 1024);
  if constexpr (MODE == M_STEADY) {
    if constexpr (I < 8) {
      a.s0[8 + I] = EXP2(a.s0[8 + I]);
      a.ps += a.s0[7 + I];
      if constexpr (I >= 4) { constexpr int k = I - 4; a.pw[k] = cvtpk(a.s0[2 * k], a.s0[2 * k + 1]); PIN(a.pw[k]); }
    } else {
      constexpr int i = I - 8;
      a.s1[i] = EXP2(a.s1[i]);
      if constexpr (i == 0) a.ps += a.s0[15]; else a.ps += a.s1[i - 1];
      if constexpr (I < 12) { constexpr int k = I - 4; a.pw[k] = cvtpk(a.s0[2 * k], a.s0[2 * k + 1]); PIN(a.pw[k]); }
    }
  } else if constexpr (NEW) {
    if constexpr (I < 4) {
      constexpr int r = 4 * I;
      if constexpr (I == 0) { mx0 = fmaxf(fmaxf(a.s0[0], a.s0[1]), a.s0[2]); mx1 = fmaxf(fmaxf(a.s1[0], a.s1[1]), a.s1[2]); mx0 = fmaxf(mx0, a.s0[3]); mx1 = fmaxf(mx1, a.s1[3]); }
      else { mx0 = fmaxf(fmaxf(mx0, a.s0[r]), a.s0[r + 1]); mx1 = fmaxf(fmaxf(mx1, a.s1[r]), a.s1[r + 1]); mx0 = fmaxf(fmaxf(mx0, a.s0[r + 2]), a.s0[r + 3]); mx1 = fmaxf(fmaxf(mx1, a.s1[r + 2]), a.s1[r + 3]); }
      PIN(mx0); PIN(mx1);
    } else {
      if constexpr (I == 4) { mx0 = fmaxf(hmax(fmaxf(mx0, mx1)), -1e30f); a.m = mx0; a.ps = 0.f; }
      if constexpr (I < 12) {
        constexpr int i = 2 * (I - 4);
        a.s0[i] = EXP2(a.s0[i] - mx0); a.s0[i + 1] = EXP2(a.s0[i + 1] - mx0);
        if constexpr (i >= 2) { a.ps += a.s0[i - 2]; a.ps += a.s0[i - 1]; }
      } else {
        constexpr int i = 2 * (I - 12);
        if constexpr (I == 12) { a.ps += a.s0[14]; a.ps += a.s0[15]; }
        a.s1[i] = EXP2(a.s1[i] - mx0); a.s1[i + 1] = EXP2(a.s1[i + 1] - mx0);
        a.s1[8 + i] -= mx0; a.s1[9 + i] -= mx0;
        if constexpr (i >= 2) { a.ps += a.s1[i - 2]; a.ps += a.s1[i - 1]; }
      }
      if constexpr (I >= 8) { constexpr int k = I - 8; if constexpr (2 * k + 1 < 2 * (I - 4)) { a.pw[k] = cvtpk(a.s0[2 * k], a.s0[2 * k + 1]); PIN(a.pw[k]); } }
    }
  }
  PIN(a.ps);
  SBAR();
}
template <int O, int MODE, int... Is> DI void phaseA_(AttB& a, const StepCtx& c, __amdgpu_buffer_rsrc_t sK, __amdgpu_buffer_rsrc_t sV, std::integer_sequence<int, Is...>) { (gapA<O + Is, MODE>(a, c, sK, sV), ...); }
template <int MODE, int... Is> DI void phaseB_(AttB& a, const StepCtx& c, float& mx0, float& mx1, std::integer_sequence<int, Is...>) { (gapB<Is, MODE>(a, c, mx0, mx1), ...); }

struct K2P { char* ws; };
struct AttEnv {
  int grp, wq, lane, hi, l32, n1, L, head; char* lds; unsigned lds0; char* ws;
  __amdgpu_buffer_rsrc_t sK, sV, sQ, sC;
};
DI int tile_of(const AttEnv& e, int x) { return e.grp == 0 ? x : (x < e.n1 ? x : 17 + (x - e.n1)); }
DI void step_top(const AttEnv& e, int s, StepCtx& c) {
  asm volatile("s_waitcnt vmcnt(0)\n\ts_barrier" ::: "memory");
  SBAR();
  const unsigned gb = e.grp * 81920;
  const int k3 = s % 3, k3n = (s + 1) % 3, k3d = (s + 2) % 3;
  c.kp = (lds_cptr)e.lds + gb + k3 * 16384 + e.lane * 16;
  c.kpn = (lds_cptr)e.lds + gb + k3n * 16384 + e.lane * 16;
  c.vp = (lds_cptr)e.lds + gb + 49152 + ((s + 1) & 1) * 16384 + e.lane * 16;
  c.dk = (s + 2 < 17); c.dv = (s < 17);
  c.m0k = rfl(e.lds0 + gb + k3d * 16384 + e.wq * 4096); c.m0v = rfl(e.lds0 + gb + 49152 + (s & 1) * 16384 + e.wq * 4096);
  c.sok = rfl((unsigned)(tile_of(e, s + 2) * 16384 + e.wq * 4096)); c.sov = rfl((unsigned)(tile_of(e, s) * 16384 + e.wq * 4096));
  c.voff = e.lane * 16;
}
DI void dma_all(const AttEnv& e, const StepCtx& c) {
  if (c.dk) {
#pragma unroll
    for (int i = 0; i < 4; ++i) dma16s(c.m0k + i * 1024, c.voff, e.sK, c.sok + i * 1024); }
  if (c.dv) {
#pragma unroll
    for (int i = 0; i < 4; ++i) dma16s(c.m0v + i * 1024, c.voff, e.sV, c.sov + i * 1024); }
}
template <bool HASCUR> DI void check_prev(AttB& a, const AttEnv& e, int qb, int prev_t) {
  if (__builtin_expect(__any(!(a.l + a.ps <= LTHR_V)), 0)) {
    const int rmin = qb * 128 + e.wq * 32;
    rare_fix<HASCUR>(a, e.sK, e.sQ, e.lane * 16, rfl((unsigned)(prev_t * 16384)), rfl((unsigned)(((qb == e.L ? 0 : 4) + e.wq) * 8192)),
                     prev_t * 64 + 4 * e.hi, rmin + e.l32, prev_t * 64 + 63 > rmin);
  }
  a.l += a.ps; a.ps = 0.f;
}
DI void store_block(AttB& a, const AttEnv& e, int qb) {
  const float inv = 1.f / hsum(a.l);
#pragma unroll
  for (int db = 0; db < 4; ++db)
#pragma unroll
    for (int g = 0; g < 2; ++g)
      bst16(pack8(a.o[db], g, inv), e.sC, e.lane * 16, rfl((unsigned)(((qb * 4 + e.wq) * 256 + db * 2 + g) * 1024)));
}
template <int MODE> DI void att_step(AttB& a, const AttEnv& e, int s, int qb_prev, int qb, bool loadq = false) {
  constexpr bool PREV = MODE != M_FIRST, CUR = MODE != M_DRAIN, NEW = MODE == M_FIRST || MODE == M_SWITCH;
  StepCtx c; step_top(e, s, c);
  if constexpr (NEW) asm volatile("" : "+v"(a.qf[0]), "+v"(a.qf[1]), "+v"(a.qf[2]), "+v"(a.qf[3]), "+v"(a.qf[4]), "+v"(a.qf[5]), "+v"(a.qf[6]), "+v"(a.qf[7]));
  const int t = tile_of(e, s), rmin = qb * 128 + e.wq * 32;
  const bool mask = t * 64 + 63 > rmin;
  if constexpr (MODE == M_FIRST) {
#pragma unroll
    for (int J = 0; J < 3; ++J) a.fr[J] = lds_rd16(c.kp + J * 1024);
  }
  phaseA_<0, MODE>(a, c, e.sK, e.sV, std::make_integer_sequence<int, 8>{});
  if constexpr (PREV) {
    a.ps += a.s1[15]; a.pw[15] = cvtpk(a.s1[14], a.s1[15]);
    check_prev<MODE == M_STEADY>(a, e, qb_prev, tile_of(e, s - 1));
  }
  if constexpr (MODE == M_STEADY) { if (mask) cmask1(a.s0, t * 64 + 4 * e.hi, rmin + e.l32); }
  SBAR();
  phaseA_<8, MODE>(a, c, e.sK, e.sV, std::make_integer_sequence<int, 8>{});
  if constexpr (CUR) {
    if (mask) { if constexpr (NEW) cmask1(a.s0, t * 64 + 4 * e.hi, rmin + e.l32); cmask1(a.s1, t * 64 + 32 + 4 * e.hi, rmin + e.l32); }
  }
  if constexpr (MODE == M_FIRST || MODE == M_STEADY) {
    if (loadq) {
#pragma unroll
      for (int ds = 0; ds < 8; ++ds)
        asm volatile("buffer_load_dwordx4 %0, %1, %2, %3 offen" : "=v"(a.qf[ds]) : "v"(e.lane * 16), "s"(e.sQ), "s"(rfl((unsigned)(e.wq * 8192 + ds * 1024))) : "memory");
    }
  }
  SBAR();
  float mx0 = 0.f, mx1 = 0.f; const float lold = a.l;
  phaseB_<MODE>(a, c, mx0, mx1, std::make_integer_sequence<int, 16>{});
  if constexpr (MODE == M_FIRST || MODE == M_STEADY) {
    if (loadq) asm volatile("s_waitcnt vmcnt(0)" : "+v"(a.qf[0]), "+v"(a.qf[1]), "+v"(a.qf[2]), "+v"(a.qf[3]), "+v"(a.qf[4]), "+v"(a.qf[5]), "+v"(a.qf[6]), "+v"(a.qf[7]) :: "memory");
  }
  if constexpr (NEW) {
    a.ps += a.s1[6];
    if constexpr (MODE == M_SWITCH) { a.l = lold; store_block(a, e, qb_prev); }
#pragma unroll
    for (int db = 0; db < 4; ++db) a.o[db] = f32x16{};
    a.l = 0.f; a.negm = splat16(-a.m); PIN(a.negm);
  }
}

struct AttA { f32x16 o; bf16x8 qf0, qf1; };
#define E4(sc, i) do { sc[i] = EXP2(sc[i]); sc[i + 1] = EXP2(sc[i + 1]); sc[i + 2] = EXP2(sc[i + 2]); sc[i + 3] = EXP2(sc[i + 3]); } while (0)
#define C2(pw, j, sc, i) do { pw[j] = cvtpk(sc[i], sc[i + 1]); pw[j + 1] = cvtpk(sc[i + 2], sc[i + 3]); } while (0)
DI int TA(int g) { return (g * 64) / NPSTEP; }
DI void a_loop(AttA& A, AttA& B, char* lds, int lane, const char* flags, bool poller) {
  const int l32 = lane & 31, hi = lane >> 5;
  const int vrow = l32 < 19 ? l32 : (((l32 >= 20 && l32 <= 27)) ? 0 : 4);
  lds_cptr k0p = (lds_cptr)lds + ARING + lane * 16;
  lds_cptr k1p = (lds_cptr)lds + ARING + 1024 + l32 * 16;
  lds_cptr vp = (lds_cptr)lds + ARING + 1536 + (hi * 19 + vrow) * 16;
  f32x16 sA, sB, tA, tB; u32x4 pa0, pa1, pb0, pb1, qa0, qa1, qb0, qb1;
  bf16x8 k0, k1, v0, v1, w0, w1;
  int g = 0;
#define SLOT(kt) (((kt) % 10) * ATB)
#define LDK(kt) do { const int so_ = SLOT(kt); k0 = lds_rd16(k0p + so_); k1 = lds_rd16(k1p + so_); } while (0)
#define TOPCHK(kt) do { if (g < NPSTEP && TA(g) == (kt)) { asm volatile("s_barrier" ::: "memory"); SBAR(); ++g; } } while (0)
#define DMP(i) do { } while (0)
#define ASTEP(SC, SN, TC, TN, PP0, PP1, PN0, PN1, QP0, QP1, QN0, QN1, kt, VC0, VC1, VN0, VN1, DOQK, DOPV, DOLDK) do { \
    TOPCHK(kt); \
    const int sv_ = SLOT(kt); \
    if (DOQK) { SN = MFMA(k0, A.qf0, f32x16{}); } SBAR(); VN0 = lds_rd16(vp + sv_); VN1 = lds_rd16(vp + sv_ + 608); E4(SC, 0); PIN(SC); DMP(0); SBAR(); \
    if (DOQK) { TN = MFMA(k0, B.qf0, f32x16{}); } SBAR(); E4(TC, 0); PIN(TC); DMP(1); SBAR(); \
    if (DOPV) { A.o = MFMA(VC0, PKB(PP0), A.o); } SBAR(); E4(SC, 4); C2(PN0, 0, SC, 0); PIN(SC); PIN(PN0); DMP(2); SBAR(); \
    if (DOPV) { B.o = MFMA(VC0, PKB(QP0), B.o); } SBAR(); E4(TC, 4); C2(QN0, 0, TC, 0); PIN(TC); PIN(QN0); DMP(3); SBAR(); \
    if (DOQK) { SN = MFMA(k1, A.qf1, SN); } SBAR(); E4(SC, 8); C2(PN0, 2, SC, 4); PIN(SC); PIN(PN0); DMP(4); SBAR(); \
    if (DOQK) { TN = MFMA(k1, B.qf1, TN); } SBAR(); if (DOLDK) LDK((kt) + 2); E4(TC, 8); C2(QN0, 2, TC, 4); PIN(TC); PIN(QN0); DMP(5); SBAR(); \
    if (DOPV) { A.o = MFMA(VC1, PKB(PP1), A.o); } SBAR(); E4(SC, 12); C2(PN1, 0, SC, 8); C2(PN1, 2, SC, 12); PIN(SC); PIN(PN1); DMP(6); SBAR(); \
    if (DOPV) { B.o = MFMA(VC1, PKB(QP1), B.o); } SBAR(); E4(TC, 12); C2(QN1, 0, TC, 8); C2(QN1, 2, TC, 12); PIN(TC); PIN(QN1); DMP(7); SBAR(); } while (0)
  pa0 = u32x4{0,0,0,0}; pa1 = pa0; pb0 = pa0; pb1 = pa0; qa0 = pa0; qa1 = pa0; qb0 = pa0; qb1 = pa0; v0 = bf16x8{}; v1 = v0; w0 = v0; w1 = v0;
  asm volatile("s_barrier" ::: "memory"); SBAR();
  LDK(0); sA = MFMA(k0, A.qf0, f32x16{}); tA = MFMA(k0, B.qf0, f32x16{}); sA = MFMA(k1, A.qf1, sA); tA = MFMA(k1, B.qf1, tA); LDK(1);
  g = 1;
#define TOPCHK0(kt) do { } while (0)
  {
#pragma push_macro("TOPCHK")
#undef TOPCHK
#define TOPCHK(kt) TOPCHK0(kt)
    ASTEP(sA, sB, tA, tB, pb0, pb1, pa0, pa1, qb0, qb1, qa0, qa1, 0, v0, v1, w0, w1, true, false, true);
#pragma pop_macro("TOPCHK")
  }
#pragma unroll 1
  for (int kt = 1; kt < 61; kt += 2) {
    ASTEP(sB, sA, tB, tA, pa0, pa1, pb0, pb1, qa0, qa1, qb0, qb1, kt, w0, w1, v0, v1, true, true, true);
    ASTEP(sA, sB, tA, tB, pb0, pb1, pa0, pa1, qb0, qb1, qa0, qa1, kt + 1, v0, v1, w0, w1, true, true, true);
  }
  if (poller) {
    const __attribute__((address_space(1))) unsigned* fl = (const __attribute__((address_space(1))) unsigned*)(flags + (lane & 7) * 128);
    for (unsigned spin = 0; spin < (1u << 20); ++spin) {
      const unsigned v = __hip_atomic_load(fl, __ATOMIC_RELAXED, __HIP_MEMORY_SCOPE_AGENT);
      if (__all(v == 1u)) break;
      __builtin_amdgcn_s_sleep(2);
    }
    __builtin_amdgcn_fence(__ATOMIC_ACQUIRE, "agent");
  }
  ASTEP(sB, sA, tB, tA, pa0, pa1, pb0, pb1, qa0, qa1, qb0, qb1, 61, w0, w1, v0, v1, true, true, true);
  ASTEP(sA, sB, tA, tB, pb0, pb1, pa0, pa1, qb0, qb1, qa0, qa1, 62, v0, v1, w0, w1, true, true, false);
  ASTEP(sB, sA, tB, tA, pa0, pa1, pb0, pb1, qa0, qa1, qb0, qb1, 63, w0, w1, v0, v1, false, true, false);
  A.o = MFMA(v0, PKB(pb0), A.o); B.o = MFMA(v0, PKB(qb0), B.o); A.o = MFMA(v1, PKB(pb1), A.o); B.o = MFMA(v1, PKB(qb1), B.o);
#undef LDK
#undef ASTEP
#undef TOPCHK
#undef TOPCHK0
#undef DMP
#undef SLOT
}
DI void a_initq(AttA& A, const char* ws, int qrow, int ah, int lane, float km) {
  const int hi = lane >> 5;
  const char* qp = ws + WS_QA + (size_t)qrow * 1152 + ah * 36;
  u32x4 q0; { const unsigned* qq = (const unsigned*)(qp + 16 * hi); q0 = u32x4{qq[0], qq[1], qq[2], qq[3]}; }
  const unsigned q16 = *(const unsigned*)(qp + 32);
  float qs = 0.f;
#pragma unroll
  for (int e = 0; e < 4; ++e) { const float x = __uint_as_float(q0[e] << 16), y = __uint_as_float(q0[e] & 0xFFFF0000u); qs += x * x + y * y; }
  if (hi == 0) { const float x = __uint_as_float(q16 << 16), y = __uint_as_float(q16 & 0xFFFF0000u); qs += x * x + y * y; }
  qs = hsum(qs);
  const float bound = sqrtf(qs * km) * 1.0009765625f + 1e-30f;
  const unsigned bb = (__float_as_uint(bound) + 0xFFFFu) >> 16;
  A.qf0 = __builtin_bit_cast(bf16x8, q0);
  A.qf1 = __builtin_bit_cast(bf16x8, hi == 0 ? u32x4{q16, bb | 0x8000u, 0u, 0u} : u32x4{0u, 0u, 0u, 0u});
  A.o = f32x16{};
}
DI void a_finish(AttA& A, char* ws, int lane, int qrow, int ah) {
  const int l32 = lane & 31, hi = lane >> 5;
  f32x16 o = A.o;
  float l = o[10];
  { auto rr = __builtin_amdgcn_permlane32_swap(__float_as_uint(l), __float_as_uint(l), false, false); l = __uint_as_float(rr[0]); }
  if (__builtin_expect(__any(!(l >= 8.6736174e-19f)), 0)) {
    float m = -1e30f; o = f32x16{};
    bf16x8 qz = A.qf1; qz[2] = 0;
    const int vrow = l32 < 19 ? l32 : 0;
    for (int kt = 0; kt < 64; ++kt) {
      const char* hb = ws + WS_KA + (size_t)kt * ATB;
      const bf16x8 k0 = *(const bf16x8*)(hb + lane * 16), k1 = *(const bf16x8*)(hb + 1024 + l32 * 16);
      const bf16x8 v0 = *(const bf16x8*)(hb + 1536 + (hi * 19 + vrow) * 16), v1 = *(const bf16x8*)(hb + 1536 + 608 + (hi * 19 + vrow) * 16);
      f32x16 sc = MFMA(k0, A.qf0, f32x16{});
      sc = MFMA(k1, qz, sc);
      const float mn = fmaxf(m, hmax(max16(sc))), f = EXP2(m - mn);
      m = mn;
#pragma unroll
      for (int r = 0; r < 16; ++r) { sc[r] = EXP2(sc[r] - mn); o[r] *= f; }
      o = MFMA(v0, pack8n(sc, 0), o);
      o = MFMA(v1, pack8n(sc, 1), o);
    }
    l = o[10];
    auto rr = __builtin_amdgcn_permlane32_swap(__float_as_uint(l), __float_as_uint(l), false, false); l = __uint_as_float(rr[0]);
  }
  const float inv = 1.f / l;
  char* ob = ws + WS_OA + (size_t)(qrow >> 5) * 36 * 1024 + l32 * 16;
  auto st2 = [&](int d, float x, float y) { const int c = ah * 18 + d; *(unsigned*)(ob + (c >> 4) * 1024 + ((c >> 2) & 1) * 512 + (((c >> 3) & 1) * 4 + (c & 3)) * 2) = cvtpk(x * inv, y * inv); };
  st2(4 * hi, o[0], o[1]); st2(4 * hi + 2, o[2], o[3]); st2(8 + 4 * hi, o[4], o[5]); st2(10 + 4 * hi, o[6], o[7]);
  if (hi == 0) st2(16, o[8], o[9]);
}

__global__ __launch_bounds__(512, 2) void kb_main(K1P p) {
  extern __shared__ __attribute__((aligned(16))) char lds[];
  const int tid = threadIdx.x, lane = tid & 63, l32 = lane & 31, hi = lane >> 5;
  const int b = blockIdx.x, xcd = b & 7, ii = b >> 3, head = xcd * 4 + (ii & 3), j = ii >> 2;
  const int w = rfl(tid >> 6);
  const unsigned lds0 = (unsigned)(size_t)(lds_ptr)lds, voff = lane * 16;
  const int grp = w >> 2, wq = w & 3, pp = j;
  char* flagw = p.ws + WS_FLAG + (head * 8 + j) * 128;
  AttB a;
  if (grp == 0) {
    AttA A, B;
    float km = *(const float*)(p.ws + WS_KMAX + lane * 4);
#pragma unroll
    for (int o = 32; o >= 1; o >>= 1) km = fmaxf(km, __shfl_xor(km, o));
    const int aqrow = j * 256 + wq * 64 + l32;
    a_initq(A, p.ws, aqrow, head, lane, km); a_initq(B, p.ws, aqrow + 32, head, lane, km);
    PIN(A.qf0); PIN(A.qf1); PIN(B.qf0); PIN(B.qf1);
    unsigned wv[7] = {0, 0, 0, 0, 0, 0, 0};
    if (WARM) {
      const char* src[7] = {(const char*)p.cx, (const char*)p.wuk, (const char*)p.wuv, (const char*)p.bx, (const char*)p.wq, (const char*)p.wo, (const char*)p.awo};
      const unsigned nline[7] = {2048u * 1024 / 128, 512u * 8192 / 128, 512u * 8192 / 128, 2048u * 1152 / 128, 576u * 8192 / 128, 4096u * 1152 / 128, 576u * 1152 / 128};
#pragma unroll
      for (int a7 = 0; a7 < 7; ++a7) {
        const unsigned i = (unsigned)(b * 192 + tid);
        if (tid < 192 && i < nline[a7]) wv[a7] = *(const unsigned*)(src[a7] + (size_t)i * 128);
      }
    }
    a_loop(A, B, lds, lane, p.ws + WS_FLAG + head * 8 * 128, w == 0);
    if ((wv[0] ^ wv[1] ^ wv[2] ^ wv[3] ^ wv[4] ^ wv[5] ^ wv[6]) == 0x9E3779B9u) *(unsigned*)(p.ws + WS_FLAG + 256 * 128 + 64) = 1u;

    a_finish(A, p.ws, lane, aqrow, head); a_finish(B, p.ws, lane, aqrow + 32, head);
    asm volatile("s_waitcnt vmcnt(0) lgkmcnt(0)\n\ts_barrier" ::: "memory");
  } else {
    __builtin_amdgcn_s_setprio(PPRIO);
    PG c;
    const int gr = wq >> 1, gc = wq & 1;
    c.lds0 = lds0; c.aw = wq; c.voff = voff;
    { const int r = lane >> 3, chp = lane & 7;
#pragma unroll
      for (int par = 0; par < 2; ++par) { const int ch = chp ^ ((4 * par + (r >> 1)) & 7); c.xoC[par] = (unsigned)(r * 1024 + ch * 16); c.xoX[par] = (unsigned)(r * 1152 + ch * 16); }
      c.woff = (unsigned)(((lane & 31) >> 2) * 8192 + (32 * (lane >> 5) + 8 * (lane & 3)) * 2); }
    c.sC = mksrd((const char*)p.cx + (size_t)j * 256 * 1024, 256 * 1024);
    c.sX = mksrd((const char*)p.bx, 2048 * 1152);
    c.rowL = (unsigned)((15 - j) * 128 * 1152); c.rowS = (unsigned)(j * 128 * 1152);
    c.sUK = mksrd((const char*)p.wuk + head * 256, 512 * 8192 - head * 256);
    c.sUV = mksrd((const char*)p.wuv + head * 256, 512 * 8192 - head * 256);
    c.sWQ = mksrd((const char*)p.wq + head * 256, 576 * 8192 - head * 256);
    c.sA = mksrd(p.ws + WS_KA, 64 * ATB);
    {
      auto da = [&](int q) { if (q > 14) q = 14; dma16(rfl(lds0 + ARING + (q / 3) * ATB + (q % 3) * 1024), voff, c.sA, rfl((unsigned)((q / 3) * ATB + (q % 3) * 1024))); };
      auto dw = [&](int st) { const unsigned m0 = rfl(lds0 + WRING + st * WSLOT + wq * 2048), so = rfl((unsigned)((st * 32 + wq * 8) * 8192)); dma16(m0, c.woff, c.sUK, so); dma16(m0 + 1024, c.woff + 128, c.sUK, so); };
      auto dx = [&](int T) {
#pragma unroll
        for (int half = 0; half < 2; ++half)
#pragma unroll
          for (int i = 0; i < 4; ++i)
            dma16(rfl(lds0 + T * XSLOT + (16 * half + 4 * wq + i) * 1024), c.xoC[i & 1], c.sC, rfl((unsigned)((128 * half + 32 * wq + 8 * i) * 1024 + T * 128))); };
      { const int q0 = 3 * wq; da(q0 < 9 ? q0 : 0); da(q0 + 1 < 9 ? q0 + 1 : 1); da(q0 + 2 < 9 ? q0 + 2 : 2); }
      dw(0); dx(0); dw(1); dw(2); dx(1);
      da(9 + 2 * wq); da(10 + 2 * wq);
    }
    const int blk = (lane >> 4) & 1, q4 = (lane & 15) >> 2, p4 = lane & 3;
#pragma unroll
    for (int s = 0; s < 4; ++s) c.xb[s] = (lds_cptr)lds + (gr * 4) * 4096 + 128 * l32 + 16 * ((2 * s + hi) ^ ((l32 >> 1) & 7));
    c.wb = (lds_cptr)lds + WRING + (2 * gc) * 512 + 2048 * hi + 64 * q4 + 32 * blk + 8 * p4;
    f32x16 acc[4][2]; FragKV fa, fb; bf16x8 kvp[16]; KVSt ks; ks.voff = voff;
#define ZACC() do { _Pragma("unroll") for (int r = 0; r < 4; ++r) { acc[r][0] = f32x16{}; acc[r][1] = f32x16{}; } } while (0)
#define PACKKV() do { _Pragma("unroll") for (int r = 0; r < 4; ++r) _Pragma("unroll") for (int ct = 0; ct < 2; ++ct) _Pragma("unroll") for (int g8 = 0; g8 < 2; ++g8) kvp[r * 4 + ct * 2 + g8] = pack8n(acc[r][ct], g8); SBAR(); } while (0)
#pragma unroll
    for (int i = 0; i < 16; ++i) kvp[i] = bf16x8{};
    ks.srd = mksrd(p.ws + WS_KF + ((size_t)(head * 64 + 8 * j + 4 * gr) * 8 + 4 * gc) * 1024, 65536);
    ZACC();
    GLoop<0, 0, 2>::run(c, acc, fa, fb, kvp, ks, flagw);
    GLoop<0, 2, 16>::run(c, acc, fa, fb, kvp, ks, flagw);
    PACKKV(); ZACC();
    GLoop<1, 16, 32>::run(c, acc, fa, fb, kvp, ks, flagw);
    PACKKV(); ZACC();
    ks.srd = mksrd(p.ws + WS_VF + ((size_t)(head * 32 + 4 * j + 2 * gr) * 16 + 2 * gc) * 1024, 65536);
    GLoop<2, 32, 50>::run(c, acc, fa, fb, kvp, ks, flagw);
    __builtin_amdgcn_s_setprio(0);
    asm volatile("s_waitcnt vmcnt(0) lgkmcnt(0)\n\ts_barrier" ::: "memory");
#pragma unroll
    for (int r = 0; r < 4; ++r)
#pragma unroll
      for (int ct = 0; ct < 2; ++ct)
#pragma unroll
        for (int g8 = 0; g8 < 2; ++g8) {
          const int fi = (4 * gr + r) * 8 + 2 * (2 * gc + ct) + g8;
          *(bf16x8*)(lds + fi * 1024 + lane * 16) = pack8(acc[r][ct], g8, CB);
        }
  }
  {
    const __amdgpu_buffer_rsrc_t sQLw = mksrd(p.ws + WS_QF + (size_t)b * 65536, 65536);
    asm volatile("s_waitcnt lgkmcnt(0)\n\ts_barrier" ::: "memory");
#pragma unroll
    for (int ds = 0; ds < 8; ++ds) a.qf[ds] = lds_rd16((lds_cptr)lds + ((grp * 4 + wq) * 8 + ds) * 1024 + lane * 16);
    if (w == 0) asm volatile("s_waitcnt vmcnt(0)" ::: "memory");
    asm volatile("s_waitcnt lgkmcnt(0)\n\ts_barrier" ::: "memory");
#pragma unroll
    for (int ds = 0; ds < 8; ++ds) bst16(a.qf[ds], sQLw, voff, (unsigned)(((grp * 4 + wq) * 8 + ds) * 1024));
  }
  const int L = 15 - pp, n1 = 2 * pp + 2;
  {
  AttEnv e; e.grp = grp; e.wq = wq; e.lane = lane; e.hi = hi; e.l32 = l32; e.n1 = n1; e.L = L; e.head = head; e.lds = lds; e.lds0 = lds0; e.ws = p.ws;
  e.sK = mksrd(p.ws + WS_KF + (size_t)head * S * 256, S * 256);
  e.sV = mksrd(p.ws + WS_VF + (size_t)head * S * 256, S * 256);
  e.sQ = mksrd(p.ws + WS_QF + (size_t)b * 65536, 65536);
  e.sC = mksrd(p.ws + WS_CF + (size_t)head * 8192, 64 * 256 * 1024 - head * 8192);
  int qb = grp == 0 ? L : pp;
  {
    const unsigned gb = grp * 81920;
#pragma unroll
    for (int t = 0; t < 2; ++t)
#pragma unroll
      for (int i = 0; i < 4; ++i) dma16s(rfl(lds0 + gb + t * 16384 + wq * 4096 + i * 1024), voff, e.sK, rfl((unsigned)(t * 16384 + wq * 4096 + i * 1024)));
  }
  if (grp == BPRIO_G) __builtin_amdgcn_s_setprio(BPRIO);
  {
    const int send0 = grp == 0 ? 17 : n1;
    const int rmin0 = qb * 128 + wq * 32;
    int nt0 = ((rmin0 + 31) >> 6) + 1; if (nt0 > send0) nt0 = send0;
    int nt1 = 0;
    if (grp == 1) { const int rminL = L * 128 + wq * 32; nt1 = ((rminL + 31) >> 6) - 17 + 1; if (nt1 > 17 - n1) nt1 = 17 - n1; if (nt1 < 0) nt1 = 0; }
    a.l = 0.f; a.ps = 0.f; a.m = -1e30f;
#pragma unroll
    for (int db = 0; db < 4; ++db) a.o[db] = f32x16{};
    int s = 0; bool carry = false; int qprev = qb;
    const int nseg = grp == 0 ? 1 : 2;
#pragma unroll 1
    for (int seg = 0; seg < nseg; ++seg) {
      const int nt = seg == 0 ? nt0 : nt1;
      const int send = seg == 0 ? send0 : 17;
      const bool shortblk = grp == 1 && seg == 0;
      if (nt > 0) {
        const int slast = s + nt;
        if (carry) att_step<M_SWITCH>(a, e, s, qprev, qb); else att_step<M_FIRST>(a, e, s, qb, qb, shortblk && nt == 1);
        for (++s; s < slast; ++s) att_step<M_STEADY>(a, e, s, qb, qb, shortblk && s == slast - 1);
        carry = shortblk && s == send && nt1 > 0;
        if (!carry) {
          att_step<M_DRAIN>(a, e, s, qb, qb); ++s;
          if (shortblk) {
            store_block(a, e, qb);
#pragma unroll
            for (int db = 0; db < 4; ++db) a.o[db] = f32x16{};
            a.l = 0.f; a.ps = 0.f; a.m = -1e30f;
          }
        }
      }
      for (; s < send; ++s) { StepCtx sc; step_top(e, s, sc); dma_all(e, sc); }
      qprev = qb; qb = L;
    }
    for (; s < 18; ++s) { StepCtx sc; step_top(e, s, sc); dma_all(e, sc); }
  }
  asm volatile("s_waitcnt vmcnt(0) lgkmcnt(0)\n\ts_barrier" ::: "memory");
  {
    const float lt = hsum(a.l);
    char* xb = lds + w * 8192;
    float* st = (float*)(lds + 65536) + w * 128;
#pragma unroll
    for (int i = 0; i < 2; ++i)
#pragma unroll
      for (int q = 0; q < 4; ++q) {
        const f32x16& o = grp == 0 ? a.o[2 + i] : a.o[i];
        *(f32x4*)(xb + (i * 4 + q) * 1024 + lane * 16) = f32x4{o[4 * q], o[4 * q + 1], o[4 * q + 2], o[4 * q + 3]};
      }
    st[lane] = a.m; st[64 + lane] = lt;
    __syncthreads();
    const int ow = w ^ 4;
    const char* yb = lds + ow * 8192; const float* so = (const float*)(lds + 65536) + ow * 128;
    const float m2 = so[lane], l2 = so[64 + lane];
    const float mn = fmaxf(a.m, m2), f1 = EXP2(a.m - mn), f2 = EXP2(m2 - mn);
    const float inv = 1.f / (lt * f1 + l2 * f2);
    const float g1 = f1 * inv, g2 = f2 * inv;
#pragma unroll
    for (int i = 0; i < 2; ++i) {
      f32x16 o = grp == 0 ? a.o[i] : a.o[2 + i];
#pragma unroll
      for (int q = 0; q < 4; ++q) { const f32x4 y = *(const f32x4*)(yb + (i * 4 + q) * 1024 + lane * 16);
#pragma unroll
        for (int e = 0; e < 4; ++e) o[4 * q + e] = o[4 * q + e] * g1 + y[e] * g2; }
      const int db = (grp == 0 ? 0 : 2) + i;
#pragma unroll
      for (int g = 0; g < 2; ++g)
        bst16(pack8n(o, g), e.sC, voff, rfl((unsigned)(((L * 4 + wq) * 256 + db * 2 + g) * 1024)));
    }
  }
  }
}

struct K3P { char* ws; const void* wo; const void* awo; void* out; };
constexpr int K3_WS = 24576;
struct K3C {
  unsigned lds0, w, xvoff, wvoff;
  __amdgpu_buffer_rsrc_t sX, sW;
  lds_cptr wb;
};
DI void xload(bf16x8& d, unsigned voff, __amdgpu_buffer_rsrc_t srd, unsigned soff) {
  asm volatile("buffer_load_dwordx4 %0, %1, %2, %3 offen" : "=v"(d) : "v"(voff), "s"(srd), "s"(soff) : "memory");
}
template <int I> DI void k3_dmaW(const K3C& c, int st) {
  dma16(c.lds0 + (st & 3) * K3_WS + (c.w * 3 + I) * 1024, c.wvoff + I * 128, c.sW, (unsigned)((st * 64 + c.w * 8) * 1152));
}
struct K3F { s16x4 wl[3], wh[3]; };
DI void k3_stage(const K3C& c, f32x16 (&acc)[6], bf16x8& x, K3F& fa, K3F& fb, int st, unsigned xso) {
  const int so = (st & 3) * K3_WS, sn = ((st + 1) & 3) * K3_WS;
#pragma unroll
  for (int m = 0; m < 3; ++m) {
    acc[m] = MFMA(cat(fa.wl[m], fa.wh[m]), x, acc[m]);
    lds_cptr p = c.wb + so + (3 + m) * 512;
    fb.wl[m] = lds_tr(p); fb.wh[m] = lds_tr(p + 3072);
    if (m == 0) k3_dmaW<0>(c, st + 3);
    if (m == 2) k3_dmaW<1>(c, st + 3);
    SBAR();
  }
#pragma unroll
  for (int m = 0; m < 3; ++m) {
    acc[3 + m] = MFMA(cat(fb.wl[m], fb.wh[m]), x, acc[3 + m]);
    lds_cptr p = c.wb + sn + m * 512;
    fa.wl[m] = lds_tr(p); fa.wh[m] = lds_tr(p + 3072);
    if (m == 1) k3_dmaW<2>(c, st + 3);
    SBAR();
  }
  xload(x, c.xvoff, c.sX, xso);
  SBAR();
}
__global__ __launch_bounds__(512, 2) void k3_out(K3P p) {
  extern __shared__ __attribute__((aligned(16))) char lds[];
  const int tid = threadIdx.x, lane = tid & 63, l32 = lane & 31, hi = lane >> 5;
  const int b = blockIdx.x;
  const bool isB = b < 192;
  K3C c;
  c.lds0 = (unsigned)(size_t)(lds_ptr)lds; c.w = rfl(tid >> 6);
  const int rt = c.w & 1, kq = c.w >> 1;
  const int blk = (lane >> 4) & 1, q4 = (lane & 15) >> 2, p4 = lane & 3;
  const int nst = isB ? 32 : 9;
  c.xvoff = lane * 16;
  { const int sub = lane >> 5, rr = (lane & 31) >> 2, c4 = lane & 3; c.wvoff = (unsigned)(rr * 1152 + (32 * sub + 8 * c4) * 2); }
  c.wb = (lds_cptr)lds + (2 * kq) * 3072 + 32 * blk + 8 * p4 + (4 * hi + q4) * 64;
  const int ntile = isB ? 1 : ((b - 192) < 32 ? 2 : 1);
#pragma unroll 1
  for (int it = 0; it < ntile; ++it) {
    int t, sk = 0;
    if (isB) { const int x = b & 7, i = b >> 3; t = x * 12 + (i >> 1); sk = i & 1; } else t = (b - 192) + 64 * it;
    const int rb = t / 3, cb = t - rb * 3;
    c.sW = isB ? mksrd((const char*)p.wo + (size_t)sk * 2048 * 1152 + cb * 384, 2048 * 1152 - cb * 384) : mksrd((const char*)p.awo + cb * 384, 576 * 1152 - cb * 384);
    {
      const char* xb = isB ? p.ws + WS_CF + ((size_t)(rb * 2 + rt) * 256 + sk * 128) * 1024 : p.ws + WS_OA + (size_t)(rb * 2 + rt) * 36 * 1024;
      c.sX = mksrd(xb + kq * 1024, nst * 4096 - kq * 1024);
    }
    f32x16 acc[6];
#pragma unroll
    for (int ct = 0; ct < 6; ++ct) acc[ct] = f32x16{};
    bf16x8 x0, x1, x2;
    if (it) asm volatile("s_waitcnt lgkmcnt(0)\n\ts_barrier" ::: "memory");
    k3_dmaW<0>(c, 0); k3_dmaW<1>(c, 0); k3_dmaW<2>(c, 0);
    xload(x0, c.xvoff, c.sX, 0);
    k3_dmaW<0>(c, 1); k3_dmaW<1>(c, 1); k3_dmaW<2>(c, 1);
    xload(x1, c.xvoff, c.sX, 4096);
    k3_dmaW<0>(c, 2); k3_dmaW<1>(c, 2); k3_dmaW<2>(c, 2);
    (void)x2;
    K3F fa, fb;
#define K3_WAIT(N, X) asm volatile("s_waitcnt vmcnt(" #N ")\n\ts_barrier" : "+v"(X) :: "memory")
    K3_WAIT(4, x0); SBAR();
#pragma unroll
    for (int m = 0; m < 3; ++m) { fa.wl[m] = lds_tr(c.wb + m * 512); fa.wh[m] = lds_tr(c.wb + m * 512 + 3072); }
    SBAR();
    int st = 0;
    for (; st + 1 < nst; st += 2) {
      k3_stage(c, acc, x0, fa, fb, st, (unsigned)((st + 2) * 4096));
      K3_WAIT(4, x1); SBAR();
      k3_stage(c, acc, x1, fa, fb, st + 1, (unsigned)((st + 3) * 4096));
      K3_WAIT(4, x0); SBAR();
    }
    if (st < nst) k3_stage(c, acc, x0, fa, fb, st, (unsigned)((st + 2) * 4096));
    asm volatile("s_waitcnt vmcnt(0) lgkmcnt(0)\n\ts_barrier" : "+v"(x0), "+v"(x1) :: "memory");
    f32x4 h[12];
    {
      const bool up = (kq & 2) != 0;
      char* mine = lds + c.w * 12288; const char* theirs = lds + (c.w ^ 4) * 12288;
#pragma unroll
      for (int c3 = 0; c3 < 3; ++c3)
#pragma unroll
        for (int g = 0; g < 4; ++g) {
          const f32x16& give = up ? acc[c3] : acc[3 + c3];
          *(f32x4*)(mine + (c3 * 4 + g) * 1024 + lane * 16) = f32x4{give[4 * g], give[4 * g + 1], give[4 * g + 2], give[4 * g + 3]};
        }
      __syncthreads();
#pragma unroll
      for (int c3 = 0; c3 < 3; ++c3)
#pragma unroll
        for (int g = 0; g < 4; ++g) {
          const f32x16& keep = up ? acc[3 + c3] : acc[c3];
          h[c3 * 4 + g] = f32x4{keep[4 * g], keep[4 * g + 1], keep[4 * g + 2], keep[4 * g + 3]} + *(const f32x4*)(theirs + (c3 * 4 + g) * 1024 + lane * 16);
        }
      __syncthreads();
    }
    f32x4 q[6];
    {
      const bool up = (kq & 1) != 0;
      char* mine = lds + c.w * 6144; const char* theirs = lds + (c.w ^ 2) * 6144;
#pragma unroll
      for (int i = 0; i < 6; ++i) *(f32x4*)(mine + i * 1024 + lane * 16) = up ? h[i] : h[6 + i];
      __syncthreads();
#pragma unroll
      for (int i = 0; i < 6; ++i) q[i] = (up ? h[6 + i] : h[i]) + *(const f32x4*)(theirs + i * 1024 + lane * 16);
    }
    const int it0 = (kq & 2) * 6 + (kq & 1) * 6;
    auto store_out = [&](int coloff) {
      const int row = rb * 64 + rt * 32 + l32;
#pragma unroll
      for (int i = 0; i < 6; ++i) { const int item = it0 + i, ct = item >> 2, g = item & 3;
        *(u32x2*)((char*)p.out + (size_t)row * 2304 + coloff + (cb * 192 + ct * 32 + 8 * g + 4 * hi) * 2) = u32x2{cvtpk(q[i][0], q[i][1]), cvtpk(q[i][2], q[i][3])}; } };
    if (!isB) store_out(0);
    else {
      char* slab = p.ws + WS_KF + (size_t)(t * 2) * 12288;
      if (rt != sk) {
        const __amdgpu_buffer_rsrc_t sP = mksrd(slab + sk * 12288 + kq * 3072, 3072);
#pragma unroll
        for (int i = 0; i < 3; ++i)
          __builtin_amdgcn_raw_buffer_store_b128(u32x4{cvtpk(q[2 * i][0], q[2 * i][1]), cvtpk(q[2 * i][2], q[2 * i][3]), cvtpk(q[2 * i + 1][0], q[2 * i + 1][1]), cvtpk(q[2 * i + 1][2], q[2 * i + 1][3])}, sP, lane * 16 + i * 1024, 0, 16);
        asm volatile("s_waitcnt vmcnt(0)" ::: "memory");
        if (lane == 0) __hip_atomic_fetch_add((__attribute__((address_space(1))) unsigned*)(p.ws + WS_CNT + (t * 2 + sk) * 128), 1u, __ATOMIC_RELAXED, __HIP_MEMORY_SCOPE_AGENT);
      } else {
        const __attribute__((address_space(1))) unsigned* fl = (const __attribute__((address_space(1))) unsigned*)(p.ws + WS_CNT + (t * 2 + (sk ^ 1)) * 128);
        for (unsigned spin = 0; spin < (1u << 20); ++spin) {
          if (__hip_atomic_load(fl, __ATOMIC_RELAXED, __HIP_MEMORY_SCOPE_AGENT) >= 4u) break;
          __builtin_amdgcn_s_sleep(1);
        }
        __builtin_amdgcn_fence(__ATOMIC_ACQUIRE, "wavefront");
        const __amdgpu_buffer_rsrc_t sP = mksrd(slab + (sk ^ 1) * 12288 + kq * 3072, 3072);
#pragma unroll
        for (int i = 0; i < 3; ++i) {
          const u32x4 y = __builtin_bit_cast(u32x4, __builtin_amdgcn_raw_buffer_load_b128(sP, lane * 16 + i * 1024, 0, 16));
          q[2 * i][0] += __uint_as_float(y[0] << 16); q[2 * i][1] += __uint_as_float(y[0] & 0xFFFF0000u); q[2 * i][2] += __uint_as_float(y[1] << 16); q[2 * i][3] += __uint_as_float(y[1] & 0xFFFF0000u);
          q[2 * i + 1][0] += __uint_as_float(y[2] << 16); q[2 * i + 1][1] += __uint_as_float(y[2] & 0xFFFF0000u); q[2 * i + 1][2] += __uint_as_float(y[3] << 16); q[2 * i + 1][3] += __uint_as_float(y[3] & 0xFFFF0000u);
        }
        store_out(1152);
      }
    }
  }
}

extern "C" void kernel_launch(void* const* d_in, const int* in_sizes, int n_in, void* d_out, int out_size, void* d_ws, size_t ws_size, hipStream_t stream) {
  (void)hipFuncSetAttribute((const void*)k0_aproj, hipFuncAttributeMaxDynamicSharedMemorySize, 147456);
  (void)hipFuncSetAttribute((const void*)kb_main, hipFuncAttributeMaxDynamicSharedMemorySize, 163840);
  (void)hipFuncSetAttribute((const void*)k3_out, hipFuncAttributeMaxDynamicSharedMemorySize, 4 * K3_WS);
  K1P p1; p1.cx = d_in[6]; p1.bx = d_in[5]; p1.wuk = d_in[8]; p1.wuv = d_in[9]; p1.wq = d_in[7];
  p1.ax = d_in[0]; p1.awq = d_in[1]; p1.awk = d_in[2]; p1.awv = d_in[3]; p1.wo = d_in[10]; p1.awo = d_in[4]; p1.ws = (char*)d_ws;
  k0_aproj<<<256, 512, 147456, stream>>>(p1);
  kb_main<<<256, 512, 163840, stream>>>(p1);
  K3P p3; p3.ws = (char*)d_ws; p3.wo = d_in[10]; p3.awo = d_in[4]; p3.out = d_out;
  k3_out<<<256, 512, 4 * K3_WS, stream>>>(p3);
}
```

```cpp
#include <hip/hip_runtime.h>
#include <hip/hip_bf16.h>
#include <cstdio>
#include <cstdint>
#include <utility>

#define PUBMODE 0
#define KV_ACQ 0
#define BPRIO 1
#define BPRIO_G 1
#define PPRIO 2
#define TLBW 1
#define WARM 1
#define ST_AUX 0
#define DI __device__ __forceinline__
typedef short bf16x8 __attribute__((ext_vector_type(8)));
typedef short s16x4 __attribute__((ext_vector_type(4)));
typedef float f32x16 __attribute__((ext_vector_type(16)));
typedef float f32x4 __attribute__((ext_vector_type(4)));
typedef float f32x2 __attribute__((ext_vector_type(2)));
typedef __bf16 bf16x2_t __attribute__((ext_vector_type(2)));
typedef unsigned u32x4 __attribute__((ext_vector_type(4)));
typedef unsigned u32x2 __attribute__((ext_vector_type(2)));
typedef __attribute__((address_space(3))) const char* lds_cptr;
typedef __attribute__((address_space(3))) char* lds_ptr;
#define SBAR() __builtin_amdgcn_sched_barrier(0)
#define PIN(x) asm volatile("" : "+v"(x))
#define MFMA(a, b, c) __builtin_amdgcn_mfma_f32_32x32x16_bf16(a, b, c, 0, 0, 0)
#define SRD_W3 0x00020000

DI unsigned rfl(unsigned x) { return __builtin_amdgcn_readfirstlane(x); }
DI unsigned cvtpk(float lo, float hi) { f32x2 v = {lo, hi}; bf16x2_t b = __builtin_convertvector(v, bf16x2_t); return __builtin_bit_cast(unsigned, b); }
DI void dma16(unsigned m0, unsigned voff, __amdgpu_buffer_rsrc_t srd, unsigned soff) {
  asm volatile("s_mov_b32 m0, %0\n\ts_nop 0\n\tbuffer_load_dwordx4 %1, %2, %3 offen lds" :: "s"(m0), "v"(voff), "s"(srd), "s"(soff) : "m0", "memory");
}
template <int OFF> DI void dma16o(unsigned m0, unsigned voff, __amdgpu_buffer_rsrc_t srd, unsigned soff) {
  asm volatile("s_mov_b32 m0, %0\n\ts_nop 0\n\tbuffer_load_dwordx4 %1, %2, %3 offen offset:%4 lds" :: "s"(m0), "v"(voff), "s"(srd), "s"(soff), "i"(OFF) : "m0", "memory");
}
DI void dma16s(unsigned m0, unsigned voff, __amdgpu_buffer_rsrc_t srd, unsigned soff) {
  asm volatile("s_mov_b32 m0, %0\n\ts_nop 0\n\tbuffer_load_dwordx4 %1, %2, %3 offen sc1 lds" :: "s"(m0), "v"(voff), "s"(srd), "s"(soff) : "m0", "memory");
}
template <int OFF> DI void dma16os(unsigned m0, unsigned voff, __amdgpu_buffer_rsrc_t srd, unsigned soff) {
  asm volatile("s_mov_b32 m0, %0\n\ts_nop 0\n\tbuffer_load_dwordx4 %1, %2, %3 offen offset:%4 sc1 lds" :: "s"(m0), "v"(voff), "s"(srd), "s"(soff), "i"(OFF) : "m0", "memory");
}
DI bf16x8 bld16s(__amdgpu_buffer_rsrc_t srd, unsigned voff, unsigned soff) { return __builtin_bit_cast(bf16x8, __builtin_amdgcn_raw_buffer_load_b128(srd, voff, soff, 16)); }
DI void dma4(unsigned m0, unsigned voff, __amdgpu_buffer_rsrc_t srd, unsigned soff) {
  asm volatile("s_mov_b32 m0, %0\n\ts_nop 0\n\tbuffer_load_dword %1, %2, %3 offen lds" :: "s"(m0), "v"(voff), "s"(srd), "s"(soff) : "m0", "memory");
}
DI __amdgpu_buffer_rsrc_t mksrd(const void* p, unsigned bytes) { return __builtin_amdgcn_make_buffer_rsrc((void*)p, (short)0, (int)bytes, SRD_W3); }
DI void bst16s(bf16x8 v, __amdgpu_buffer_rsrc_t srd, unsigned voff, unsigned soff) { __builtin_amdgcn_raw_buffer_store_b128(__builtin_bit_cast(u32x4, v), srd, voff + soff, 0, 16); }
DI bf16x8 bld16(__amdgpu_buffer_rsrc_t srd, unsigned voff, unsigned soff) { return __builtin_bit_cast(bf16x8, __builtin_amdgcn_raw_buffer_load_b128(srd, voff, soff, 0)); }
DI void bst16(bf16x8 v, __amdgpu_buffer_rsrc_t srd, unsigned voff, unsigned soff) { __builtin_amdgcn_raw_buffer_store_b128(__builtin_bit_cast(u32x4, v), srd, voff + soff, 0, ST_AUX); }
template <int N> DI void wait_bar() { asm volatile("s_waitcnt vmcnt(%0)\n\ts_barrier" :: "i"(N) : "memory"); }
DI bf16x8 lds_rd16(lds_cptr p) { return *(const __attribute__((address_space(3))) bf16x8*)p; }
DI s16x4 lds_tr(lds_cptr p) { return __builtin_bit_cast(s16x4, __builtin_amdgcn_ds_read_tr16_b64_v4i16((__attribute__((address_space(3))) s16x4*)p)); }
DI bf16x8 cat(s16x4 a, s16x4 b) { return __builtin_shufflevector(a, b, 0, 1, 2, 3, 4, 5, 6, 7); }
DI bf16x8 pack8(const f32x16& x, int g, float sc) {
  u32x4 w;
#pragma unroll
  for (int e = 0; e < 4; ++e) w[e] = cvtpk(x[8 * g + 2 * e] * sc, x[8 * g + 2 * e + 1] * sc);
  return __builtin_bit_cast(bf16x8, w);
}
DI bf16x8 pack8n(const f32x16& x, int g) {
  u32x4 w;
#pragma unroll
  for (int e = 0; e < 4; ++e) w[e] = cvtpk(x[8 * g + 2 * e], x[8 * g + 2 * e + 1]);
  return __builtin_bit_cast(bf16x8, w);
}

DI float hmax(float m) { auto rr = __builtin_amdgcn_permlane32_swap(__float_as_uint(m), __float_as_uint(m), false, false); return fmaxf(__uint_as_float(rr[0]), __uint_as_float(rr[1])); }
DI float hsum(float m) { auto rr = __builtin_amdgcn_permlane32_swap(__float_as_uint(m), __float_as_uint(m), false, false); return __uint_as_float(rr[0]) + __uint_as_float(rr[1]); }
constexpr int S = 2048, H = 32, DB = 128, MB = 576, DC = 512, DA = 576, DHA = 18;
constexpr float CB = 0.08838834764831845f * 1.4426950408889634f;
constexpr float CA = 0.23570226039551584f * 1.4426950408889634f;
constexpr size_t WS_KF = 0;
constexpr size_t WS_VF = WS_KF + (size_t)H * S * DB * 2;
constexpr size_t WS_QF = WS_VF + (size_t)H * S * DB * 2;
constexpr size_t WS_CF = WS_QF + (size_t)H * S * DB * 2;
constexpr size_t WS_QA = WS_CF + (size_t)H * S * DB * 2;
constexpr int ATB = 3072;
constexpr size_t WS_KA = WS_QA + (size_t)S * DA * 2 + 256;
constexpr size_t WS_KMAX = WS_KA + 64 * ATB;
constexpr size_t WS_OA = WS_KMAX + 1024;
constexpr size_t WS_PART = WS_OA + (size_t)S * DA * 2;
constexpr size_t WS_FLAG = WS_PART + (size_t)4 * S * 576 * 4;
constexpr size_t WS_CNT = WS_FLAG + 256 * 128 + 1024;
constexpr size_t WS_DONE = WS_CNT + 192 * 128 + 1024;
constexpr size_t WS_END = WS_DONE + 256 * 128;

struct K1P {
  const void *cx, *bx, *wuk, *wuv, *wq;
  const void *ax, *awq, *awk, *awv;
  const void *wo, *awo;
  void* out;
  char* ws;
};
constexpr int XSLOT = 32768, WRING = 98304, WSLOT = 8192, ARING = 131072, NPSTEP = 50;
struct PG {
  lds_cptr xb[4];
  lds_cptr wb;
  unsigned lds0, aw;
  unsigned xoC[2], xoX[2], woff, voff;
  __amdgpu_buffer_rsrc_t sC, sX, sUK, sUV, sWQ, sA;
  unsigned rowL, rowS;
};
constexpr int TAc(int g) { return (g * 64) / NPSTEP; }
template <int G, int I> DI void g_piece(const PG& c) {
  if constexpr (I < 2) {
    constexpr int gw = G + 3, pass = gw < 16 ? 0 : (gw < 32 ? 1 : 2), lg = gw - (pass == 0 ? 0 : (pass == 1 ? 16 : 32));
    const unsigned m0 = c.lds0 + WRING + (gw & 3) * WSLOT + c.aw * 2048 + I * 1024, so = (unsigned)(lg * 32 * 8192) + c.aw * 65536;
    dma16(m0, c.woff + I * 128, pass == 0 ? c.sUK : (pass == 1 ? c.sUV : c.sWQ), so);
  } else if constexpr (I < 6) {
    constexpr int i = I - 2, T = (G >> 1) + 2, half = G & 1;
    const unsigned m0 = c.lds0 + (T % 3) * XSLOT + (16 * half + i) * 1024 + c.aw * 4096;
    if constexpr (T < 16) dma16(m0, c.xoC[i & 1], c.sC, (unsigned)((128 * half + 8 * i) * 1024 + (T & 7) * 128) + c.aw * 32768);
    else dma16(m0, c.xoX[i & 1], c.sX, (half ? c.rowS : c.rowL) + (unsigned)(8 * i * 1152 + (T - 16) * 128) + (T > 24 ? 0x40000000u : 0u) + c.aw * (32 * 1152));
  } else {
    constexpr int f0 = TAc(G + 4), nf = (TAc(G + 5) - f0) * 3;
    int q = 2 * c.aw + (I - 6); if (q >= nf) q = q - nf < nf ? q - nf : 0;
    const int kt = f0 + q / 3, part = q % 3;
    dma16(rfl(c.lds0 + ARING + (kt % 10) * ATB + part * 1024), c.voff, c.sA, rfl((unsigned)(kt * ATB + part * 1024)));
  }
}
DI void st16_sc1(bf16x8 v, unsigned voff, __amdgpu_buffer_rsrc_t srd, unsigned soff) {
  asm volatile("buffer_store_dwordx4 %0, %1, %2, %3 offen sc1\n\ts_nop 1" :: "v"(v), "v"(voff), "s"(srd), "s"(soff) : "memory");
}
struct FragKV { bf16x8 x[4]; s16x4 wl[2], wh[2]; };
template <int G, int SS, int I> DI void kv_ld(const PG& c, FragKV& f) {
  constexpr int T = G >> 1;
  lds_cptr xp = c.xb[2 * (G & 1) + SS] + (T % 3) * XSLOT;
  lds_cptr wp = c.wb + (G & 3) * WSLOT + SS * 4096;
  if constexpr (I == 0) f.x[0] = lds_rd16(xp);
  else if constexpr (I == 1) f.wl[0] = lds_tr(wp);
  else if constexpr (I == 2) f.wh[0] = lds_tr(wp + 256);
  else if constexpr (I == 3) f.wl[1] = lds_tr(wp + 512);
  else if constexpr (I == 4) f.wh[1] = lds_tr(wp + 768);
  else f.x[I - 4] = lds_rd16(xp + (I - 4) * 4096);
}
template <bool ISV, int M> DI void kv_mfma(f32x16 (&acc)[4][2], const FragKV& f) {
  constexpr int r = M >> 1, ct = M & 1;
  const bf16x8 wf = cat(f.wl[ct], f.wh[ct]);
  acc[r][ct] = ISV ? MFMA(f.x[r], wf, acc[r][ct]) : MFMA(wf, f.x[r], acc[r][ct]);
}
struct KVSt { __amdgpu_buffer_rsrc_t srd; unsigned voff; };
constexpr int relK(int i) { return ((i >> 2) * 8 + ((i >> 1) & 1) * 2 + (i & 1)) * 1024; }
constexpr int relV(int i) { return (((i >> 2) >> 1) * 16 + (2 * ((i >> 2) & 1) + (i & 1)) * 4 + ((i >> 1) & 1)) * 1024; }
template <bool ISV, int GN, int SN, bool LOAD, int STI, bool PV, int DG, int DB, int M> DI void kv_gap(const PG& c, f32x16 (&acc)[4][2], const FragKV& cur, FragKV& nxt, const bf16x8 (&kvp)[16], const KVSt& ks) {
  kv_mfma<ISV, M>(acc, cur);
  if constexpr (LOAD) kv_ld<GN, SN, M>(c, nxt);
  if constexpr ((M & 1) && DB + (M >> 1) < 8) g_piece<DG, DB + (M >> 1)>(c);
  if constexpr (STI >= 0 && M == 6) st16_sc1(kvp[STI], ks.voff, ks.srd, (unsigned)(PV ? relV(STI) : relK(STI)));
  SBAR();
}
template <bool ISV, int GN, int SN, bool LOAD, int STI, bool PV, int DG, int DB> DI void kv_phase(const PG& c, f32x16 (&acc)[4][2], const FragKV& cur, FragKV& nxt, const bf16x8 (&kvp)[16], const KVSt& ks) {
  kv_gap<ISV, GN, SN, LOAD, STI, PV, DG, DB, 0>(c, acc, cur, nxt, kvp, ks); kv_gap<ISV, GN, SN, LOAD, STI, PV, DG, DB, 1>(c, acc, cur, nxt, kvp, ks);
  kv_gap<ISV, GN, SN, LOAD, STI, PV, DG, DB, 2>(c, acc, cur, nxt, kvp, ks); kv_gap<ISV, GN, SN, LOAD, STI, PV, DG, DB, 3>(c, acc, cur, nxt, kvp, ks);
  kv_gap<ISV, GN, SN, LOAD, STI, PV, DG, DB, 4>(c, acc, cur, nxt, kvp, ks); kv_gap<ISV, GN, SN, LOAD, STI, PV, DG, DB, 5>(c, acc, cur, nxt, kvp, ks);
  kv_gap<ISV, GN, SN, LOAD, STI, PV, DG, DB, 6>(c, acc, cur, nxt, kvp, ks); kv_gap<ISV, GN, SN, LOAD, STI, PV, DG, DB, 7>(c, acc, cur, nxt, kvp, ks);
}
constexpr int nSt(int g) { return (g >= 16 && g < 32) ? 1 : ((g >= 32 && g < 40) ? 2 : 0); }
constexpr int FLAGG = 43;
template <int PASS, int G> DI void g_step(const PG& c, f32x16 (&acc)[4][2], FragKV& fa, FragKV& fb, const bf16x8 (&kvp)[16], const KVSt& ks, char* flag) {
  constexpr int G0 = PASS == 0 ? 0 : (PASS == 1 ? 16 : 32), LG = G - G0;
  if constexpr (G == 0) wait_bar<12>(); else if constexpr (G == 1) wait_bar<8>(); else wait_bar<nSt(G - 2) + 8 + nSt(G - 1)>();
  SBAR();
  if constexpr (G == FLAGG) { if (threadIdx.x == 256) __hip_atomic_store((__attribute__((address_space(1))) unsigned*)flag, 1u, __ATOMIC_RELAXED, __HIP_MEMORY_SCOPE_AGENT); }
  if constexpr (G == 0) {
    kv_ld<0, 0, 0>(c, fa); kv_ld<0, 0, 1>(c, fa); kv_ld<0, 0, 2>(c, fa); kv_ld<0, 0, 3>(c, fa);
    kv_ld<0, 0, 4>(c, fa); kv_ld<0, 0, 5>(c, fa); kv_ld<0, 0, 6>(c, fa); kv_ld<0, 0, 7>(c, fa); SBAR();
  }
  constexpr int STA = PASS == 1 ? LG : ((PASS == 2 && LG < 8) ? 2 * LG : -1);
  constexpr int STB = (PASS == 2 && LG < 8) ? 2 * LG + 1 : -1;
  kv_phase<PASS == 1, G, 1, true, -1, PASS == 2, G, 0>(c, acc, fa, fb, kvp, ks);
  kv_phase<PASS == 1, G + 1, 0, (G + 1 < NPSTEP), -1, PASS == 2, G, 4>(c, acc, fb, fa, kvp, ks);
  if constexpr (STA >= 0) st16_sc1(kvp[STA], ks.voff, ks.srd, (unsigned)(PASS == 2 ? relV(STA) : relK(STA)));
  if constexpr (STB >= 0) st16_sc1(kvp[STB], ks.voff, ks.srd, (unsigned)relV(STB));
  SBAR();
}
template <int PASS, int G, int GEND> struct GLoop { static DI void run(const PG& c, f32x16 (&acc)[4][2], FragKV& fa, FragKV& fb, const bf16x8 (&kvp)[16], const KVSt& ks, char* flag) {
  g_step<PASS, G>(c, acc, fa, fb, kvp, ks, flag); if constexpr (G + 1 < GEND) GLoop<PASS, G + 1, GEND>::run(c, acc, fa, fb, kvp, ks, flag); } };

__global__ __launch_bounds__(512, 2) void k0_aproj(K1P p) {
  extern __shared__ __attribute__((aligned(16))) char lds[];
  const int tid = threadIdx.x, lane = tid & 63, l32 = lane & 31, hi = lane >> 5;
  const int b = blockIdx.x, xcd = b & 7, ii = b >> 3;
  struct { unsigned lds0; } c; c.lds0 = (unsigned)(size_t)(lds_ptr)lds;
  const int w = rfl(tid >> 6);
  const int blk = (lane >> 4) & 1, q4 = (lane & 15) >> 2, p4 = lane & 3;
  if (tid == 0) { *(unsigned*)(p.ws + WS_FLAG + b * 128) = 0u; if (b < 192) *(unsigned*)(p.ws + WS_CNT + b * 128) = 0u; *(unsigned*)(p.ws + WS_DONE + b * 128) = 0u; }
  if (ii >= 28) {
    const int t = (b & 7) + 8 * (ii - 28);
    unsigned v = 0;
    { const int i = t * 512 + tid; if (i < 2048 * 2) v ^= *(const unsigned*)((const char*)p.cx + (size_t)(i >> 1) * 1024 + (i & 1) * 128); }
    { const int i = t * 512 + tid; if (i < 96 * 64) v ^= *(const unsigned*)((const char*)p.wuk + (size_t)i * 128); }
    if (v == 0x9E3779B9u) *(unsigned*)(p.ws + WS_FLAG + 256 * 128 + 64) = v;
  }
  if (ii < 20) {
    const int rg = 4 * xcd + ii / 5, cg = ii % 5;
    const __amdgpu_buffer_rsrc_t sAX = mksrd((const char*)p.ax + (size_t)rg * 64 * 1152, 64 * 1152);
    const __amdgpu_buffer_rsrc_t sAQ = mksrd((const char*)p.awq + cg * 256, 576 * 1152 - cg * 256);
    constexpr int ASL = 73728, AWO = 24576;
    const unsigned xv = (unsigned)((lane >> 3) * 1152 + (((lane & 7) ^ ((4 * (w & 1) + (lane >> 4)) & 7)) * 16));
    const unsigned wv = (unsigned)(((lane & 31) >> 2) * 1152 + (32 * (lane >> 5) + 8 * (lane & 3)) * 2);
    auto issue = [&](int th) {
      const unsigned base = c.lds0 + (th & 1) * ASL;
#pragma unroll
      for (int kc = 0; kc < 3; ++kc) dma16(rfl(base + (kc * 8 + w) * 1024), xv, sAX, rfl((unsigned)(w * 8 * 1152 + (th * 3 + kc) * 128)));
#pragma unroll
      for (int i = 0; i < 6; ++i) {
        const int g = 3 * w + (i >> 1), pr = i & 1;
        dma16(rfl(base + AWO + (g * 2 + pr) * 1024), wv, sAQ, rfl((unsigned)((th * 192 + g * 8) * 1152 + pr * 128)));
      }
    };
    const int rt = w >> 2, ct = w & 3, tile = 4 * cg + ct;
    f32x16 acc = f32x16{};
    issue(0); issue(1);
    for (int th = 0; th < 3; ++th) {
      if (th < 2) asm volatile("s_waitcnt vmcnt(9) lgkmcnt(0)\n\ts_barrier" ::: "memory");
      else asm volatile("s_waitcnt vmcnt(0) lgkmcnt(0)\n\ts_barrier" ::: "memory");
      lds_cptr xp = (lds_cptr)lds + (th & 1) * ASL + rt * 4096 + 128 * l32;
      lds_cptr wp = (lds_cptr)lds + (th & 1) * ASL + AWO + ct * 512 + 2048 * hi + 64 * q4 + 32 * blk + 8 * p4;
      bf16x8 xf[4]; s16x4 wl[4], wh[4];
#define ALD(k) do { xf[(k) & 3] = lds_rd16(xp + ((k) >> 2) * 8192 + 16 * ((2 * ((k) & 3) + hi) ^ ((l32 >> 1) & 7))); wl[(k) & 3] = lds_tr(wp + (k) * 4096); wh[(k) & 3] = lds_tr(wp + (k) * 4096 + 256); } while (0)
      if (tile < 18) {
        ALD(0); ALD(1); ALD(2);
#pragma unroll
        for (int k = 0; k < 12; ++k) {
          if (k + 3 < 12) ALD(k + 3);
          acc = MFMA(cat(wl[k & 3], wh[k & 3]), xf[k & 3], acc);
          SBAR();
        }
      }
      if (th == 0) { asm volatile("s_waitcnt lgkmcnt(0)\n\ts_barrier" ::: "memory"); issue(2); }
    }
    const int row = (rg * 2 + rt) * 32 + l32;
    if (tile < 18) {
#pragma unroll
      for (int g = 0; g < 4; ++g) {
        u32x2 v = {cvtpk(acc[4 * g] * CA, acc[4 * g + 1] * CA), cvtpk(acc[4 * g + 2] * CA, acc[4 * g + 3] * CA)};
        *(u32x2*)(p.ws + WS_QA + (size_t)row * 1152 + (tile * 32 + 8 * g + 4 * hi) * 2) = v;
      }
    }
  } else if (ii < 28) {
    const int r32 = 8 * xcd + (ii - 20);
    const __amdgpu_buffer_rsrc_t sAX = mksrd((const char*)p.ax + (size_t)r32 * 32 * 1152, 32 * 1152);
    for (int pc = w; pc < 36; pc += 8) {
      const int kc = pc >> 2, rq = pc & 3, r = lane >> 3, row = 8 * rq + r, ch = (lane & 7) ^ ((row >> 1) & 7);
      dma16(rfl(c.lds0 + pc * 1024), (unsigned)(row * 1152 + ch * 16), sAX, rfl((unsigned)(kc * 128)));
    }
    {
      const unsigned* gk = (const unsigned*)p.awk; const unsigned* gv = (const unsigned*)p.awv;
      unsigned vk[11], vv[11];
#pragma unroll
      for (int it = 0; it < 11; ++it) { const int i = tid + it * 512; const int ic = i < 5184 ? i : 0; vk[it] = gk[ic]; vv[it] = gv[ic]; }
#pragma unroll
      for (int it = 0; it < 11; ++it) {
        const int i = tid + it * 512;
        if (i < 5184) { const int r = i / 9, d = i - r * 9; char* dst = lds + 36864 + (r >> 3) * 1024 + (r & 7) * 64 + d * 4; *(unsigned*)dst = vk[it]; *(unsigned*)(dst + 512) = vv[it]; }
      }
    }
    asm volatile("s_waitcnt vmcnt(0) lgkmcnt(0)\n\ts_barrier" ::: "memory");
    const int isv = w >> 2, kq = w & 3;
    f32x16 acc = f32x16{};
    lds_cptr xp = (lds_cptr)lds + 128 * l32;
    lds_cptr wp = (lds_cptr)lds + 36864 + isv * 512 + 1024 * hi + 64 * q4 + 32 * blk + 8 * p4;
#pragma unroll
    for (int k9 = 0; k9 < 9; ++k9) {
      const int k = kq * 9 + k9;
      const bf16x8 xf = lds_rd16(xp + (k >> 2) * 4096 + 16 * ((2 * (k & 3) + hi) ^ ((l32 >> 1) & 7)));
      const bf16x8 wf = cat(lds_tr(wp + k * 2048), lds_tr(wp + k * 2048 + 256));
      if (isv) acc = MFMA(xf, wf, acc); else acc = MFMA(wf, xf, acc);
    }
    asm volatile("s_waitcnt lgkmcnt(0)\n\ts_barrier" ::: "memory");
    float* red = (float*)lds + w * 1024;
#pragma unroll
    for (int q = 0; q < 4; ++q) *(f32x4*)(red + q * 256 + lane * 4) = f32x4{acc[4 * q], acc[4 * q + 1], acc[4 * q + 2], acc[4 * q + 3]};
    __syncthreads();
    if (kq == 0) {
#pragma unroll
      for (int o = 1; o < 4; ++o)
#pragma unroll
        for (int q = 0; q < 4; ++q) { const f32x4 y = *(const f32x4*)(red + o * 1024 + q * 256 + lane * 4); acc[4 * q] += y[0]; acc[4 * q + 1] += y[1]; acc[4 * q + 2] += y[2]; acc[4 * q + 3] += y[3]; }
      const int row = r32 * 32 + l32;
      char* hb = p.ws + WS_KA + (size_t)r32 * ATB;
      if (!isv) {
        char* k0 = hb;
        const unsigned w0 = cvtpk(acc[0], acc[1]), w1 = cvtpk(acc[2], acc[3]), w2 = cvtpk(acc[4], acc[5]), w3 = cvtpk(acc[6], acc[7]), w4 = cvtpk(acc[8], acc[9]);
        *(u32x2*)(k0 + (l32) * 16 + 8 * hi) = u32x2{w0, w1};
        *(u32x2*)(k0 + (l32 + 32) * 16 + 8 * hi) = u32x2{w2, w3};
        if (hi == 0) *(u32x4*)(hb + 1024 + l32 * 16) = u32x4{w4, 0x00003F80u, 0u, 0u};
        auto sq = [](unsigned x) { const float a = __uint_as_float(x << 16), bq = __uint_as_float(x & 0xFFFF0000u); return a * a + bq * bq; };
        float s2 = sq(w0) + sq(w1) + sq(w2) + sq(w3) + (hi == 0 ? sq(w4) : 0.f);
        s2 = hsum(s2);
#pragma unroll
        for (int o = 16; o >= 1; o >>= 1) s2 = fmaxf(s2, __shfl_xor(s2, o));
        if (lane == 0) *(float*)(p.ws + WS_KMAX + r32 * 4) = s2;
      } else {
#pragma unroll
        for (int g = 0; g < 2; ++g) {
          bf16x8 v = pack8n(acc, g);
          if (l32 == 18) v = bf16x8{0x3F80, 0x3F80, 0x3F80, 0x3F80, 0x3F80, 0x3F80, 0x3F80, 0x3F80};
          if (l32 < 19) *(bf16x8*)(hb + 1536 + ((g * 2 + hi) * 19 + l32) * 16) = v;
        }
      }
    }
  }

}

constexpr float LTHR = 7.9228162514264338e28f;
#define LTHR_V LTHR
struct AttB {
  f32x16 o[4]; bf16x8 qf[8]; f32x16 negm; float m, l;
  f32x16 s0, s1;
  unsigned pw[16];
  bf16x8 fr[4];
  float ps;
};
DI void cmask1(f32x16& s, int key0, int row) {
  const int d = row - key0;
#pragma unroll
  for (int r = 0; r < 16; ++r) { const int c = (r & 3) + 8 * (r >> 2); s[r] = (c > d) ? -INFINITY : s[r]; }
}
DI float max16(const f32x16& s) { float a = fmaxf(fmaxf(s[0], s[1]), s[2]);
#pragma unroll
  for (int r = 3; r < 15; r += 2) a = fmaxf(fmaxf(a, s[r]), s[r + 1]);
  return fmaxf(a, s[15]); }
DI f32x16 splat16(float x) { f32x16 v;
#pragma unroll
  for (int r = 0; r < 16; ++r) v[r] = x;
  return v; }
#define PKB(w) __builtin_bit_cast(bf16x8, w)
#define EXP2(x) __builtin_amdgcn_exp2f(x)
DI bf16x8 pfrag(const AttB& a, int ks) { u32x4 w = {a.pw[4 * ks], a.pw[4 * ks + 1], a.pw[4 * ks + 2], a.pw[4 * ks + 3]}; return __builtin_bit_cast(bf16x8, w); }

template <bool HASCUR> DI void rare_fix(AttB& a, __amdgpu_buffer_rsrc_t sK, __amdgpu_buffer_rsrc_t sQ, unsigned voff, unsigned kso, unsigned qso, int key0, int row, bool mask) {
  float tm = -INFINITY;
  f32x16 t;
#pragma unroll 1
  for (int kb = 0; kb < 2; ++kb) {
    t = f32x16{};
#pragma unroll 2
    for (int ds = 0; ds < 8; ++ds) t = MFMA(bld16s(sK, voff, kso + kb * 8192 + ds * 1024), bld16(sQ, voff, qso + ds * 1024), t);
    if (mask) cmask1(t, key0 + 32 * kb, row);
    tm = fmaxf(tm, max16(t));
  }
  tm = hmax(tm);
  const float mn = fmaxf(a.m, tm), dl = mn - a.m, f = EXP2(-dl);
#pragma unroll
  for (int db = 0; db < 4; ++db)
#pragma unroll
    for (int r = 0; r < 16; ++r) a.o[db][r] *= f;
  float ps = 0.f;
#pragma unroll 1
  for (int kb = 0; kb < 2; ++kb) {
    t = f32x16{};
#pragma unroll 2
    for (int ds = 0; ds < 8; ++ds) t = MFMA(bld16s(sK, voff, kso + kb * 8192 + ds * 1024), bld16(sQ, voff, qso + ds * 1024), t);
    if (mask) cmask1(t, key0 + 32 * kb, row);
#pragma unroll
    for (int r = 0; r < 16; ++r) { t[r] = EXP2(t[r] - mn); ps += t[r]; }
    if (kb == 0) {
#pragma unroll
      for (int k = 0; k < 8; ++k) a.pw[k] = cvtpk(t[2 * k], t[2 * k + 1]);
    } else {
#pragma unroll
      for (int k = 0; k < 8; ++k) a.pw[8 + k] = cvtpk(t[2 * k], t[2 * k + 1]);
    }
  }
  a.l *= f; a.ps = ps;
  if (HASCUR) {
#pragma unroll
    for (int r = 0; r < 16; ++r) a.s0[r] -= dl;
  }
  a.m = mn; a.negm = splat16(-mn); PIN(a.negm);
}

struct StepCtx {
  lds_cptr kp;
  lds_cptr kpn;
  lds_cptr vp;
  unsigned m0k, m0v, sok, sov; bool dk, dv;
  unsigned voff;
};
constexpr int M_STEADY = 0, M_FIRST = 1, M_SWITCH = 2, M_DRAIN = 3;
template <int I, int MODE> DI void gapA(AttB& a, const StepCtx& c, __amdgpu_buffer_rsrc_t sK, __amdgpu_buffer_rsrc_t sV) {
  constexpr bool PREV = MODE != M_FIRST, CUR = MODE != M_DRAIN, NEW = MODE == M_FIRST || MODE == M_SWITCH;
  constexpr int ds = I & 7;
  if constexpr (CUR) {
    if constexpr (I < 8) { if constexpr (ds == 0) a.s0 = NEW ? MFMA(a.fr[I & 3], a.qf[0], f32x16{}) : MFMA(a.fr[I & 3], a.qf[0], a.negm); else a.s0 = MFMA(a.fr[I & 3], a.qf[ds], a.s0); }
    else { if constexpr (ds == 0) a.s1 = NEW ? MFMA(a.fr[I & 3], a.qf[0], f32x16{}) : MFMA(a.fr[I & 3], a.qf[0], a.negm); else a.s1 = MFMA(a.fr[I & 3], a.qf[ds], a.s1); }
  }
  if constexpr (I + 3 < 16) { if constexpr (CUR) a.fr[(I + 3) & 3] = lds_rd16(c.kp + (I + 3) * 1024); }
  else if constexpr (PREV) a.fr[(I + 3) & 3] = lds_rd16(c.vp + (I + 3 - 16) * 1024);
  if constexpr (I < 8) {
    if constexpr (PREV) {
      a.s1[8 + I] = EXP2(a.s1[8 + I]);
      a.ps += a.s1[7 + I];
      if constexpr (I < 4) { a.pw[8 + I] = cvtpk(a.s1[2 * I], a.s1[2 * I + 1]); PIN(a.pw[8 + I]); }
      else if constexpr (I != 6) { constexpr int k = I == 7 ? 6 : I; a.pw[8 + k] = cvtpk(a.s1[2 * k], a.s1[2 * k + 1]); PIN(a.pw[8 + k]); }
    }
    if constexpr (I & 1) { if (MODE != M_DRAIN || c.dv) dma16os<(I >> 1) * 1024>(c.m0v, c.voff, sV, c.sov); }
  } else {
    constexpr int i = I - 8;
    if constexpr (MODE == M_STEADY) {
      a.s0[i] = EXP2(a.s0[i]);
      if constexpr (i >= 1) a.ps += a.s0[i - 1];
    }
    if constexpr (i & 1) { if (MODE != M_DRAIN || c.dk) dma16os<(i >> 1) * 1024>(c.m0k, c.voff, sK, c.sok); }
  }
  PIN(a.ps);
  SBAR();
}
template <int I, int MODE> DI void gapB(AttB& a, const StepCtx& c, float& mx0, float& mx1) {
  constexpr bool PREV = MODE != M_FIRST, NEW = MODE == M_FIRST || MODE == M_SWITCH;
  constexpr int ks = I >> 2, db = I & 3;
  if constexpr (PREV) {
    a.o[db] = MFMA(a.fr[I & 3], pfrag(a, ks), a.o[db]);
    if constexpr (I + 3 < 16) a.fr[(I + 3) & 3] = lds_rd16(c.vp + (I + 3) * 1024);
  }
  if constexpr (I + 3 >= 16) a.fr[(I + 3) & 3] = lds_rd16(c.kpn + (I + 3 - 16) * 1024);
  if constexpr (MODE == M_STEADY) {
    if constexpr (I < 8) {
      a.s0[8 + I] = EXP2(a.s0[8 + I]);
      a.ps += a.s0[7 + I];
      if constexpr (I >= 4) { constexpr int k = I - 4; a.pw[k] = cvtpk(a.s0[2 * k], a.s0[2 * k + 1]); PIN(a.pw[k]); }
    } else {
      constexpr int i = I - 8;
      a.s1[i] = EXP2(a.s1[i]);
      if constexpr (i == 0) a.ps += a.s0[15]; else a.ps += a.s1[i - 1];
      if constexpr (I < 12) { constexpr int k = I - 4; a.pw[k] = cvtpk(a.s0[2 * k], a.s0[2 * k + 1]); PIN(a.pw[k]); }
    }
  } else if constexpr (NEW) {
    if constexpr (I < 4) {
      constexpr int r = 4 * I;
      if constexpr (I == 0) { mx0 = fmaxf(fmaxf(a.s0[0], a.s0[1]), a.s0[2]); mx1 = fmaxf(fmaxf(a.s1[0], a.s1[1]), a.s1[2]); mx0 = fmaxf(mx0, a.s0[3]); mx1 = fmaxf(mx1, a.s1[3]); }
      else { mx0 = fmaxf(fmaxf(mx0, a.s0[r]), a.s0[r + 1]); mx1 = fmaxf(fmaxf(mx1, a.s1[r]), a.s1[r + 1]); mx0 = fmaxf(fmaxf(mx0, a.s0[r + 2]), a.s0[r + 3]); mx1 = fmaxf(fmaxf(mx1, a.s1[r + 2]), a.s1[r + 3]); }
      PIN(mx0); PIN(mx1);
    } else {
      if constexpr (I == 4) { mx0 = fmaxf(hmax(fmaxf(mx0, mx1)), -1e30f); a.m = mx0; a.ps = 0.f; }
      if constexpr (I < 12) {
        constexpr int i = 2 * (I - 4);
        a.s0[i] = EXP2(a.s0[i] - mx0); a.s0[i + 1] = EXP2(a.s0[i + 1] - mx0);
        if constexpr (i >= 2) { a.ps += a.s0[i - 2]; a.ps += a.s0[i - 1]; }
      } else {
        constexpr int i = 2 * (I - 12);
        if constexpr (I == 12) { a.ps += a.s0[14]; a.ps += a.s0[15]; }
        a.s1[i] = EXP2(a.s1[i] - mx0); a.s1[i + 1] = EXP2(a.s1[i + 1] - mx0);
        a.s1[8 + i] -= mx0; a.s1[9 + i] -= mx0;
        if constexpr (i >= 2) { a.ps += a.s1[i - 2]; a.ps += a.s1[i - 1]; }
      }
      if constexpr (I >= 8) { constexpr int k = I - 8; if constexpr (2 * k + 1 < 2 * (I - 4)) { a.pw[k] = cvtpk(a.s0[2 * k], a.s0[2 * k + 1]); PIN(a.pw[k]); } }
    }
  }
  PIN(a.ps);
  SBAR();
}
template <int O, int MODE, int... Is> DI void phaseA_(AttB& a, const StepCtx& c, __amdgpu_buffer_rsrc_t sK, __amdgpu_buffer_rsrc_t sV, std::integer_sequence<int, Is...>) { (gapA<O + Is, MODE>(a, c, sK, sV), ...); }
template <int MODE, int... Is> DI void phaseB_(AttB& a, const StepCtx& c, float& mx0, float& mx1, std::integer_sequence<int, Is...>) { (gapB<Is, MODE>(a, c, mx0, mx1), ...); }

struct K2P { char* ws; };
struct AttEnv {
  int grp, wq, lane, hi, l32, n1, L, head; char* lds; unsigned lds0; char* ws;
  __amdgpu_buffer_rsrc_t sK, sV, sQ, sC;
};
DI int tile_of(const AttEnv& e, int x) { return e.grp == 0 ? x : (x < e.n1 ? x : 17 + (x - e.n1)); }
DI void step_top(const AttEnv& e, int s, StepCtx& c) {
  asm volatile("s_waitcnt vmcnt(0)\n\ts_barrier" ::: "memory");
  SBAR();
  const unsigned gb = e.grp * 81920;
  const int k3 = s % 3, k3n = (s + 1) % 3, k3d = (s + 2) % 3;
  c.kp = (lds_cptr)e.lds + gb + k3 * 16384 + e.lane * 16;
  c.kpn = (lds_cptr)e.lds + gb + k3n * 16384 + e.lane * 16;
  c.vp = (lds_cptr)e.lds + gb + 49152 + ((s + 1) & 1) * 16384 + e.lane * 16;
  c.dk = (s + 2 < 17); c.dv = (s < 17);
  c.m0k = rfl(e.lds0 + gb + k3d * 16384 + e.wq * 4096); c.m0v = rfl(e.lds0 + gb + 49152 + (s & 1) * 16384 + e.wq * 4096);
  c.sok = rfl((unsigned)(tile_of(e, s + 2) * 16384 + e.wq * 4096)); c.sov = rfl((unsigned)(tile_of(e, s) * 16384 + e.wq * 4096));
  c.voff = e.lane * 16;
}
DI void dma_all(const AttEnv& e, const StepCtx& c) {
  if (c.dk) {
#pragma unroll
    for (int i = 0; i < 4; ++i) dma16s(c.m0k + i * 1024, c.voff, e.sK, c.sok + i * 1024); }
  if (c.dv) {
#pragma unroll
    for (int i = 0; i < 4; ++i) dma16s(c.m0v + i * 1024, c.voff, e.sV, c.sov + i * 1024); }
}
template <bool HASCUR> DI void check_prev(AttB& a, const AttEnv& e, int qb, int prev_t) {
  if (__builtin_expect(__any(!(a.l + a.ps <= LTHR_V)), 0)) {
    const int rmin = qb * 128 + e.wq * 32;
    rare_fix<HASCUR>(a, e.sK, e.sQ, e.lane * 16, rfl((unsigned)(prev_t * 16384)), rfl((unsigned)(((qb == e.L ? 0 : 4) + e.wq) * 8192)),
                     prev_t * 64 + 4 * e.hi, rmin + e.l32, prev_t * 64 + 63 > rmin);
  }
  a.l += a.ps; a.ps = 0.f;
}
DI void store_block(AttB& a, const AttEnv& e, int qb) {
  const float inv = 1.f / hsum(a.l);
#pragma unroll
  for (int db = 0; db < 4; ++db)
#pragma unroll
    for (int g = 0; g < 2; ++g)
      bst16s(pack8(a.o[db], g, inv), e.sC, e.lane * 16, rfl((unsigned)(((qb * 4 + e.wq) * 256 + db * 2 + g) * 1024)));
}
template <int MODE> DI void att_step(AttB& a, const AttEnv& e, int s, int qb_prev, int qb, bool loadq = false) {
  constexpr bool PREV = MODE != M_FIRST, CUR = MODE != M_DRAIN, NEW = MODE == M_FIRST || MODE == M_SWITCH;
  StepCtx c; step_top(e, s, c);
  if constexpr (NEW) asm volatile("" : "+v"(a.qf[0]), "+v"(a.qf[1]), "+v"(a.qf[2]), "+v"(a.qf[3]), "+v"(a.qf[4]), "+v"(a.qf[5]), "+v"(a.qf[6]), "+v"(a.qf[7]));
  const int t = tile_of(e, s), rmin = qb * 128 + e.wq * 32;
  const bool mask = t * 64 + 63 > rmin;
  if constexpr (MODE == M_FIRST) {
#pragma unroll
    for (int J = 0; J < 3; ++J) a.fr[J] = lds_rd16(c.kp + J * 1024);
  }
  phaseA_<0, MODE>(a, c, e.sK, e.sV, std::make_integer_sequence<int, 8>{});
  if constexpr (PREV) {
    a.ps += a.s1[15]; a.pw[15] = cvtpk(a.s1[14], a.s1[15]);
    check_prev<MODE == M_STEADY>(a, e, qb_prev, tile_of(e, s - 1));
  }
  if constexpr (MODE == M_STEADY) { if (mask) cmask1(a.s0, t * 64 + 4 * e.hi, rmin + e.l32); }
  SBAR();
  phaseA_<8, MODE>(a, c, e.sK, e.sV, std::make_integer_sequence<int, 8>{});
  if constexpr (CUR) {
    if (mask) { if constexpr (NEW) cmask1(a.s0, t * 64 + 4 * e.hi, rmin + e.l32); cmask1(a.s1, t * 64 + 32 + 4 * e.hi, rmin + e.l32); }
  }
  if constexpr (MODE == M_FIRST || MODE == M_STEADY) {
    if (loadq) {
#pragma unroll
      for (int ds = 0; ds < 8; ++ds)
        asm volatile("buffer_load_dwordx4 %0, %1, %2, %3 offen" : "=v"(a.qf[ds]) : "v"(e.lane * 16), "s"(e.sQ), "s"(rfl((unsigned)(e.wq * 8192 + ds * 1024))) : "memory");
    }
  }
  SBAR();
  float mx0 = 0.f, mx1 = 0.f; const float lold = a.l;
  phaseB_<MODE>(a, c, mx0, mx1, std::make_integer_sequence<int, 16>{});
  if constexpr (MODE == M_FIRST || MODE == M_STEADY) {
    if (loadq) asm volatile("s_waitcnt vmcnt(0)" : "+v"(a.qf[0]), "+v"(a.qf[1]), "+v"(a.qf[2]), "+v"(a.qf[3]), "+v"(a.qf[4]), "+v"(a.qf[5]), "+v"(a.qf[6]), "+v"(a.qf[7]) :: "memory");
  }
  if constexpr (NEW) {
    a.ps += a.s1[6];
    if constexpr (MODE == M_SWITCH) { a.l = lold; store_block(a, e, qb_prev); }
#pragma unroll
    for (int db = 0; db < 4; ++db) a.o[db] = f32x16{};
    a.l = 0.f; a.negm = splat16(-a.m); PIN(a.negm);
  }
}

struct AttA { f32x16 o; bf16x8 qf0, qf1; };
#define E4(sc, i) do { sc[i] = EXP2(sc[i]); sc[i + 1] = EXP2(sc[i + 1]); sc[i + 2] = EXP2(sc[i + 2]); sc[i + 3] = EXP2(sc[i + 3]); } while (0)
#define C2(pw, j, sc, i) do { pw[j] = cvtpk(sc[i], sc[i + 1]); pw[j + 1] = cvtpk(sc[i + 2], sc[i + 3]); } while (0)
DI int TA(int g) { return (g * 64) / NPSTEP; }
DI void a_loop(AttA& A, AttA& B, char* lds, int lane, const char* flags, bool poller) {
  const int l32 = lane & 31, hi = lane >> 5;
  const int vrow = l32 < 19 ? l32 : (((l32 >= 20 && l32 <= 27)) ? 0 : 4);
  lds_cptr k0p = (lds_cptr)lds + ARING + lane * 16;
  lds_cptr k1p = (lds_cptr)lds + ARING + 1024 + l32 * 16;
  lds_cptr vp = (lds_cptr)lds + ARING + 1536 + (hi * 19 + vrow) * 16;
  f32x16 sA, sB, tA, tB; u32x4 pa0, pa1, pb0, pb1, qa0, qa1, qb0, qb1;
  bf16x8 k0, k1, v0, v1, w0, w1;
  int g = 0;
#define SLOT(kt) (((kt) % 10) * ATB)
#define LDK(kt) do { const int so_ = SLOT(kt); k0 = lds_rd16(k0p + so_); k1 = lds_rd16(k1p + so_); } while (0)
#define TOPCHK(kt) do { if (g < NPSTEP && TA(g) == (kt)) { asm volatile("s_barrier" ::: "memory"); SBAR(); ++g; } } while (0)
#define DMP(i) do { } while (0)
#define ASTEP(SC, SN, TC, TN, PP0, PP1, PN0, PN1, QP0, QP1, QN0, QN1, kt, VC0, VC1, VN0, VN1, DOQK, DOPV, DOLDK) do { \
    TOPCHK(kt); \
    const int sv_ = SLOT(kt); \
    if (DOQK) { SN = MFMA(k0, A.qf0, f32x16{}); } SBAR(); VN0 = lds_rd16(vp + sv_); VN1 = lds_rd16(vp + sv_ + 608); E4(SC, 0); PIN(SC); DMP(0); SBAR(); \
    if (DOQK) { TN = MFMA(k0, B.qf0, f32x16{}); } SBAR(); E4(TC, 0); PIN(TC); DMP(1); SBAR(); \
    if (DOPV) { A.o = MFMA(VC0, PKB(PP0), A.o); } SBAR(); E4(SC, 4); C2(PN0, 0, SC, 0); PIN(SC); PIN(PN0); DMP(2); SBAR(); \
    if (DOPV) { B.o = MFMA(VC0, PKB(QP0), B.o); } SBAR(); E4(TC, 4); C2(QN0, 0, TC, 0); PIN(TC); PIN(QN0); DMP(3); SBAR(); \
    if (DOQK) { SN = MFMA(k1, A.qf1, SN); } SBAR(); E4(SC, 8); C2(PN0, 2, SC, 4); PIN(SC); PIN(PN0); DMP(4); SBAR(); \
    if (DOQK) { TN = MFMA(k1, B.qf1, TN); } SBAR(); if (DOLDK) LDK((kt) + 2); E4(TC, 8); C2(QN0, 2, TC, 4); PIN(TC); PIN(QN0); DMP(5); SBAR(); \
    if (DOPV) { A.o = MFMA(VC1, PKB(PP1), A.o); } SBAR(); E4(SC, 12); C2(PN1, 0, SC, 8); C2(PN1, 2, SC, 12); PIN(SC); PIN(PN1); DMP(6); SBAR(); \
    if (DOPV) { B.o = MFMA(VC1, PKB(QP1), B.o); } SBAR(); E4(TC, 12); C2(QN1, 0, TC, 8); C2(QN1, 2, TC, 12); PIN(TC); PIN(QN1); DMP(7); SBAR(); } while (0)
  pa0 = u32x4{0,0,0,0}; pa1 = pa0; pb0 = pa0; pb1 = pa0; qa0 = pa0; qa1 = pa0; qb0 = pa0; qb1 = pa0; v0 = bf16x8{}; v1 = v0; w0 = v0; w1 = v0;
  asm volatile("s_barrier" ::: "memory"); SBAR();
  LDK(0); sA = MFMA(k0, A.qf0, f32x16{}); tA = MFMA(k0, B.qf0, f32x16{}); sA = MFMA(k1, A.qf1, sA); tA = MFMA(k1, B.qf1, tA); LDK(1);
  g = 1;
#define TOPCHK0(kt) do { } while (0)
  {
#pragma push_macro("TOPCHK")
#undef TOPCHK
#define TOPCHK(kt) TOPCHK0(kt)
    ASTEP(sA, sB, tA, tB, pb0, pb1, pa0, pa1, qb0, qb1, qa0, qa1, 0, v0, v1, w0, w1, true, false, true);
#pragma pop_macro("TOPCHK")
  }
#pragma unroll 1
  for (int kt = 1; kt < 61; kt += 2) {
    ASTEP(sB, sA, tB, tA, pa0, pa1, pb0, pb1, qa0, qa1, qb0, qb1, kt, w0, w1, v0, v1, true, true, true);
    ASTEP(sA, sB, tA, tB, pb0, pb1, pa0, pa1, qb0, qb1, qa0, qa1, kt + 1, v0, v1, w0, w1, true, true, true);
  }
  if (poller) {
    const __attribute__((address_space(1))) unsigned* fl = (const __attribute__((address_space(1))) unsigned*)(flags + (lane & 7) * 128);
    for (unsigned spin = 0; spin < (1u << 20); ++spin) {
      const unsigned v = __hip_atomic_load(fl, __ATOMIC_RELAXED, __HIP_MEMORY_SCOPE_AGENT);
      if (__all(v == 1u)) break;
      __builtin_amdgcn_s_sleep(2);
    }
    __builtin_amdgcn_fence(__ATOMIC_ACQUIRE, "agent");
  }
  ASTEP(sB, sA, tB, tA, pa0, pa1, pb0, pb1, qa0, qa1, qb0, qb1, 61, w0, w1, v0, v1, true, true, true);
  ASTEP(sA, sB, tA, tB, pb0, pb1, pa0, pa1, qb0, qb1, qa0, qa1, 62, v0, v1, w0, w1, true, true, false);
  ASTEP(sB, sA, tB, tA, pa0, pa1, pb0, pb1, qa0, qa1, qb0, qb1, 63, w0, w1, v0, v1, false, true, false);
  A.o = MFMA(v0, PKB(pb0), A.o); B.o = MFMA(v0, PKB(qb0), B.o); A.o = MFMA(v1, PKB(pb1), A.o); B.o = MFMA(v1, PKB(qb1), B.o);
#undef LDK
#undef ASTEP
#undef TOPCHK
#undef TOPCHK0
#undef DMP
#undef SLOT
}
DI void a_initq(AttA& A, const char* ws, int qrow, int ah, int lane, float km) {
  const int hi = lane >> 5;
  const char* qp = ws + WS_QA + (size_t)qrow * 1152 + ah * 36;
  u32x4 q0; { const unsigned* qq = (const unsigned*)(qp + 16 * hi); q0 = u32x4{qq[0], qq[1], qq[2], qq[3]}; }
  const unsigned q16 = *(const unsigned*)(qp + 32);
  float qs = 0.f;
#pragma unroll
  for (int e = 0; e < 4; ++e) { const float x = __uint_as_float(q0[e] << 16), y = __uint_as_float(q0[e] & 0xFFFF0000u); qs += x * x + y * y; }
  if (hi == 0) { const float x = __uint_as_float(q16 << 16), y = __uint_as_float(q16 & 0xFFFF0000u); qs += x * x + y * y; }
  qs = hsum(qs);
  const float bound = sqrtf(qs * km) * 1.0009765625f + 1e-30f;
  const unsigned bb = (__float_as_uint(bound) + 0xFFFFu) >> 16;
  A.qf0 = __builtin_bit_cast(bf16x8, q0);
  A.qf1 = __builtin_bit_cast(bf16x8, hi == 0 ? u32x4{q16, bb | 0x8000u, 0u, 0u} : u32x4{0u, 0u, 0u, 0u});
  A.o = f32x16{};
}
DI void a_finish(AttA& A, char* ws, int lane, int qrow, int ah) {
  const int l32 = lane & 31, hi = lane >> 5;
  f32x16 o = A.o;
  float l = o[10];
  { auto rr = __builtin_amdgcn_permlane32_swap(__float_as_uint(l), __float_as_uint(l), false, false); l = __uint_as_float(rr[0]); }
  if (__builtin_expect(__any(!(l >= 8.6736174e-19f)), 0)) {
    float m = -1e30f; o = f32x16{};
    bf16x8 qz = A.qf1; qz[2] = 0;
    const int vrow = l32 < 19 ? l32 : 0;
    for (int kt = 0; kt < 64; ++kt) {
      const char* hb = ws + WS_KA + (size_t)kt * ATB;
      const bf16x8 k0 = *(const bf16x8*)(hb + lane * 16), k1 = *(const bf16x8*)(hb + 1024 + l32 * 16);
      const bf16x8 v0 = *(const bf16x8*)(hb + 1536 + (hi * 19 + vrow) * 16), v1 = *(const bf16x8*)(hb + 1536 + 608 + (hi * 19 + vrow) * 16);
      f32x16 sc = MFMA(k0, A.qf0, f32x16{});
      sc = MFMA(k1, qz, sc);
      const float mn = fmaxf(m, hmax(max16(sc))), f = EXP2(m - mn);
      m = mn;
#pragma unroll
      for (int r = 0; r < 16; ++r) { sc[r] = EXP2(sc[r] - mn); o[r] *= f; }
      o = MFMA(v0, pack8n(sc, 0), o);
      o = MFMA(v1, pack8n(sc, 1), o);
    }
    l = o[10];
    auto rr = __builtin_amdgcn_permlane32_swap(__float_as_uint(l), __float_as_uint(l), false, false); l = __uint_as_float(rr[0]);
  }
  const float inv = 1.f / l;
  char* ob = ws + WS_OA + (size_t)(qrow >> 5) * 36 * 1024 + l32 * 16;
  auto st2 = [&](int d, float x, float y) { const int c = ah * 18 + d; __hip_atomic_store((__attribute__((address_space(1))) unsigned*)(ob + (c >> 4) * 1024 + ((c >> 2) & 1) * 512 + (((c >> 3) & 1) * 4 + (c & 3)) * 2), cvtpk(x * inv, y * inv), __ATOMIC_RELAXED, __HIP_MEMORY_SCOPE_AGENT); };
  st2(4 * hi, o[0], o[1]); st2(4 * hi + 2, o[2], o[3]); st2(8 + 4 * hi, o[4], o[5]); st2(10 + 4 * hi, o[6], o[7]);
  if (hi == 0) st2(16, o[8], o[9]);
}

constexpr int K3_WS = 24576;
struct K3C {
  unsigned lds0, w, xvoff, wvoff;
  __amdgpu_buffer_rsrc_t sX, sW;
  lds_cptr wb;
};
DI void xload(bf16x8& d, unsigned voff, __amdgpu_buffer_rsrc_t srd, unsigned soff) {
  asm volatile("buffer_load_dwordx4 %0, %1, %2, %3 offen sc1" : "=v"(d) : "v"(voff), "s"(srd), "s"(soff) : "memory");
}
template <int I> DI void k3_dmaW(const K3C& c, int st) {
  dma16(c.lds0 + (st & 3) * K3_WS + (c.w * 3 + I) * 1024, c.wvoff + I * 128, c.sW, (unsigned)((st * 64 + c.w * 8) * 1152));
}
struct K3F { s16x4 wl[3], wh[3]; };
DI void k3_stage(const K3C& c, f32x16 (&acc)[6], bf16x8& x, K3F& fa, K3F& fb, int st, unsigned xso) {
  const int so = (st & 3) * K3_WS, sn = ((st + 1) & 3) * K3_WS;
#pragma unroll
  for (int m = 0; m < 3; ++m) {
    acc[m] = MFMA(cat(fa.wl[m], fa.wh[m]), x, acc[m]);
    lds_cptr p = c.wb + so + (3 + m) * 512;
    fb.wl[m] = lds_tr(p); fb.wh[m] = lds_tr(p + 3072);
    if (m == 0) k3_dmaW<0>(c, st + 3);
    if (m == 2) k3_dmaW<1>(c, st + 3);
    SBAR();
  }
#pragma unroll
  for (int m = 0; m < 3; ++m) {
    acc[3 + m] = MFMA(cat(fb.wl[m], fb.wh[m]), x, acc[3 + m]);
    lds_cptr p = c.wb + sn + m * 512;
    fa.wl[m] = lds_tr(p); fa.wh[m] = lds_tr(p + 3072);
    if (m == 1) k3_dmaW<2>(c, st + 3);
    SBAR();
  }
  xload(x, c.xvoff, c.sX, xso);
  SBAR();
}
DI void k3_phase(const K1P& p, char* lds) {
  const int tid = threadIdx.x, lane = tid & 63, l32 = lane & 31, hi = lane >> 5;
  const int b = blockIdx.x;
  const bool isB = b < 192;
  K3C c;
  c.lds0 = (unsigned)(size_t)(lds_ptr)lds; c.w = rfl(tid >> 6);
  const int rt = c.w & 1, kq = c.w >> 1;
  const int blk = (lane >> 4) & 1, q4 = (lane & 15) >> 2, p4 = lane & 3;
  const int nst = isB ? 32 : 9;
  c.xvoff = lane * 16;
  { const int sub = lane >> 5, rr = (lane & 31) >> 2, c4 = lane & 3; c.wvoff = (unsigned)(rr * 1152 + (32 * sub + 8 * c4) * 2); }
  c.wb = (lds_cptr)lds + (2 * kq) * 3072 + 32 * blk + 8 * p4 + (4 * hi + q4) * 64;
  const int ntile = isB ? 1 : ((b - 192) < 32 ? 2 : 1);
#pragma unroll 1
  for (int it = 0; it < ntile; ++it) {
    int t, sk = 0;
    if (isB) { const int x = b & 7, i = b >> 3; t = x * 12 + (i >> 1); sk = i & 1; } else t = (b - 192) + 64 * it;
    const int rb = t / 3, cb = t - rb * 3;
    c.sW = isB ? mksrd((const char*)p.wo + (size_t)sk * 2048 * 1152 + cb * 384, 2048 * 1152 - cb * 384) : mksrd((const char*)p.awo + cb * 384, 576 * 1152 - cb * 384);
    {
      const char* xb = isB ? p.ws + WS_CF + ((size_t)(rb * 2 + rt) * 256 + sk * 128) * 1024 : p.ws + WS_OA + (size_t)(rb * 2 + rt) * 36 * 1024;
      c.sX = mksrd(xb + kq * 1024, nst * 4096 - kq * 1024);
    }
    f32x16 acc[6];
#pragma unroll
    for (int ct = 0; ct < 6; ++ct) acc[ct] = f32x16{};
    bf16x8 x0, x1, x2;
    if (it) asm volatile("s_waitcnt lgkmcnt(0)\n\ts_barrier" ::: "memory");
    k3_dmaW<0>(c, 0); k3_dmaW<1>(c, 0); k3_dmaW<2>(c, 0);
    k3_dmaW<0>(c, 1); k3_dmaW<1>(c, 1); k3_dmaW<2>(c, 1);
    k3_dmaW<0>(c, 2); k3_dmaW<1>(c, 2); k3_dmaW<2>(c, 2);
    if (c.w == 0) {
      const int beta = rb >> 1, pj = isB ? (beta < 8 ? beta : 15 - beta) : (rb >> 2);
      const int hd = isB ? 16 * sk + (lane & 15) : (lane & 31);
      const int pb = (hd >> 2) + 8 * ((hd & 3) + 4 * pj);
      const __attribute__((address_space(1))) unsigned* fl = (const __attribute__((address_space(1))) unsigned*)(p.ws + WS_DONE + pb * 128);
      for (unsigned spin = 0; spin < (1u << 20); ++spin) {
        const unsigned v = __hip_atomic_load(fl, __ATOMIC_RELAXED, __HIP_MEMORY_SCOPE_AGENT);
        if (__all(v == 1u)) break;
        __builtin_amdgcn_s_sleep(2);
      }
      __builtin_amdgcn_fence(__ATOMIC_ACQUIRE, "wavefront");
    }
    asm volatile("s_waitcnt vmcnt(0)\n\ts_barrier" ::: "memory");
    xload(x0, c.xvoff, c.sX, 0);
    xload(x1, c.xvoff, c.sX, 4096);
    (void)x2;
    K3F fa, fb;
#define K3_WAIT(N, X) asm volatile("s_waitcnt vmcnt(" #N ")\n\ts_barrier" : "+v"(X) :: "memory")
    K3_WAIT(1, x0); SBAR();
#pragma unroll
    for (int m = 0; m < 3; ++m) { fa.wl[m] = lds_tr(c.wb + m * 512); fa.wh[m] = lds_tr(c.wb + m * 512 + 3072); }
    SBAR();
    int st = 0;
    for (; st + 1 < nst; st += 2) {
      k3_stage(c, acc, x0, fa, fb, st, (unsigned)((st + 2) * 4096));
      K3_WAIT(4, x1); SBAR();
      k3_stage(c, acc, x1, fa, fb, st + 1, (unsigned)((st + 3) * 4096));
      K3_WAIT(4, x0); SBAR();
    }
    if (st < nst) k3_stage(c, acc, x0, fa, fb, st, (unsigned)((st + 2) * 4096));
    asm volatile("s_waitcnt vmcnt(0) lgkmcnt(0)\n\ts_barrier" : "+v"(x0), "+v"(x1) :: "memory");
    f32x4 h[12];
    {
      const bool up = (kq & 2) != 0;
      char* mine = lds + c.w * 12288; const char* theirs = lds + (c.w ^ 4) * 12288;
#pragma unroll
      for (int c3 = 0; c3 < 3; ++c3)
#pragma unroll
        for (int g = 0; g < 4; ++g) {
          const f32x16& give = up ? acc[c3] : acc[3 + c3];
          *(f32x4*)(mine + (c3 * 4 + g) * 1024 + lane * 16) = f32x4{give[4 * g], give[4 * g + 1], give[4 * g + 2], give[4 * g + 3]};
        }
      __syncthreads();
#pragma unroll
      for (int c3 = 0; c3 < 3; ++c3)
#pragma unroll
        for (int g = 0; g < 4; ++g) {
          const f32x16& keep = up ? acc[3 + c3] : acc[c3];
          h[c3 * 4 + g] = f32x4{keep[4 * g], keep[4 * g + 1], keep[4 * g + 2], keep[4 * g + 3]} + *(const f32x4*)(theirs + (c3 * 4 + g) * 1024 + lane * 16);
        }
      __syncthreads();
    }
    f32x4 q[6];
    {
      const bool up = (kq & 1) != 0;
      char* mine = lds + c.w * 6144; const char* theirs = lds + (c.w ^ 2) * 6144;
#pragma unroll
      for (int i = 0; i < 6; ++i) *(f32x4*)(mine + i * 1024 + lane * 16) = up ? h[i] : h[6 + i];
      __syncthreads();
#pragma unroll
      for (int i = 0; i < 6; ++i) q[i] = (up ? h[6 + i] : h[i]) + *(const f32x4*)(theirs + i * 1024 + lane * 16);
    }
    const int it0 = (kq & 2) * 6 + (kq & 1) * 6;
    auto store_out = [&](int coloff) {
      const int row = rb * 64 + rt * 32 + l32;
#pragma unroll
      for (int i = 0; i < 6; ++i) { const int item = it0 + i, ct = item >> 2, g = item & 3;
        *(u32x2*)((char*)p.out + (size_t)row * 2304 + coloff + (cb * 192 + ct * 32 + 8 * g + 4 * hi) * 2) = u32x2{cvtpk(q[i][0], q[i][1]), cvtpk(q[i][2], q[i][3])}; } };
    if (!isB) store_out(0);
    else {
      char* slab = p.ws + WS_PART + (size_t)(t * 2) * 12288;
      if (rt != sk) {
        const __amdgpu_buffer_rsrc_t sP = mksrd(slab + sk * 12288 + kq * 3072, 3072);
#pragma unroll
        for (int i = 0; i < 3; ++i)
          __builtin_amdgcn_raw_buffer_store_b128(u32x4{cvtpk(q[2 * i][0], q[2 * i][1]), cvtpk(q[2 * i][2], q[2 * i][3]), cvtpk(q[2 * i + 1][0], q[2 * i + 1][1]), cvtpk(q[2 * i + 1][2], q[2 * i + 1][3])}, sP, lane * 16 + i * 1024, 0, 16);
        asm volatile("s_waitcnt vmcnt(0)" ::: "memory");
        if (lane == 0) __hip_atomic_fetch_add((__attribute__((address_space(1))) unsigned*)(p.ws + WS_CNT + (t * 2 + sk) * 128), 1u, __ATOMIC_RELAXED, __HIP_MEMORY_SCOPE_AGENT);
      } else {
        const __attribute__((address_space(1))) unsigned* fl = (const __attribute__((address_space(1))) unsigned*)(p.ws + WS_CNT + (t * 2 + (sk ^ 1)) * 128);
        for (unsigned spin = 0; spin < (1u << 20); ++spin) {
          if (__hip_atomic_load(fl, __ATOMIC_RELAXED, __HIP_MEMORY_SCOPE_AGENT) >= 4u) break;
          __builtin_amdgcn_s_sleep(1);
        }
        __builtin_amdgcn_fence(__ATOMIC_ACQUIRE, "wavefront");
        const __amdgpu_buffer_rsrc_t sP = mksrd(slab + (sk ^ 1) * 12288 + kq * 3072, 3072);
#pragma unroll
        for (int i = 0; i < 3; ++i) {
          const u32x4 y = __builtin_bit_cast(u32x4, __builtin_amdgcn_raw_buffer_load_b128(sP, lane * 16 + i * 1024, 0, 16));
          q[2 * i][0] += __uint_as_float(y[0] << 16); q[2 * i][1] += __uint_as_float(y[0] & 0xFFFF0000u); q[2 * i][2] += __uint_as_float(y[1] << 16); q[2 * i][3] += __uint_as_float(y[1] & 0xFFFF0000u);
          q[2 * i + 1][0] += __uint_as_float(y[2] << 16); q[2 * i + 1][1] += __uint_as_float(y[2] & 0xFFFF0000u); q[2 * i + 1][2] += __uint_as_float(y[3] << 16); q[2 * i + 1][3] += __uint_as_float(y[3] & 0xFFFF0000u);
        }
        store_out(1152);
      }
    }
  }
}

__global__ __launch_bounds__(512, 2) void kb_main(K1P p) {
  extern __shared__ __attribute__((aligned(16))) char lds[];
  const int tid = threadIdx.x, lane = tid & 63, l32 = lane & 31, hi = lane >> 5;
  const int b = blockIdx.x, xcd = b & 7, ii = b >> 3, head = xcd * 4 + (ii & 3), j = ii >> 2;
  const int w = rfl(tid >> 6);
  const unsigned lds0 = (unsigned)(size_t)(lds_ptr)lds, voff = lane * 16;
  const int grp = w >> 2, wq = w & 3, pp = j;
  char* flagw = p.ws + WS_FLAG + (head * 8 + j) * 128;
  AttB a;
  if (grp == 0) {
    AttA A, B;
    float km = *(const float*)(p.ws + WS_KMAX + lane * 4);
#pragma unroll
    for (int o = 32; o >= 1; o >>= 1) km = fmaxf(km, __shfl_xor(km, o));
    const int aqrow = j * 256 + wq * 64 + l32;
    a_initq(A, p.ws, aqrow, head, lane, km); a_initq(B, p.ws, aqrow + 32, head, lane, km);
    PIN(A.qf0); PIN(A.qf1); PIN(B.qf0); PIN(B.qf1);
    unsigned tw = 0;
    if (TLBW && w == 3) {
      const unsigned pg = (unsigned)(ii * 64 + lane);
      if ((size_t)pg * 65536 < WS_END) tw = *(const unsigned*)(p.ws + (size_t)pg * 65536);
      else if ((size_t)(pg - 1536) * 65536 < (size_t)2048 * 2304 && pg >= 1536) tw = *(const unsigned*)((const char*)p.out + (size_t)(pg - 1536) * 65536);
    }
    unsigned wv[7] = {0, 0, 0, 0, 0, 0, 0};
    if (WARM) {
      const char* src[7] = {(const char*)p.cx, (const char*)p.wuk, (const char*)p.wuv, (const char*)p.bx, (const char*)p.wq, (const char*)p.wo, (const char*)p.awo};
      const unsigned nline[7] = {2048u * 1024 / 128, 512u * 8192 / 128, 512u * 8192 / 128, 2048u * 1152 / 128, 576u * 8192 / 128, 4096u * 1152 / 128, 576u * 1152 / 128};
#pragma unroll
      for (int a7 = 0; a7 < 7; ++a7) {
        const unsigned i = (unsigned)(b * 192 + tid);
        if (tid < 192 && i < nline[a7]) wv[a7] = *(const unsigned*)(src[a7] + (size_t)i * 128);
      }
    }
    a_loop(A, B, lds, lane, p.ws + WS_FLAG + head * 8 * 128, w == 0);
    if ((wv[0] ^ wv[1] ^ wv[2] ^ wv[3] ^ wv[4] ^ wv[5] ^ wv[6] ^ tw) == 0x9E3779B9u) *(unsigned*)(p.ws + WS_FLAG + 256 * 128 + 64) = 1u;

    a_finish(A, p.ws, lane, aqrow, head); a_finish(B, p.ws, lane, aqrow + 32, head);
    asm volatile("s_waitcnt vmcnt(0) lgkmcnt(0)\n\ts_barrier" ::: "memory");
  } else {
    __builtin_amdgcn_s_setprio(PPRIO);
    PG c;
    const int gr = wq >> 1, gc = wq & 1;
    c.lds0 = lds0; c.aw = wq; c.voff = voff;
    { const int r = lane >> 3, chp = lane & 7;
#pragma unroll
      for (int par = 0; par < 2; ++par) { const int ch = chp ^ ((4 * par + (r >> 1)) & 7); c.xoC[par] = (unsigned)(r * 1024 + ch * 16); c.xoX[par] = (unsigned)(r * 1152 + ch * 16); }
      c.woff = (unsigned)(((lane & 31) >> 2) * 8192 + (32 * (lane >> 5) + 8 * (lane & 3)) * 2); }
    c.sC = mksrd((const char*)p.cx + (size_t)j * 256 * 1024, 256 * 1024);
    c.sX = mksrd((const char*)p.bx, 2048 * 1152);
    c.rowL = (unsigned)((15 - j) * 128 * 1152); c.rowS = (unsigned)(j * 128 * 1152);
    c.sUK = mksrd((const char*)p.wuk + head * 256, 512 * 8192 - head * 256);
    c.sUV = mksrd((const char*)p.wuv + head * 256, 512 * 8192 - head * 256);
    c.sWQ = mksrd((const char*)p.wq + head * 256, 576 * 8192 - head * 256);
    c.sA = mksrd(p.ws + WS_KA, 64 * ATB);
    {
      auto da = [&](int q) { if (q > 14) q = 14; dma16(rfl(lds0 + ARING + (q / 3) * ATB + (q % 3) * 1024), voff, c.sA, rfl((unsigned)((q / 3) * ATB + (q % 3) * 1024))); };
      auto dw = [&](int st) { const unsigned m0 = rfl(lds0 + WRING + st * WSLOT + wq * 2048), so = rfl((unsigned)((st * 32 + wq * 8) * 8192)); dma16(m0, c.woff, c.sUK, so); dma16(m0 + 1024, c.woff + 128, c.sUK, so); };
      auto dx = [&](int T) {
#pragma unroll
        for (int half = 0; half < 2; ++half)
#pragma unroll
          for (int i = 0; i < 4; ++i)
            dma16(rfl(lds0 + T * XSLOT + (16 * half + 4 * wq + i) * 1024), c.xoC[i & 1], c.sC, rfl((unsigned)((128 * half + 32 * wq + 8 * i) * 1024 + T * 128))); };
      { const int q0 = 3 * wq; da(q0 < 9 ? q0 : 0); da(q0 + 1 < 9 ? q0 + 1 : 1); da(q0 + 2 < 9 ? q0 + 2 : 2); }
      dw(0); dx(0); dw(1); dw(2); dx(1);
      da(9 + 2 * wq); da(10 + 2 * wq);
    }
    const int blk = (lane >> 4) & 1, q4 = (lane & 15) >> 2, p4 = lane & 3;
#pragma unroll
    for (int s = 0; s < 4; ++s) c.xb[s] = (lds_cptr)lds + (gr * 4) * 4096 + 128 * l32 + 16 * ((2 * s + hi) ^ ((l32 >> 1) & 7));
    c.wb = (lds_cptr)lds + WRING + (2 * gc) * 512 + 2048 * hi + 64 * q4 + 32 * blk + 8 * p4;
    f32x16 acc[4][2]; FragKV fa, fb; bf16x8 kvp[16]; KVSt ks; ks.voff = voff;
#define ZACC() do { _Pragma("unroll") for (int r = 0; r < 4; ++r) { acc[r][0] = f32x16{}; acc[r][1] = f32x16{}; } } while (0)
#define PACKKV() do { _Pragma("unroll") for (int r = 0; r < 4; ++r) _Pragma("unroll") for (int ct = 0; ct < 2; ++ct) _Pragma("unroll") for (int g8 = 0; g8 < 2; ++g8) kvp[r * 4 + ct * 2 + g8] = pack8n(acc[r][ct], g8); SBAR(); } while (0)
#pragma unroll
    for (int i = 0; i < 16; ++i) kvp[i] = bf16x8{};
    ks.srd = mksrd(p.ws + WS_KF + ((size_t)(head * 64 + 8 * j + 4 * gr) * 8 + 4 * gc) * 1024, 65536);
    ZACC();
    GLoop<0, 0, 2>::run(c, acc, fa, fb, kvp, ks, flagw);
    GLoop<0, 2, 16>::run(c, acc, fa, fb, kvp, ks, flagw);
    PACKKV(); ZACC();
    GLoop<1, 16, 32>::run(c, acc, fa, fb, kvp, ks, flagw);
    PACKKV(); ZACC();
    ks.srd = mksrd(p.ws + WS_VF + ((size_t)(head * 32 + 4 * j + 2 * gr) * 16 + 2 * gc) * 1024, 65536);
    GLoop<2, 32, 50>::run(c, acc, fa, fb, kvp, ks, flagw);
    __builtin_amdgcn_s_setprio(0);
    asm volatile("s_waitcnt vmcnt(0) lgkmcnt(0)\n\ts_barrier" ::: "memory");
#pragma unroll
    for (int r = 0; r < 4; ++r)
#pragma unroll
      for (int ct = 0; ct < 2; ++ct)
#pragma unroll
        for (int g8 = 0; g8 < 2; ++g8) {
          const int fi = (4 * gr + r) * 8 + 2 * (2 * gc + ct) + g8;
          *(bf16x8*)(lds + fi * 1024 + lane * 16) = pack8(acc[r][ct], g8, CB);
        }
  }
  {
    const __amdgpu_buffer_rsrc_t sQLw = mksrd(p.ws + WS_QF + (size_t)b * 65536, 65536);
    asm volatile("s_waitcnt lgkmcnt(0)\n\ts_barrier" ::: "memory");
#pragma unroll
    for (int ds = 0; ds < 8; ++ds) a.qf[ds] = lds_rd16((lds_cptr)lds + ((grp * 4 + wq) * 8 + ds) * 1024 + lane * 16);
    if (w == 0) asm volatile("s_waitcnt vmcnt(0)" ::: "memory");
    asm volatile("s_waitcnt lgkmcnt(0)\n\ts_barrier" ::: "memory");
#pragma unroll
    for (int ds = 0; ds < 8; ++ds) bst16(a.qf[ds], sQLw, voff, (unsigned)(((grp * 4 + wq) * 8 + ds) * 1024));
  }
  const int L = 15 - pp, n1 = 2 * pp + 2;
  {
  AttEnv e; e.grp = grp; e.wq = wq; e.lane = lane; e.hi = hi; e.l32 = l32; e.n1 = n1; e.L = L; e.head = head; e.lds = lds; e.lds0 = lds0; e.ws = p.ws;
  e.sK = mksrd(p.ws + WS_KF + (size_t)head * S * 256, S * 256);
  e.sV = mksrd(p.ws + WS_VF + (size_t)head * S * 256, S * 256);
  e.sQ = mksrd(p.ws + WS_QF + (size_t)b * 65536, 65536);
  e.sC = mksrd(p.ws + WS_CF + (size_t)head * 8192, 64 * 256 * 1024 - head * 8192);
  int qb = grp == 0 ? L : pp;
  {
    const unsigned gb = grp * 81920;
#pragma unroll
    for (int t = 0; t < 2; ++t)
#pragma unroll
      for (int i = 0; i < 4; ++i) dma16s(rfl(lds0 + gb + t * 16384 + wq * 4096 + i * 1024), voff, e.sK, rfl((unsigned)(t * 16384 + wq * 4096 + i * 1024)));
  }
  if (grp == BPRIO_G) __builtin_amdgcn_s_setprio(BPRIO);
  {
    const int send0 = grp == 0 ? 17 : n1;
    const int rmin0 = qb * 128 + wq * 32;
    int nt0 = ((rmin0 + 31) >> 6) + 1; if (nt0 > send0) nt0 = send0;
    int nt1 = 0;
    if (grp == 1) { const int rminL = L * 128 + wq * 32; nt1 = ((rminL + 31) >> 6) - 17 + 1; if (nt1 > 17 - n1) nt1 = 17 - n1; if (nt1 < 0) nt1 = 0; }
    a.l = 0.f; a.ps = 0.f; a.m = -1e30f;
#pragma unroll
    for (int db = 0; db < 4; ++db) a.o[db] = f32x16{};
    int s = 0; bool carry = false; int qprev = qb;
    const int nseg = grp == 0 ? 1 : 2;
#pragma unroll 1
    for (int seg = 0; seg < nseg; ++seg) {
      const int nt = seg == 0 ? nt0 : nt1;
      const int send = seg == 0 ? send0 : 17;
      const bool shortblk = grp == 1 && seg == 0;
      if (nt > 0) {
        const int slast = s + nt;
        if (carry) att_step<M_SWITCH>(a, e, s, qprev, qb); else att_step<M_FIRST>(a, e, s, qb, qb, shortblk && nt == 1);
        for (++s; s < slast; ++s) att_step<M_STEADY>(a, e, s, qb, qb, shortblk && s == slast - 1);
        carry = shortblk && s == send && nt1 > 0;
        if (!carry) {
          att_step<M_DRAIN>(a, e, s, qb, qb); ++s;
          if (shortblk) {
            store_block(a, e, qb);
#pragma unroll
            for (int db = 0; db < 4; ++db) a.o[db] = f32x16{};
            a.l = 0.f; a.ps = 0.f; a.m = -1e30f;
          }
        }
      }
      for (; s < send; ++s) { StepCtx sc; step_top(e, s, sc); dma_all(e, sc); }
      qprev = qb; qb = L;
    }
    for (; s < 18; ++s) { StepCtx sc; step_top(e, s, sc); dma_all(e, sc); }
  }
  asm volatile("s_waitcnt vmcnt(0) lgkmcnt(0)\n\ts_barrier" ::: "memory");
  {
    const float lt = hsum(a.l);
    char* xb = lds + w * 8192;
    float* st = (float*)(lds + 65536) + w * 128;
#pragma unroll
    for (int i = 0; i < 2; ++i)
#pragma unroll
      for (int q = 0; q < 4; ++q) {
        const f32x16& o = grp == 0 ? a.o[2 + i] : a.o[i];
        *(f32x4*)(xb + (i * 4 + q) * 1024 + lane * 16) = f32x4{o[4 * q], o[4 * q + 1], o[4 * q + 2], o[4 * q + 3]};
      }
    st[lane] = a.m; st[64 + lane] = lt;
    __syncthreads();
    const int ow = w ^ 4;
    const char* yb = lds + ow * 8192; const float* so = (const float*)(lds + 65536) + ow * 128;
    const float m2 = so[lane], l2 = so[64 + lane];
    const float mn = fmaxf(a.m, m2), f1 = EXP2(a.m - mn), f2 = EXP2(m2 - mn);
    const float inv = 1.f / (lt * f1 + l2 * f2);
    const float g1 = f1 * inv, g2 = f2 * inv;
#pragma unroll
    for (int i = 0; i < 2; ++i) {
      f32x16 o = grp == 0 ? a.o[i] : a.o[2 + i];
#pragma unroll
      for (int q = 0; q < 4; ++q) { const f32x4 y = *(const f32x4*)(yb + (i * 4 + q) * 1024 + lane * 16);
#pragma unroll
        for (int e = 0; e < 4; ++e) o[4 * q + e] = o[4 * q + e] * g1 + y[e] * g2; }
      const int db = (grp == 0 ? 0 : 2) + i;
#pragma unroll
      for (int g = 0; g < 2; ++g)
        bst16s(pack8n(o, g), e.sC, voff, rfl((unsigned)(((L * 4 + wq) * 256 + db * 2 + g) * 1024)));
    }
  }
  }
  __builtin_amdgcn_s_setprio(0);
  asm volatile("s_waitcnt vmcnt(0) lgkmcnt(0)\n\ts_barrier" ::: "memory");
  if (tid == 0) __hip_atomic_store((__attribute__((address_space(1))) unsigned*)(p.ws + WS_DONE + b * 128), 1u, __ATOMIC_RELAXED, __HIP_MEMORY_SCOPE_AGENT);
  k3_phase(p, lds);
}

extern "C" void kernel_launch(void* const* d_in, const int* in_sizes, int n_in, void* d_out, int out_size, void* d_ws, size_t ws_size, hipStream_t stream) {
  (void)hipFuncSetAttribute((const void*)k0_aproj, hipFuncAttributeMaxDynamicSharedMemorySize, 147456);
  (void)hipFuncSetAttribute((const void*)kb_main, hipFuncAttributeMaxDynamicSharedMemorySize, 163840);
  K1P p1; p1.cx = d_in[6]; p1.bx = d_in[5]; p1.wuk = d_in[8]; p1.wuv = d_in[9]; p1.wq = d_in[7];
  p1.ax = d_in[0]; p1.awq = d_in[1]; p1.awk = d_in[2]; p1.awv = d_in[3]; p1.wo = d_in[10]; p1.awo = d_in[4]; p1.out = d_out; p1.ws = (char*)d_ws;
  k0_aproj<<<256, 512, 147456, stream>>>(p1);
  kb_main<<<256, 512, 163840, stream>>>(p1);
}
```

```cpp
#include <hip/hip_runtime.h>
#include <hip/hip_bf16.h>
#include <cstdio>
#include <cstdint>
#include <utility>

#define PUBMODE 0
#define KV_ACQ 0
#define BPRIO 1
#define BPRIO_G 1
#define PPRIO 2
#define WARM 1
#define ST_AUX 0
#define DI __device__ __forceinline__
typedef short bf16x8 __attribute__((ext_vector_type(8)));
typedef short s16x4 __attribute__((ext_vector_type(4)));
typedef float f32x16 __attribute__((ext_vector_type(16)));
typedef float f32x4 __attribute__((ext_vector_type(4)));
typedef float f32x2 __attribute__((ext_vector_type(2)));
typedef __bf16 bf16x2_t __attribute__((ext_vector_type(2)));
typedef unsigned u32x4 __attribute__((ext_vector_type(4)));
typedef unsigned u32x2 __attribute__((ext_vector_type(2)));
typedef __attribute__((address_space(3))) const char* lds_cptr;
typedef __attribute__((address_space(3))) char* lds_ptr;
#define SBAR() __builtin_amdgcn_sched_barrier(0)
#define PIN(x) asm volatile("" : "+v"(x))
#define MFMA(a, b, c) __builtin_amdgcn_mfma_f32_32x32x16_bf16(a, b, c, 0, 0, 0)
#define SRD_W3 0x00020000

DI unsigned rfl(unsigned x) { return __builtin_amdgcn_readfirstlane(x); }
DI unsigned cvtpk(float lo, float hi) { f32x2 v = {lo, hi}; bf16x2_t b = __builtin_convertvector(v, bf16x2_t); return __builtin_bit_cast(unsigned, b); }
DI void dma16(unsigned m0, unsigned voff, __amdgpu_buffer_rsrc_t srd, unsigned soff) {
  asm volatile("s_mov_b32 m0, %0\n\ts_nop 0\n\tbuffer_load_dwordx4 %1, %2, %3 offen lds" :: "s"(m0), "v"(voff), "s"(srd), "s"(soff) : "m0", "memory");
}
template <int OFF> DI void dma16o(unsigned m0, unsigned voff, __amdgpu_buffer_rsrc_t srd, unsigned soff) {
  asm volatile("s_mov_b32 m0, %0\n\ts_nop 0\n\tbuffer_load_dwordx4 %1, %2, %3 offen offset:%4 lds" :: "s"(m0), "v"(voff), "s"(srd), "s"(soff), "i"(OFF) : "m0", "memory");
}
DI void dma16s(unsigned m0, unsigned voff, __amdgpu_buffer_rsrc_t srd, unsigned soff) {
  asm volatile("s_mov_b32 m0, %0\n\ts_nop 0\n\tbuffer_load_dwordx4 %1, %2, %3 offen sc1 lds" :: "s"(m0), "v"(voff), "s"(srd), "s"(soff) : "m0", "memory");
}
template <int OFF> DI void dma16os(unsigned m0, unsigned voff, __amdgpu_buffer_rsrc_t srd, unsigned soff) {
  asm volatile("s_mov_b32 m0, %0\n\ts_nop 0\n\tbuffer_load_dwordx4 %1, %2, %3 offen offset:%4 sc1 lds" :: "s"(m0), "v"(voff), "s"(srd), "s"(soff), "i"(OFF) : "m0", "memory");
}
DI bf16x8 bld16s(__amdgpu_buffer_rsrc_t srd, unsigned voff, unsigned soff) { return __builtin_bit_cast(bf16x8, __builtin_amdgcn_raw_buffer_load_b128(srd, voff, soff, 16)); }
DI void dma4(unsigned m0, unsigned voff, __amdgpu_buffer_rsrc_t srd, unsigned soff) {
  asm volatile("s_mov_b32 m0, %0\n\ts_nop 0\n\tbuffer_load_dword %1, %2, %3 offen lds" :: "s"(m0), "v"(voff), "s"(srd), "s"(soff) : "m0", "memory");
}
DI __amdgpu_buffer_rsrc_t mksrd(const void* p, unsigned bytes) { return __builtin_amdgcn_make_buffer_rsrc((void*)p, (short)0, (int)bytes, SRD_W3); }
DI void bst16s(bf16x8 v, __amdgpu_buffer_rsrc_t srd, unsigned voff, unsigned soff) { __builtin_amdgcn_raw_buffer_store_b128(__builtin_bit_cast(u32x4, v), srd, voff + soff, 0, 16); }
DI bf16x8 bld16(__amdgpu_buffer_rsrc_t srd, unsigned voff, unsigned soff) { return __builtin_bit_cast(bf16x8, __builtin_amdgcn_raw_buffer_load_b128(srd, voff, soff, 0)); }
DI void bst16(bf16x8 v, __amdgpu_buffer_rsrc_t srd, unsigned voff, unsigned soff) { __builtin_amdgcn_raw_buffer_store_b128(__builtin_bit_cast(u32x4, v), srd, voff + soff, 0, ST_AUX); }
template <int N> DI void wait_bar() { asm volatile("s_waitcnt vmcnt(%0)\n\ts_barrier" :: "i"(N) : "memory"); }
DI bf16x8 lds_rd16(lds_cptr p) { return *(const __attribute__((address_space(3))) bf16x8*)p; }
DI s16x4 lds_tr(lds_cptr p) { return __builtin_bit_cast(s16x4, __builtin_amdgcn_ds_read_tr16_b64_v4i16((__attribute__((address_space(3))) s16x4*)p)); }
DI bf16x8 cat(s16x4 a, s16x4 b) { return __builtin_shufflevector(a, b, 0, 1, 2, 3, 4, 5, 6, 7); }
DI bf16x8 pack8(const f32x16& x, int g, float sc) {
  u32x4 w;
#pragma unroll
  for (int e = 0; e < 4; ++e) w[e] = cvtpk(x[8 * g + 2 * e] * sc, x[8 * g + 2 * e + 1] * sc);
  return __builtin_bit_cast(bf16x8, w);
}
DI bf16x8 pack8n(const f32x16& x, int g) {
  u32x4 w;
#pragma unroll
  for (int e = 0; e < 4; ++e) w[e] = cvtpk(x[8 * g + 2 * e], x[8 * g + 2 * e + 1]);
  return __builtin_bit_cast(bf16x8, w);
}

DI float hmax(float m) { auto rr = __builtin_amdgcn_permlane32_swap(__float_as_uint(m), __float_as_uint(m), false, false); return fmaxf(__uint_as_float(rr[0]), __uint_as_float(rr[1])); }
DI float hsum(float m) { auto rr = __builtin_amdgcn_permlane32_swap(__float_as_uint(m), __float_as_uint(m), false, false); return __uint_as_float(rr[0]) + __uint_as_float(rr[1]); }
constexpr int S = 2048, H = 32, DB = 128, MB = 576, DC = 512, DA = 576, DHA = 18;
constexpr float CB = 0.08838834764831845f * 1.4426950408889634f;
constexpr float CA = 0.23570226039551584f * 1.4426950408889634f;
constexpr size_t WS_KF = 0;
constexpr size_t WS_VF = WS_KF + (size_t)H * S * DB * 2;
constexpr size_t WS_QF = WS_VF + (size_t)H * S * DB * 2;
constexpr size_t WS_CF = WS_QF + (size_t)H * S * DB * 2;
constexpr size_t WS_QA = WS_CF + (size_t)H * S * DB * 2;
constexpr int ATB = 3072;
constexpr size_t WS_KA = WS_QA + (size_t)S * DA * 2 + 256;
constexpr size_t WS_KMAX = WS_KA + 64 * ATB;
constexpr size_t WS_OA = WS_KMAX + 1024;
constexpr size_t WS_PART = WS_OA + (size_t)S * DA * 2;
constexpr size_t WS_FLAG = WS_PART + (size_t)4 * S * 576 * 4;
constexpr size_t WS_CNT = WS_FLAG + 256 * 128 + 1024;
constexpr size_t WS_DONE = WS_CNT + 192 * 128 + 1024;
constexpr size_t WS_END = WS_DONE + 256 * 128;

struct K1P {
  const void *cx, *bx, *wuk, *wuv, *wq;
  const void *ax, *awq, *awk, *awv;
  const void *wo, *awo;
  void* out;
  char* ws;
};
constexpr int XSLOT = 32768, WRING = 98304, WSLOT = 8192, ARING = 131072, NPSTEP = 50;
struct PG {
  lds_cptr xb[4];
  lds_cptr wb;
  unsigned lds0, aw;
  unsigned xoC[2], xoX[2], woff, voff;
  __amdgpu_buffer_rsrc_t sC, sX, sUK, sUV, sWQ, sA;
  unsigned rowL, rowS;
};
constexpr int TAc(int g) { return (g * 64) / NPSTEP; }
template <int G, int I> DI void g_piece(const PG& c) {
  if constexpr (I < 2) {
    constexpr int gw = G + 3, pass = gw < 16 ? 0 : (gw < 32 ? 1 : 2), lg = gw - (pass == 0 ? 0 : (pass == 1 ? 16 : 32));
    const unsigned m0 = c.lds0 + WRING + (gw & 3) * WSLOT + c.aw * 2048 + I * 1024, so = (unsigned)(lg * 32 * 8192) + c.aw * 65536;
    dma16(m0, c.woff + I * 128, pass == 0 ? c.sUK : (pass == 1 ? c.sUV : c.sWQ), so);
  } else if constexpr (I < 6) {
    constexpr int i = I - 2, T = (G >> 1) + 2, half = G & 1;
    const unsigned m0 = c.lds0 + (T % 3) * XSLOT + (16 * half + i) * 1024 + c.aw * 4096;
    if constexpr (T < 16) dma16(m0, c.xoC[i & 1], c.sC, (unsigned)((128 * half + 8 * i) * 1024 + (T & 7) * 128) + c.aw * 32768);
    else dma16(m0, c.xoX[i & 1], c.sX, (half ? c.rowS : c.rowL) + (unsigned)(8 * i * 1152 + (T - 16) * 128) + (T > 24 ? 0x40000000u : 0u) + c.aw * (32 * 1152));
  } else {
    constexpr int f0 = TAc(G + 4), nf = (TAc(G + 5) - f0) * 3;
    int q = 2 * c.aw + (I - 6); if (q >= nf) q = q - nf < nf ? q - nf : 0;
    const int kt = f0 + q / 3, part = q % 3;
    dma16(rfl(c.lds0 + ARING + (kt % 10) * ATB + part * 1024), c.voff, c.sA, rfl((unsigned)(kt * ATB + part * 1024)));
  }
}
DI void st16_sc1(bf16x8 v, unsigned voff, __amdgpu_buffer_rsrc_t srd, unsigned soff) {
  asm volatile("buffer_store_dwordx4 %0, %1, %2, %3 offen sc1\n\ts_nop 1" :: "v"(v), "v"(voff), "s"(srd), "s"(soff) : "memory");
}
struct FragKV { bf16x8 x[4]; s16x4 wl[2], wh[2]; };
template <int G, int SS, int I> DI void kv_ld(const PG& c, FragKV& f) {
  constexpr int T = G >> 1;
  lds_cptr xp = c.xb[2 * (G & 1) + SS] + (T % 3) * XSLOT;
  lds_cptr wp = c.wb + (G & 3) * WSLOT + SS * 4096;
  if constexpr (I == 0) f.x[0] = lds_rd16(xp);
  else if constexpr (I == 1) f.wl[0] = lds_tr(wp);
  else if constexpr (I == 2) f.wh[0] = lds_tr(wp + 256);
  else if constexpr (I == 3) f.wl[1] = lds_tr(wp + 512);
  else if constexpr (I == 4) f.wh[1] = lds_tr(wp + 768);
  else f.x[I - 4] = lds_rd16(xp + (I - 4) * 4096);
}
template <bool ISV, int M> DI void kv_mfma(f32x16 (&acc)[4][2], const FragKV& f) {
  constexpr int r = M >> 1, ct = M & 1;
  const bf16x8 wf = cat(f.wl[ct], f.wh[ct]);
  acc[r][ct] = ISV ? MFMA(f.x[r], wf, acc[r][ct]) : MFMA(wf, f.x[r], acc[r][ct]);
}
struct KVSt { __amdgpu_buffer_rsrc_t srd; unsigned voff; };
constexpr int relK(int i) { return ((i >> 2) * 8 + ((i >> 1) & 1) * 2 + (i & 1)) * 1024; }
constexpr int relV(int i) { return (((i >> 2) >> 1) * 16 + (2 * ((i >> 2) & 1) + (i & 1)) * 4 + ((i >> 1) & 1)) * 1024; }
template <bool ISV, int GN, int SN, bool LOAD, int STI, bool PV, int DG, int DB, int M> DI void kv_gap(const PG& c, f32x16 (&acc)[4][2], const FragKV& cur, FragKV& nxt, const bf16x8 (&kvp)[16], const KVSt& ks) {
  kv_mfma<ISV, M>(acc, cur);
  if constexpr (LOAD) kv_ld<GN, SN, M>(c, nxt);
  if constexpr ((M & 1) && DB + (M >> 1) < 8) g_piece<DG, DB + (M >> 1)>(c);
  if constexpr (STI >= 0 && M == 6) st16_sc1(kvp[STI], ks.voff, ks.srd, (unsigned)(PV ? relV(STI) : relK(STI)));
  SBAR();
}
template <bool ISV, int GN, int SN, bool LOAD, int STI, bool PV, int DG, int DB> DI void kv_phase(const PG& c, f32x16 (&acc)[4][2], const FragKV& cur, FragKV& nxt, const bf16x8 (&kvp)[16], const KVSt& ks) {
  kv_gap<ISV, GN, SN, LOAD, STI, PV, DG, DB, 0>(c, acc, cur, nxt, kvp, ks); kv_gap<ISV, GN, SN, LOAD, STI, PV, DG, DB, 1>(c, acc, cur, nxt, kvp, ks);
  kv_gap<ISV, GN, SN, LOAD, STI, PV, DG, DB, 2>(c, acc, cur, nxt, kvp, ks); kv_gap<ISV, GN, SN, LOAD, STI, PV, DG, DB, 3>(c, acc, cur, nxt, kvp, ks);
  kv_gap<ISV, GN, SN, LOAD, STI, PV, DG, DB, 4>(c, acc, cur, nxt, kvp, ks); kv_gap<ISV, GN, SN, LOAD, STI, PV, DG, DB, 5>(c, acc, cur, nxt, kvp, ks);
  kv_gap<ISV, GN, SN, LOAD, STI, PV, DG, DB, 6>(c, acc, cur, nxt, kvp, ks); kv_gap<ISV, GN, SN, LOAD, STI, PV, DG, DB, 7>(c, acc, cur, nxt, kvp, ks);
}
constexpr int nSt(int g) { return (g >= 16 && g < 32) ? 1 : ((g >= 32 && g < 40) ? 2 : 0); }
constexpr int FLAGG = 43;
template <int PASS, int G> DI void g_step(const PG& c, f32x16 (&acc)[4][2], FragKV& fa, FragKV& fb, const bf16x8 (&kvp)[16], const KVSt& ks, char* flag) {
  constexpr int G0 = PASS == 0 ? 0 : (PASS == 1 ? 16 : 32), LG = G - G0;
  if constexpr (G == 0) wait_bar<12>(); else if constexpr (G == 1) wait_bar<8>(); else wait_bar<nSt(G - 2) + 8 + nSt(G - 1)>();
  SBAR();
  if constexpr (G == FLAGG) { if (threadIdx.x == 256) __hip_atomic_store((__attribute__((address_space(1))) unsigned*)flag, 1u, __ATOMIC_RELAXED, __HIP_MEMORY_SCOPE_AGENT); }
  if constexpr (G == 0) {
    kv_ld<0, 0, 0>(c, fa); kv_ld<0, 0, 1>(c, fa); kv_ld<0, 0, 2>(c, fa); kv_ld<0, 0, 3>(c, fa);
    kv_ld<0, 0, 4>(c, fa); kv_ld<0, 0, 5>(c, fa); kv_ld<0, 0, 6>(c, fa); kv_ld<0, 0, 7>(c, fa); SBAR();
  }
  constexpr int STA = PASS == 1 ? LG : ((PASS == 2 && LG < 8) ? 2 * LG : -1);
  constexpr int STB = (PASS == 2 && LG < 8) ? 2 * LG + 1 : -1;
  kv_phase<PASS == 1, G, 1, true, -1, PASS == 2, G, 0>(c, acc, fa, fb, kvp, ks);
  kv_phase<PASS == 1, G + 1, 0, (G + 1 < NPSTEP), -1, PASS == 2, G, 4>(c, acc, fb, fa, kvp, ks);
  if constexpr (STA >= 0) st16_sc1(kvp[STA], ks.voff, ks.srd, (unsigned)(PASS == 2 ? relV(STA) : relK(STA)));
  if constexpr (STB >= 0) st16_sc1(kvp[STB], ks.voff, ks.srd, (unsigned)relV(STB));
  SBAR();
}
template <int PASS, int G, int GEND> struct GLoop { static DI void run(const PG& c, f32x16 (&acc)[4][2], FragKV& fa, FragKV& fb, const bf16x8 (&kvp)[16], const KVSt& ks, char* flag) {
  g_step<PASS, G>(c, acc, fa, fb, kvp, ks, flag); if constexpr (G + 1 < GEND) GLoop<PASS, G + 1, GEND>::run(c, acc, fa, fb, kvp, ks, flag); } };

__global__ __launch_bounds__(512, 2) void k0_aproj(K1P p) {
  extern __shared__ __attribute__((aligned(16))) char lds[];
  const int tid = threadIdx.x, lane = tid & 63, l32 = lane & 31, hi = lane >> 5;
  const int b = blockIdx.x, xcd = b & 7, ii = b >> 3;
  struct { unsigned lds0; } c; c.lds0 = (unsigned)(size_t)(lds_ptr)lds;
  const int w = rfl(tid >> 6);
  const int blk = (lane >> 4) & 1, q4 = (lane & 15) >> 2, p4 = lane & 3;
  if (tid == 0) { *(unsigned*)(p.ws + WS_FLAG + b * 128) = 0u; if (b < 192) *(unsigned*)(p.ws + WS_CNT + b * 128) = 0u; *(unsigned*)(p.ws + WS_DONE + b * 128) = 0u; }
  if (ii >= 28) {
    const int t = (b & 7) + 8 * (ii - 28);
    unsigned v = 0;
    { const int i = t * 512 + tid; if (i < 2048 * 2) v ^= *(const unsigned*)((const char*)p.cx + (size_t)(i >> 1) * 1024 + (i & 1) * 128); }
    { const int i = t * 512 + tid; if (i < 96 * 64) v ^= *(const unsigned*)((const char*)p.wuk + (size_t)i * 128); }
    if (v == 0x9E3779B9u) *(unsigned*)(p.ws + WS_FLAG + 256 * 128 + 64) = v;
  }
  if (ii < 20) {
    const int rg = 4 * xcd + ii / 5, cg = ii % 5;
    const __amdgpu_buffer_rsrc_t sAX = mksrd((const char*)p.ax + (size_t)rg * 64 * 1152, 64 * 1152);
    const __amdgpu_buffer_rsrc_t sAQ = mksrd((const char*)p.awq + cg * 256, 576 * 1152 - cg * 256);
    constexpr int ASL = 73728, AWO = 24576;
    const unsigned xv = (unsigned)((lane >> 3) * 1152 + (((lane & 7) ^ ((4 * (w & 1) + (lane >> 4)) & 7)) * 16));
    const unsigned wv = (unsigned)(((lane & 31) >> 2) * 1152 + (32 * (lane >> 5) + 8 * (lane & 3)) * 2);
    auto issue = [&](int th) {
      const unsigned base = c.lds0 + (th & 1) * ASL;
#pragma unroll
      for (int kc = 0; kc < 3; ++kc) dma16(rfl(base + (kc * 8 + w) * 1024), xv, sAX, rfl((unsigned)(w * 8 * 1152 + (th * 3 + kc) * 128)));
#pragma unroll
      for (int i = 0; i < 6; ++i) {
        const int g = 3 * w + (i >> 1), pr = i & 1;
        dma16(rfl(base + AWO + (g * 2 + pr) * 1024), wv, sAQ, rfl((unsigned)((th * 192 + g * 8) * 1152 + pr * 128)));
      }
    };
    const int rt = w >> 2, ct = w & 3, tile = 4 * cg + ct;
    f32x16 acc = f32x16{};
    issue(0); issue(1);
    for (int th = 0; th < 3; ++th) {
      if (th < 2) asm volatile("s_waitcnt vmcnt(9) lgkmcnt(0)\n\ts_barrier" ::: "memory");
      else asm volatile("s_waitcnt vmcnt(0) lgkmcnt(0)\n\ts_barrier" ::: "memory");
      lds_cptr xp = (lds_cptr)lds + (th & 1) * ASL + rt * 4096 + 128 * l32;
      lds_cptr wp = (lds_cptr)lds + (th & 1) * ASL + AWO + ct * 512 + 2048 * hi + 64 * q4 + 32 * blk + 8 * p4;
      bf16x8 xf[4]; s16x4 wl[4], wh[4];
#define ALD(k) do { xf[(k) & 3] = lds_rd16(xp + ((k) >> 2) * 8192 + 16 * ((2 * ((k) & 3) + hi) ^ ((l32 >> 1) & 7))); wl[(k) & 3] = lds_tr(wp + (k) * 4096); wh[(k) & 3] = lds_tr(wp + (k) * 4096 + 256); } while (0)
      if (tile < 18) {
        ALD(0); ALD(1); ALD(2);
#pragma unroll
        for (int k = 0; k < 12; ++k) {
          if (k + 3 < 12) ALD(k + 3);
          acc = MFMA(cat(wl[k & 3], wh[k & 3]), xf[k & 3], acc);
          SBAR();
        }
      }
      if (th == 0) { asm volatile("s_waitcnt lgkmcnt(0)\n\ts_barrier" ::: "memory"); issue(2); }
    }
    const int row = (rg * 2 + rt) * 32 + l32;
    if (tile < 18) {
#pragma unroll
      for (int g = 0; g < 4; ++g) {
        u32x2 v = {cvtpk(acc[4 * g] * CA, acc[4 * g + 1] * CA), cvtpk(acc[4 * g + 2] * CA, acc[4 * g + 3] * CA)};
        *(u32x2*)(p.ws + WS_QA + (size_t)row * 1152 + (tile * 32 + 8 * g + 4 * hi) * 2) = v;
      }
    }
  } else if (ii < 28) {
    const int r32 = 8 * xcd + (ii - 20);
    const __amdgpu_buffer_rsrc_t sAX = mksrd((const char*)p.ax + (size_t)r32 * 32 * 1152, 32 * 1152);
    for (int pc = w; pc < 36; pc += 8) {
      const int kc = pc >> 2, rq = pc & 3, r = lane >> 3, row = 8 * rq + r, ch = (lane & 7) ^ ((row >> 1) & 7);
      dma16(rfl(c.lds0 + pc * 1024), (unsigned)(row * 1152 + ch * 16), sAX, rfl((unsigned)(kc * 128)));
    }
    {
      const unsigned* gk = (const unsigned*)p.awk; const unsigned* gv = (const unsigned*)p.awv;
      unsigned vk[11], vv[11];
#pragma unroll
      for (int it = 0; it < 11; ++it) { const int i = tid + it * 512; const int ic = i < 5184 ? i : 0; vk[it] = gk[ic]; vv[it] = gv[ic]; }
#pragma unroll
      for (int it = 0; it < 11; ++it) {
        const int i = tid + it * 512;
        if (i < 5184) { const int r = i / 9, d = i - r * 9; char* dst = lds + 36864 + (r >> 3) * 1024 + (r & 7) * 64 + d * 4; *(unsigned*)dst = vk[it]; *(unsigned*)(dst + 512) = vv[it]; }
      }
    }
    asm volatile("s_waitcnt vmcnt(0) lgkmcnt(0)\n\ts_barrier" ::: "memory");
    const int isv = w >> 2, kq = w & 3;
    f32x16 acc = f32x16{};
    lds_cptr xp = (lds_cptr)lds + 128 * l32;
    lds_cptr wp = (lds_cptr)lds + 36864 + isv * 512 + 1024 * hi + 64 * q4 + 32 * blk + 8 * p4;
#pragma unroll
    for (int k9 = 0; k9 < 9; ++k9) {
      const int k = kq * 9 + k9;
      const bf16x8 xf = lds_rd16(xp + (k >> 2) * 4096 + 16 * ((2 * (k & 3) + hi) ^ ((l32 >> 1) & 7)));
      const bf16x8 wf = cat(lds_tr(wp + k * 2048), lds_tr(wp + k * 2048 + 256));
      if (isv) acc = MFMA(xf, wf, acc); else acc = MFMA(wf, xf, acc);
    }
    asm volatile("s_waitcnt lgkmcnt(0)\n\ts_barrier" ::: "memory");
    float* red = (float*)lds + w * 1024;
#pragma unroll
    for (int q = 0; q < 4; ++q) *(f32x4*)(red + q * 256 + lane * 4) = f32x4{acc[4 * q], acc[4 * q + 1], acc[4 * q + 2], acc[4 * q + 3]};
    __syncthreads();
    if (kq == 0) {
#pragma unroll
      for (int o = 1; o < 4; ++o)
#pragma unroll
        for (int q = 0; q < 4; ++q) { const f32x4 y = *(const f32x4*)(red + o * 1024 + q * 256 + lane * 4); acc[4 * q] += y[0]; acc[4 * q + 1] += y[1]; acc[4 * q + 2] += y[2]; acc[4 * q + 3] += y[3]; }
      const int row = r32 * 32 + l32;
      char* hb = p.ws + WS_KA + (size_t)r32 * ATB;
      if (!isv) {
        char* k0 = hb;
        const unsigned w0 = cvtpk(acc[0], acc[1]), w1 = cvtpk(acc[2], acc[3]), w2 = cvtpk(acc[4], acc[5]), w3 = cvtpk(acc[6], acc[7]), w4 = cvtpk(acc[8], acc[9]);
        *(u32x2*)(k0 + (l32) * 16 + 8 * hi) = u32x2{w0, w1};
        *(u32x2*)(k0 + (l32 + 32) * 16 + 8 * hi) = u32x2{w2, w3};
        if (hi == 0) *(u32x4*)(hb + 1024 + l32 * 16) = u32x4{w4, 0x00003F80u, 0u, 0u};
        auto sq = [](unsigned x) { const float a = __uint_as_float(x << 16), bq = __uint_as_float(x & 0xFFFF0000u); return a * a + bq * bq; };
        float s2 = sq(w0) + sq(w1) + sq(w2) + sq(w3) + (hi == 0 ? sq(w4) : 0.f);
        s2 = hsum(s2);
#pragma unroll
        for (int o = 16; o >= 1; o >>= 1) s2 = fmaxf(s2, __shfl_xor(s2, o));
        if (lane == 0) *(float*)(p.ws + WS_KMAX + r32 * 4) = s2;
      } else {
#pragma unroll
        for (int g = 0; g < 2; ++g) {
          bf16x8 v = pack8n(acc, g);
          if (l32 == 18) v = bf16x8{0x3F80, 0x3F80, 0x3F80, 0x3F80, 0x3F80, 0x3F80, 0x3F80, 0x3F80};
          if (l32 < 19) *(bf16x8*)(hb + 1536 + ((g * 2 + hi) * 19 + l32) * 16) = v;
        }
      }
    }
  }

}

constexpr float LTHR = 7.9228162514264338e28f;
#define LTHR_V LTHR
struct AttB {
  f32x16 o[4]; bf16x8 qf[8]; f32x16 negm; float m, l;
  f32x16 s0, s1;
  unsigned pw[16];
  bf16x8 fr[4];
  float ps;
};
DI void cmask1(f32x16& s, int key0, int row) {
  const int d = row - key0;
#pragma unroll
  for (int r = 0; r < 16; ++r) { const int c = (r & 3) + 8 * (r >> 2); s[r] = (c > d) ? -INFINITY : s[r]; }
}
DI float max16(const f32x16& s) { float a = fmaxf(fmaxf(s[0], s[1]), s[2]);
#pragma unroll
  for (int r = 3; r < 15; r += 2) a = fmaxf(fmaxf(a, s[r]), s[r + 1]);
  return fmaxf(a, s[15]); }
DI f32x16 splat16(float x) { f32x16 v;
#pragma unroll
  for (int r = 0; r < 16; ++r) v[r] = x;
  return v; }
#define PKB(w) __builtin_bit_cast(bf16x8, w)
#define EXP2(x) __builtin_amdgcn_exp2f(x)
DI bf16x8 pfrag(const AttB& a, int ks) { u32x4 w = {a.pw[4 * ks], a.pw[4 * ks + 1], a.pw[4 * ks + 2], a.pw[4 * ks + 3]}; return __builtin_bit_cast(bf16x8, w); }

template <bool HASCUR> DI void rare_fix(AttB& a, __amdgpu_buffer_rsrc_t sK, __amdgpu_buffer_rsrc_t sQ, unsigned voff, unsigned kso, unsigned qso, int key0, int row, bool mask) {
  float tm = -INFINITY;
  f32x16 t;
#pragma unroll 1
  for (int kb = 0; kb < 2; ++kb) {
    t = f32x16{};
#pragma unroll 2
    for (int ds = 0; ds < 8; ++ds) t = MFMA(bld16s(sK, voff, kso + kb * 8192 + ds * 1024), bld16(sQ, voff, qso + ds * 1024), t);
    if (mask) cmask1(t, key0 + 32 * kb, row);
    tm = fmaxf(tm, max16(t));
  }
  tm = hmax(tm);
  const float mn = fmaxf(a.m, tm), dl = mn - a.m, f = EXP2(-dl);
#pragma unroll
  for (int db = 0; db < 4; ++db)
#pragma unroll
    for (int r = 0; r < 16; ++r) a.o[db][r] *= f;
  float ps = 0.f;
#pragma unroll 1
  for (int kb = 0; kb < 2; ++kb) {
    t = f32x16{};
#pragma unroll 2
    for (int ds = 0; ds < 8; ++ds) t = MFMA(bld16s(sK, voff, kso + kb * 8192 + ds * 1024), bld16(sQ, voff, qso + ds * 1024), t);
    if (mask) cmask1(t, key0 + 32 * kb, row);
#pragma unroll
    for (int r = 0; r < 16; ++r) { t[r] = EXP2(t[r] - mn); ps += t[r]; }
    if (kb == 0) {
#pragma unroll
      for (int k = 0; k < 8; ++k) a.pw[k] = cvtpk(t[2 * k], t[2 * k + 1]);
    } else {
#pragma unroll
      for (int k = 0; k < 8; ++k) a.pw[8 + k] = cvtpk(t[2 * k], t[2 * k + 1]);
    }
  }
  a.l *= f; a.ps = ps;
  if (HASCUR) {
#pragma unroll
    for (int r = 0; r < 16; ++r) a.s0[r] -= dl;
  }
  a.m = mn; a.negm = splat16(-mn); PIN(a.negm);
}

struct StepCtx {
  lds_cptr kp;
  lds_cptr kpn;
  lds_cptr vp;
  unsigned m0k, m0v, sok, sov; bool dk, dv;
  unsigned voff;
};
constexpr int M_STEADY = 0, M_FIRST = 1, M_SWITCH = 2, M_DRAIN = 3;
template <int I, int MODE> DI void gapA(AttB& a, const StepCtx& c, __amdgpu_buffer_rsrc_t sK, __amdgpu_buffer_rsrc_t sV) {
  constexpr bool PREV = MODE != M_FIRST, CUR = MODE != M_DRAIN, NEW = MODE == M_FIRST || MODE == M_SWITCH;
  constexpr int ds = I & 7;
  if constexpr (CUR) {
    if constexpr (I < 8) { if constexpr (ds == 0) a.s0 = NEW ? MFMA(a.fr[I & 3], a.qf[0], f32x16{}) : MFMA(a.fr[I & 3], a.qf[0], a.negm); else a.s0 = MFMA(a.fr[I & 3], a.qf[ds], a.s0); }
    else { if constexpr (ds == 0) a.s1 = NEW ? MFMA(a.fr[I & 3], a.qf[0], f32x16{}) : MFMA(a.fr[I & 3], a.qf[0], a.negm); else a.s1 = MFMA(a.fr[I & 3], a.qf[ds], a.s1); }
  }
  if constexpr (I + 3 < 16) { if constexpr (CUR) a.fr[(I + 3) & 3] = lds_rd16(c.kp + (I + 3) * 1024); }
  else if constexpr (PREV) a.fr[(I + 3) & 3] = lds_rd16(c.vp + (I + 3 - 16) * 1024);
  if constexpr (I < 8) {
    if constexpr (PREV) {
      a.s1[8 + I] = EXP2(a.s1[8 + I]);
      a.ps += a.s1[7 + I];
      if constexpr (I < 4) { a.pw[8 + I] = cvtpk(a.s1[2 * I], a.s1[2 * I + 1]); PIN(a.pw[8 + I]); }
      else if constexpr (I != 6) { constexpr int k = I == 7 ? 6 : I; a.pw[8 + k] = cvtpk(a.s1[2 * k], a.s1[2 * k + 1]); PIN(a.pw[8 + k]); }
    }
    if constexpr (I & 1) { if (MODE != M_DRAIN || c.dv) dma16os<(I >> 1) * 1024>(c.m0v, c.voff, sV, c.sov); }
  } else {
    constexpr int i = I - 8;
    if constexpr (MODE == M_STEADY) {
      a.s0[i] = EXP2(a.s0[i]);
      if constexpr (i >= 1) a.ps += a.s0[i - 1];
    }
    if constexpr (i & 1) { if (MODE != M_DRAIN || c.dk) dma16os<(i >> 1) * 1024>(c.m0k, c.voff, sK, c.sok); }
  }
  PIN(a.ps);
  SBAR();
}
template <int I, int MODE> DI void gapB(AttB& a, const StepCtx& c, float& mx0, float& mx1) {
  constexpr bool PREV = MODE != M_FIRST, NEW = MODE == M_FIRST || MODE == M_SWITCH;
  constexpr int ks = I >> 2, db = I & 3;
  if constexpr (PREV) {
    a.o[db] = MFMA(a.fr[I & 3], pfrag(a, ks), a.o[db]);
    if constexpr (I + 3 < 16) a.fr[(I + 3) & 3] = lds_rd16(c.vp + (I + 3) * 1024);
  }
  if constexpr (I + 3 >= 16) a.fr[(I + 3) & 3] = lds_rd16(c.kpn + (I + 3 - 16) * 1024);
  if constexpr (MODE == M_STEADY) {
    if constexpr (I < 8) {
      a.s0[8 + I] = EXP2(a.s0[8 + I]);
      a.ps += a.s0[7 + I];
      if constexpr (I >= 4) { constexpr int k = I - 4; a.pw[k] = cvtpk(a.s0[2 * k], a.s0[2 * k + 1]); PIN(a.pw[k]); }
    } else {
      constexpr int i = I - 8;
      a.s1[i] = EXP2(a.s1[i]);
      if constexpr (i == 0) a.ps += a.s0[15]; else a.ps += a.s1[i - 1];
      if constexpr (I < 12) { constexpr int k = I - 4; a.pw[k] = cvtpk(a.s0[2 * k], a.s0[2 * k + 1]); PIN(a.pw[k]); }
    }
  } else if constexpr (NEW) {
    if constexpr (I < 4) {
      constexpr int r = 4 * I;
      if constexpr (I == 0) { mx0 = fmaxf(fmaxf(a.s0[0], a.s0[1]), a.s0[2]); mx1 = fmaxf(fmaxf(a.s1[0], a.s1[1]), a.s1[2]); mx0 = fmaxf(mx0, a.s0[3]); mx1 = fmaxf(mx1, a.s1[3]); }
      else { mx0 = fmaxf(fmaxf(mx0, a.s0[r]), a.s0[r + 1]); mx1 = fmaxf(fmaxf(mx1, a.s1[r]), a.s1[r + 1]); mx0 = fmaxf(fmaxf(mx0, a.s0[r + 2]), a.s0[r + 3]); mx1 = fmaxf(fmaxf(mx1, a.s1[r + 2]), a.s1[r + 3]); }
      PIN(mx0); PIN(mx1);
    } else {
      if constexpr (I == 4) { mx0 = fmaxf(hmax(fmaxf(mx0, mx1)), -1e30f); a.m = mx0; a.ps = 0.f; }
      if constexpr (I < 12) {
        constexpr int i = 2 * (I - 4);
        a.s0[i] = EXP2(a.s0[i] - mx0); a.s0[i + 1] = EXP2(a.s0[i + 1] - mx0);
        if constexpr (i >= 2) { a.ps += a.s0[i - 2]; a.ps += a.s0[i - 1]; }
      } else {
        constexpr int i = 2 * (I - 12);
        if constexpr (I == 12) { a.ps += a.s0[14]; a.ps += a.s0[15]; }
        a.s1[i] = EXP2(a.s1[i] - mx0); a.s1[i + 1] = EXP2(a.s1[i + 1] - mx0);
        a.s1[8 + i] -= mx0; a.s1[9 + i] -= mx0;
        if constexpr (i >= 2) { a.ps += a.s1[i - 2]; a.ps += a.s1[i - 1]; }
      }
      if constexpr (I >= 8) { constexpr int k = I - 8; if constexpr (2 * k + 1 < 2 * (I - 4)) { a.pw[k] = cvtpk(a.s0[2 * k], a.s0[2 * k + 1]); PIN(a.pw[k]); } }
    }
  }
  PIN(a.ps);
  SBAR();
}
template <int O, int MODE, int... Is> DI void phaseA_(AttB& a, const StepCtx& c, __amdgpu_buffer_rsrc_t sK, __amdgpu_buffer_rsrc_t sV, std::integer_sequence<int, Is...>) { (gapA<O + Is, MODE>(a, c, sK, sV), ...); }
template <int MODE, int... Is> DI void phaseB_(AttB& a, const StepCtx& c, float& mx0, float& mx1, std::integer_sequence<int, Is...>) { (gapB<Is, MODE>(a, c, mx0, mx1), ...); }

struct K2P { char* ws; };
struct AttEnv {
  int grp, wq, lane, hi, l32, n1, L, head; char* lds; unsigned lds0; char* ws;
  __amdgpu_buffer_rsrc_t sK, sV, sQ, sC;
};
DI int tile_of(const AttEnv& e, int x) { return e.grp == 0 ? x : (x < e.n1 ? x : 17 + (x - e.n1)); }
DI void step_top(const AttEnv& e, int s, StepCtx& c) {
  asm volatile("s_waitcnt vmcnt(0)\n\ts_barrier" ::: "memory");
  SBAR();
  const unsigned gb = e.grp * 81920;
  const int k3 = s % 3, k3n = (s + 1) % 3, k3d = (s + 2) % 3;
  c.kp = (lds_cptr)e.lds + gb + k3 * 16384 + e.lane * 16;
  c.kpn = (lds_cptr)e.lds + gb + k3n * 16384 + e.lane * 16;
  c.vp = (lds_cptr)e.lds + gb + 49152 + ((s + 1) & 1) * 16384 + e.lane * 16;
  c.dk = (s + 2 < 17); c.dv = (s < 17);
  c.m0k = rfl(e.lds0 + gb + k3d * 16384 + e.wq * 4096); c.m0v = rfl(e.lds0 + gb + 49152 + (s & 1) * 16384 + e.wq * 4096);
  c.sok = rfl((unsigned)(tile_of(e, s + 2) * 16384 + e.wq * 4096)); c.sov = rfl((unsigned)(tile_of(e, s) * 16384 + e.wq * 4096));
  c.voff = e.lane * 16;
}
DI void dma_all(const AttEnv& e, const StepCtx& c) {
  if (c.dk) {
#pragma unroll
    for (int i = 0; i < 4; ++i) dma16s(c.m0k + i * 1024, c.voff, e.sK, c.sok + i * 1024); }
  if (c.dv) {
#pragma unroll
    for (int i = 0; i < 4; ++i) dma16s(c.m0v + i * 1024, c.voff, e.sV, c.sov + i * 1024); }
}
template <bool HASCUR> DI void check_prev(AttB& a, const AttEnv& e, int qb, int prev_t) {
  if (__builtin_expect(__any(!(a.l + a.ps <= LTHR_V)), 0)) {
    const int rmin = qb * 128 + e.wq * 32;
    rare_fix<HASCUR>(a, e.sK, e.sQ, e.lane * 16, rfl((unsigned)(prev_t * 16384)), rfl((unsigned)(((qb == e.L ? 0 : 4) + e.wq) * 8192)),
                     prev_t * 64 + 4 * e.hi, rmin + e.l32, prev_t * 64 + 63 > rmin);
  }
  a.l += a.ps; a.ps = 0.f;
}
DI void store_block(AttB& a, const AttEnv& e, int qb) {
  const float inv = 1.f / hsum(a.l);
#pragma unroll
  for (int db = 0; db < 4; ++db)
#pragma unroll
    for (int g = 0; g < 2; ++g)
      bst16s(pack8(a.o[db], g, inv), e.sC, e.lane * 16, rfl((unsigned)(((qb * 4 + e.wq) * 256 + db * 2 + g) * 1024)));
}
template <int MODE> DI void att_step(AttB& a, const AttEnv& e, int s, int qb_prev, int qb, bool loadq = false) {
  constexpr bool PREV = MODE != M_FIRST, CUR = MODE != M_DRAIN, NEW = MODE == M_FIRST || MODE == M_SWITCH;
  StepCtx c; step_top(e, s, c);
  if constexpr (NEW) asm volatile("" : "+v"(a.qf[0]), "+v"(a.qf[1]), "+v"(a.qf[2]), "+v"(a.qf[3]), "+v"(a.qf[4]), "+v"(a.qf[5]), "+v"(a.qf[6]), "+v"(a.qf[7]));
  const int t = tile_of(e, s), rmin = qb * 128 + e.wq * 32;
  const bool mask = t * 64 + 63 > rmin;
  if constexpr (MODE == M_FIRST) {
#pragma unroll
    for (int J = 0; J < 3; ++J) a.fr[J] = lds_rd16(c.kp + J * 1024);
  }
  phaseA_<0, MODE>(a, c, e.sK, e.sV, std::make_integer_sequence<int, 8>{});
  if constexpr (PREV) {
    a.ps += a.s1[15]; a.pw[15] = cvtpk(a.s1[14], a.s1[15]);
    check_prev<MODE == M_STEADY>(a, e, qb_prev, tile_of(e, s - 1));
  }
  if constexpr (MODE == M_STEADY) { if (mask) cmask1(a.s0, t * 64 + 4 * e.hi, rmin + e.l32); }
  SBAR();
  phaseA_<8, MODE>(a, c, e.sK, e.sV, std::make_integer_sequence<int, 8>{});
  if constexpr (CUR) {
    if (mask) { if constexpr (NEW) cmask1(a.s0, t * 64 + 4 * e.hi, rmin + e.l32); cmask1(a.s1, t * 64 + 32 + 4 * e.hi, rmin + e.l32); }
  }
  if constexpr (MODE == M_FIRST || MODE == M_STEADY) {
    if (loadq) {
#pragma unroll
      for (int ds = 0; ds < 8; ++ds)
        asm volatile("buffer_load_dwordx4 %0, %1, %2, %3 offen" : "=v"(a.qf[ds]) : "v"(e.lane * 16), "s"(e.sQ), "s"(rfl((unsigned)(e.wq * 8192 + ds * 1024))) : "memory");
    }
  }
  SBAR();
  float mx0 = 0.f, mx1 = 0.f; const float lold = a.l;
  phaseB_<MODE>(a, c, mx0, mx1, std::make_integer_sequence<int, 16>{});
  if constexpr (MODE == M_FIRST || MODE == M_STEADY) {
    if (loadq) asm volatile("s_waitcnt vmcnt(0)" : "+v"(a.qf[0]), "+v"(a.qf[1]), "+v"(a.qf[2]), "+v"(a.qf[3]), "+v"(a.qf[4]), "+v"(a.qf[5]), "+v"(a.qf[6]), "+v"(a.qf[7]) :: "memory");
  }
  if constexpr (NEW) {
    a.ps += a.s1[6];
    if constexpr (MODE == M_SWITCH) { a.l = lold; store_block(a, e, qb_prev); }
#pragma unroll
    for (int db = 0; db < 4; ++db) a.o[db] = f32x16{};
    a.l = 0.f; a.negm = splat16(-a.m); PIN(a.negm);
  }
}

struct AttA { f32x16 o; bf16x8 qf0, qf1; };
#define E4(sc, i) do { sc[i] = EXP2(sc[i]); sc[i + 1] = EXP2(sc[i + 1]); sc[i + 2] = EXP2(sc[i + 2]); sc[i + 3] = EXP2(sc[i + 3]); } while (0)
#define C2(pw, j, sc, i) do { pw[j] = cvtpk(sc[i], sc[i + 1]); pw[j + 1] = cvtpk(sc[i + 2], sc[i + 3]); } while (0)
DI int TA(int g) { return (g * 64) / NPSTEP; }
DI void a_loop(AttA& A, AttA& B, char* lds, int lane, const char* flags, bool poller) {
  const int l32 = lane & 31, hi = lane >> 5;
  const int vrow = l32 < 19 ? l32 : (((l32 >= 20 && l32 <= 27)) ? 0 : 4);
  lds_cptr k0p = (lds_cptr)lds + ARING + lane * 16;
  lds_cptr k1p = (lds_cptr)lds + ARING + 1024 + l32 * 16;
  lds_cptr vp = (lds_cptr)lds + ARING + 1536 + (hi * 19 + vrow) * 16;
  f32x16 sA, sB, tA, tB; u32x4 pa0, pa1, pb0, pb1, qa0, qa1, qb0, qb1;
  bf16x8 k0, k1, v0, v1, w0, w1;
  int g = 0;
#define SLOT(kt) (((kt) % 10) * ATB)
#define LDK(kt) do { const int so_ = SLOT(kt); k0 = lds_rd16(k0p + so_); k1 = lds_rd16(k1p + so_); } while (0)
#define TOPCHK(kt) do { if (g < NPSTEP && TA(g) == (kt)) { asm volatile("s_barrier" ::: "memory"); SBAR(); ++g; } } while (0)
#define DMP(i) do { } while (0)
#define ASTEP(SC, SN, TC, TN, PP0, PP1, PN0, PN1, QP0, QP1, QN0, QN1, kt, VC0, VC1, VN0, VN1, DOQK, DOPV, DOLDK) do { \
    TOPCHK(kt); \
    const int sv_ = SLOT(kt); \
    if (DOQK) { SN = MFMA(k0, A.qf0, f32x16{}); } SBAR(); VN0 = lds_rd16(vp + sv_); VN1 = lds_rd16(vp + sv_ + 608); E4(SC, 0); PIN(SC); DMP(0); SBAR(); \
    if (DOQK) { TN = MFMA(k0, B.qf0, f32x16{}); } SBAR(); E4(TC, 0); PIN(TC); DMP(1); SBAR(); \
    if (DOPV) { A.o = MFMA(VC0, PKB(PP0), A.o); } SBAR(); E4(SC, 4); C2(PN0, 0, SC, 0); PIN(SC); PIN(PN0); DMP(2); SBAR(); \
    if (DOPV) { B.o = MFMA(VC0, PKB(QP0), B.o); } SBAR(); E4(TC, 4); C2(QN0, 0, TC, 0); PIN(TC); PIN(QN0); DMP(3); SBAR(); \
    if (DOQK) { SN = MFMA(k1, A.qf1, SN); } SBAR(); E4(SC, 8); C2(PN0, 2, SC, 4); PIN(SC); PIN(PN0); DMP(4); SBAR(); \
    if (DOQK) { TN = MFMA(k1, B.qf1, TN); } SBAR(); if (DOLDK) LDK((kt) + 2); E4(TC, 8); C2(QN0, 2, TC, 4); PIN(TC); PIN(QN0); DMP(5); SBAR(); \
    if (DOPV) { A.o = MFMA(VC1, PKB(PP1), A.o); } SBAR(); E4(SC, 12); C2(PN1, 0, SC, 8); C2(PN1, 2, SC, 12); PIN(SC); PIN(PN1); DMP(6); SBAR(); \
    if (DOPV) { B.o = MFMA(VC1, PKB(QP1), B.o); } SBAR(); E4(TC, 12); C2(QN1, 0, TC, 8); C2(QN1, 2, TC, 12); PIN(TC); PIN(QN1); DMP(7); SBAR(); } while (0)
  pa0 = u32x4{0,0,0,0}; pa1 = pa0; pb0 = pa0; pb1 = pa0; qa0 = pa0; qa1 = pa0; qb0 = pa0; qb1 = pa0; v0 = bf16x8{}; v1 = v0; w0 = v0; w1 = v0;
  asm volatile("s_barrier" ::: "memory"); SBAR();
  LDK(0); sA = MFMA(k0, A.qf0, f32x16{}); tA = MFMA(k0, B.qf0, f32x16{}); sA = MFMA(k1, A.qf1, sA); tA = MFMA(k1, B.qf1, tA); LDK(1);
  g = 1;
#define TOPCHK0(kt) do { } while (0)
  {
#pragma push_macro("TOPCHK")
#undef TOPCHK
#define TOPCHK(kt) TOPCHK0(kt)
    ASTEP(sA, sB, tA, tB, pb0, pb1, pa0, pa1, qb0, qb1, qa0, qa1, 0, v0, v1, w0, w1, true, false, true);
#pragma pop_macro("TOPCHK")
  }
#pragma unroll 1
  for (int kt = 1; kt < 61; kt += 2) {
    ASTEP(sB, sA, tB, tA, pa0, pa1, pb0, pb1, qa0, qa1, qb0, qb1, kt, w0, w1, v0, v1, true, true, true);
    ASTEP(sA, sB, tA, tB, pb0, pb1, pa0, pa1, qb0, qb1, qa0, qa1, kt + 1, v0, v1, w0, w1, true, true, true);
  }
  if (poller) {
    const __attribute__((address_space(1))) unsigned* fl = (const __attribute__((address_space(1))) unsigned*)(flags + (lane & 7) * 128);
    for (unsigned spin = 0; spin < (1u << 20); ++spin) {
      const unsigned v = __hip_atomic_load(fl, __ATOMIC_RELAXED, __HIP_MEMORY_SCOPE_AGENT);
      if (__all(v == 1u)) break;
      __builtin_amdgcn_s_sleep(2);
    }
    __builtin_amdgcn_fence(__ATOMIC_ACQUIRE, "agent");
  }
  ASTEP(sB, sA, tB, tA, pa0, pa1, pb0, pb1, qa0, qa1, qb0, qb1, 61, w0, w1, v0, v1, true, true, true);
  ASTEP(sA, sB, tA, tB, pb0, pb1, pa0, pa1, qb0, qb1, qa0, qa1, 62, v0, v1, w0, w1, true, true, false);
  ASTEP(sB, sA, tB, tA, pa0, pa1, pb0, pb1, qa0, qa1, qb0, qb1, 63, w0, w1, v0, v1, false, true, false);
  A.o = MFMA(v0, PKB(pb0), A.o); B.o = MFMA(v0, PKB(qb0), B.o); A.o = MFMA(v1, PKB(pb1), A.o); B.o = MFMA(v1, PKB(qb1), B.o);
#undef LDK
#undef ASTEP
#undef TOPCHK
#undef TOPCHK0
#undef DMP
#undef SLOT
}
DI void a_initq(AttA& A, const char* ws, int qrow, int ah, int lane, float km) {
  const int hi = lane >> 5;
  const char* qp = ws + WS_QA + (size_t)qrow * 1152 + ah * 36;
  u32x4 q0; { const unsigned* qq = (const unsigned*)(qp + 16 * hi); q0 = u32x4{qq[0], qq[1], qq[2], qq[3]}; }
  const unsigned q16 = *(const unsigned*)(qp + 32);
  float qs = 0.f;
#pragma unroll
  for (int e = 0; e < 4; ++e) { const float x = __uint_as_float(q0[e] << 16), y = __uint_as_float(q0[e] & 0xFFFF0000u); qs += x * x + y * y; }
  if (hi == 0) { const float x = __uint_as_float(q16 << 16), y = __uint_as_float(q16 & 0xFFFF0000u); qs += x * x + y * y; }
  qs = hsum(qs);
  const float bound = sqrtf(qs * km) * 1.0009765625f + 1e-30f;
  const unsigned bb = (__float_as_uint(bound) + 0xFFFFu) >> 16;
  A.qf0 = __builtin_bit_cast(bf16x8, q0);
  A.qf1 = __builtin_bit_cast(bf16x8, hi == 0 ? u32x4{q16, bb | 0x8000u, 0u, 0u} : u32x4{0u, 0u, 0u, 0u});
  A.o = f32x16{};
}
DI void a_finish(AttA& A, char* ws, int lane, int qrow, int ah) {
  const int l32 = lane & 31, hi = lane >> 5;
  f32x16 o = A.o;
  float l = o[10];
  { auto rr = __builtin_amdgcn_permlane32_swap(__float_as_uint(l), __float_as_uint(l), false, false); l = __uint_as_float(rr[0]); }
  if (__builtin_expect(__any(!(l >= 8.6736174e-19f)), 0)) {
    float m = -1e30f; o = f32x16{};
    bf16x8 qz = A.qf1; qz[2] = 0;
    const int vrow = l32 < 19 ? l32 : 0;
    for (int kt = 0; kt < 64; ++kt) {
      const char* hb = ws + WS_KA + (size_t)kt * ATB;
      const bf16x8 k0 = *(const bf16x8*)(hb + lane * 16), k1 = *(const bf16x8*)(hb + 1024 + l32 * 16);
      const bf16x8 v0 = *(const bf16x8*)(hb + 1536 + (hi * 19 + vrow) * 16), v1 = *(const bf16x8*)(hb + 1536 + 608 + (hi * 19 + vrow) * 16);
      f32x16 sc = MFMA(k0, A.qf0, f32x16{});
      sc = MFMA(k1, qz, sc);
      const float mn = fmaxf(m, hmax(max16(sc))), f = EXP2(m - mn);
      m = mn;
#pragma unroll
      for (int r = 0; r < 16; ++r) { sc[r] = EXP2(sc[r] - mn); o[r] *= f; }
      o = MFMA(v0, pack8n(sc, 0), o);
      o = MFMA(v1, pack8n(sc, 1), o);
    }
    l = o[10];
    auto rr = __builtin_amdgcn_permlane32_swap(__float_as_uint(l), __float_as_uint(l), false, false); l = __uint_as_float(rr[0]);
  }
  const float inv = 1.f / l;
  char* ob = ws + WS_OA + (size_t)(qrow >> 5) * 36 * 1024 + l32 * 16;
  auto st2 = [&](int d, float x, float y) { const int c = ah * 18 + d; __hip_atomic_store((__attribute__((address_space(1))) unsigned*)(ob + (c >> 4) * 1024 + ((c >> 2) & 1) * 512 + (((c >> 3) & 1) * 4 + (c & 3)) * 2), cvtpk(x * inv, y * inv), __ATOMIC_RELAXED, __HIP_MEMORY_SCOPE_AGENT); };
  st2(4 * hi, o[0], o[1]); st2(4 * hi + 2, o[2], o[3]); st2(8 + 4 * hi, o[4], o[5]); st2(10 + 4 * hi, o[6], o[7]);
  if (hi == 0) st2(16, o[8], o[9]);
}

constexpr int K3_WS = 24576;
struct K3C {
  unsigned lds0, w, xvoff, wvoff;
  __amdgpu_buffer_rsrc_t sX, sW;
  lds_cptr wb;
};
DI void xload(bf16x8& d, unsigned voff, __amdgpu_buffer_rsrc_t srd, unsigned soff) {
  asm volatile("buffer_load_dwordx4 %0, %1, %2, %3 offen sc1" : "=v"(d) : "v"(voff), "s"(srd), "s"(soff) : "memory");
}
template <int I> DI void k3_dmaW(const K3C& c, int st) {
  dma16(c.lds0 + (st & 3) * K3_WS + (c.w * 3 + I) * 1024, c.wvoff + I * 128, c.sW, (unsigned)((st * 64 + c.w * 8) * 1152));
}
struct K3F { s16x4 wl[3], wh[3]; };
DI void k3_stage(const K3C& c, f32x16 (&acc)[6], bf16x8& x, K3F& fa, K3F& fb, int st, unsigned xso) {
  const int so = (st & 3) * K3_WS, sn = ((st + 1) & 3) * K3_WS;
#pragma unroll
  for (int m = 0; m < 3; ++m) {
    acc[m] = MFMA(cat(fa.wl[m], fa.wh[m]), x, acc[m]);
    lds_cptr p = c.wb + so + (3 + m) * 512;
    fb.wl[m] = lds_tr(p); fb.wh[m] = lds_tr(p + 3072);
    if (m == 0) k3_dmaW<0>(c, st + 3);
    if (m == 2) k3_dmaW<1>(c, st + 3);
    SBAR();
  }
#pragma unroll
  for (int m = 0; m < 3; ++m) {
    acc[3 + m] = MFMA(cat(fb.wl[m], fb.wh[m]), x, acc[3 + m]);
    lds_cptr p = c.wb + sn + m * 512;
    fa.wl[m] = lds_tr(p); fa.wh[m] = lds_tr(p + 3072);
    if (m == 1) k3_dmaW<2>(c, st + 3);
    SBAR();
  }
  xload(x, c.xvoff, c.sX, xso);
  SBAR();
}
DI void k3_phase(const K1P& p, char* lds) {
  const int tid = threadIdx.x, lane = tid & 63, l32 = lane & 31, hi = lane >> 5;
  const int b = blockIdx.x;
  const bool isB = b < 192;
  K3C c;
  c.lds0 = (unsigned)(size_t)(lds_ptr)lds; c.w = rfl(tid >> 6);
  const int rt = c.w & 1, kq = c.w >> 1;
  const int blk = (lane >> 4) & 1, q4 = (lane & 15) >> 2, p4 = lane & 3;
  const int nst = isB ? 32 : 9;
  c.xvoff = lane * 16;
  { const int sub = lane >> 5, rr = (lane & 31) >> 2, c4 = lane & 3; c.wvoff = (unsigned)(rr * 1152 + (32 * sub + 8 * c4) * 2); }
  c.wb = (lds_cptr)lds + (2 * kq) * 3072 + 32 * blk + 8 * p4 + (4 * hi + q4) * 64;
  const int ntile = isB ? 1 : ((b - 192) < 32 ? 2 : 1);
#pragma unroll 1
  for (int it = 0; it < ntile; ++it) {
    int t, sk = 0;
    if (isB) { const int x = b & 7, i = b >> 3; t = x * 12 + (i >> 1); sk = i & 1; } else t = (b - 192) + 64 * it;
    const int rb = t / 3, cb = t - rb * 3;
    c.sW = isB ? mksrd((const char*)p.wo + (size_t)sk * 2048 * 1152 + cb * 384, 2048 * 1152 - cb * 384) : mksrd((const char*)p.awo + cb * 384, 576 * 1152 - cb * 384);
    {
      const char* xb = isB ? p.ws + WS_CF + ((size_t)(rb * 2 + rt) * 256 + sk * 128) * 1024 : p.ws + WS_OA + (size_t)(rb * 2 + rt) * 36 * 1024;
      c.sX = mksrd(xb + kq * 1024, nst * 4096 - kq * 1024);
    }
    f32x16 acc[6];
#pragma unroll
    for (int ct = 0; ct < 6; ++ct) acc[ct] = f32x16{};
    bf16x8 x0, x1, x2;
    if (it) asm volatile("s_waitcnt lgkmcnt(0)\n\ts_barrier" ::: "memory");
    k3_dmaW<0>(c, 0); k3_dmaW<1>(c, 0); k3_dmaW<2>(c, 0);
    k3_dmaW<0>(c, 1); k3_dmaW<1>(c, 1); k3_dmaW<2>(c, 1);
    k3_dmaW<0>(c, 2); k3_dmaW<1>(c, 2); k3_dmaW<2>(c, 2);
    if (c.w == 0) {
      const int beta = rb >> 1, pj = isB ? (beta < 8 ? beta : 15 - beta) : (rb >> 2);
      const int hd = isB ? 16 * sk + (lane & 15) : (lane & 31);
      const int pb = (hd >> 2) + 8 * ((hd & 3) + 4 * pj);
      const __attribute__((address_space(1))) unsigned* fl = (const __attribute__((address_space(1))) unsigned*)(p.ws + WS_DONE + pb * 128);
      for (unsigned spin = 0; spin < (1u << 20); ++spin) {
        const unsigned v = __hip_atomic_load(fl, __ATOMIC_RELAXED, __HIP_MEMORY_SCOPE_AGENT);
        if (__all(v == 1u)) break;
        __builtin_amdgcn_s_sleep(2);
      }
      __builtin_amdgcn_fence(__ATOMIC_ACQUIRE, "wavefront");
    }
    asm volatile("s_waitcnt vmcnt(0)\n\ts_barrier" ::: "memory");
    xload(x0, c.xvoff, c.sX, 0);
    xload(x1, c.xvoff, c.sX, 4096);
    (void)x2;
    K3F fa, fb;
#define K3_WAIT(N, X) asm volatile("s_waitcnt vmcnt(" #N ")\n\ts_barrier" : "+v"(X) :: "memory")
    K3_WAIT(1, x0); SBAR();
#pragma unroll
    for (int m = 0; m < 3; ++m) { fa.wl[m] = lds_tr(c.wb + m * 512); fa.wh[m] = lds_tr(c.wb + m * 512 + 3072); }
    SBAR();
    int st = 0;
    for (; st + 1 < nst; st += 2) {
      k3_stage(c, acc, x0, fa, fb, st, (unsigned)((st + 2) * 4096));
      K3_WAIT(4, x1); SBAR();
      k3_stage(c, acc, x1, fa, fb, st + 1, (unsigned)((st + 3) * 4096));
      K3_WAIT(4, x0); SBAR();
    }
    if (st < nst) k3_stage(c, acc, x0, fa, fb, st, (unsigned)((st + 2) * 4096));
    asm volatile("s_waitcnt vmcnt(0) lgkmcnt(0)\n\ts_barrier" : "+v"(x0), "+v"(x1) :: "memory");
    f32x4 h[12];
    {
      const bool up = (kq & 2) != 0;
      char* mine = lds + c.w * 12288; const char* theirs = lds + (c.w ^ 4) * 12288;
#pragma unroll
      for (int c3 = 0; c3 < 3; ++c3)
#pragma unroll
        for (int g = 0; g < 4; ++g) {
          const f32x16& give = up ? acc[c3] : acc[3 + c3];
          *(f32x4*)(mine + (c3 * 4 + g) * 1024 + lane * 16) = f32x4{give[4 * g], give[4 * g + 1], give[4 * g + 2], give[4 * g + 3]};
        }
      __syncthreads();
#pragma unroll
      for (int c3 = 0; c3 < 3; ++c3)
#pragma unroll
        for (int g = 0; g < 4; ++g) {
          const f32x16& keep = up ? acc[3 + c3] : acc[c3];
          h[c3 * 4 + g] = f32x4{keep[4 * g], keep[4 * g + 1], keep[4 * g + 2], keep[4 * g + 3]} + *(const f32x4*)(theirs + (c3 * 4 + g) * 1024 + lane * 16);
        }
      __syncthreads();
    }
    f32x4 q[6];
    {
      const bool up = (kq & 1) != 0;
      char* mine = lds + c.w * 6144; const char* theirs = lds + (c.w ^ 2) * 6144;
#pragma unroll
      for (int i = 0; i < 6; ++i) *(f32x4*)(mine + i * 1024 + lane * 16) = up ? h[i] : h[6 + i];
      __syncthreads();
#pragma unroll
      for (int i = 0; i < 6; ++i) q[i] = (up ? h[6 + i] : h[i]) + *(const f32x4*)(theirs + i * 1024 + lane * 16);
    }
    const int it0 = (kq & 2) * 6 + (kq & 1) * 6;
    auto store_out = [&](int coloff) {
      const int row = rb * 64 + rt * 32 + l32;
#pragma unroll
      for (int i = 0; i < 6; ++i) { const int item = it0 + i, ct = item >> 2, g = item & 3;
        *(u32x2*)((char*)p.out + (size_t)row * 2304 + coloff + (cb * 192 + ct * 32 + 8 * g + 4 * hi) * 2) = u32x2{cvtpk(q[i][0], q[i][1]), cvtpk(q[i][2], q[i][3])}; } };
    if (!isB) store_out(0);
    else {
      char* slab = p.ws + WS_PART + (size_t)(t * 2) * 12288;
      if (rt != sk) {
        const __amdgpu_buffer_rsrc_t sP = mksrd(slab + sk * 12288 + kq * 3072, 3072);
#pragma unroll
        for (int i = 0; i < 3; ++i)
          __builtin_amdgcn_raw_buffer_store_b128(u32x4{cvtpk(q[2 * i][0], q[2 * i][1]), cvtpk(q[2 * i][2], q[2 * i][3]), cvtpk(q[2 * i + 1][0], q[2 * i + 1][1]), cvtpk(q[2 * i + 1][2], q[2 * i + 1][3])}, sP, lane * 16 + i * 1024, 0, 16);
        asm volatile("s_waitcnt vmcnt(0)" ::: "memory");
        if (lane == 0) __hip_atomic_fetch_add((__attribute__((address_space(1))) unsigned*)(p.ws + WS_CNT + (t * 2 + sk) * 128), 1u, __ATOMIC_RELAXED, __HIP_MEMORY_SCOPE_AGENT);
      } else {
        const __attribute__((address_space(1))) unsigned* fl = (const __attribute__((address_space(1))) unsigned*)(p.ws + WS_CNT + (t * 2 + (sk ^ 1)) * 128);
        for (unsigned spin = 0; spin < (1u << 20); ++spin) {
          if (__hip_atomic_load(fl, __ATOMIC_RELAXED, __HIP_MEMORY_SCOPE_AGENT) >= 4u) break;
          __builtin_amdgcn_s_sleep(1);
        }
        __builtin_amdgcn_fence(__ATOMIC_ACQUIRE, "wavefront");
        const __amdgpu_buffer_rsrc_t sP = mksrd(slab + (sk ^ 1) * 12288 + kq * 3072, 3072);
#pragma unroll
        for (int i = 0; i < 3; ++i) {
          const u32x4 y = __builtin_bit_cast(u32x4, __builtin_amdgcn_raw_buffer_load_b128(sP, lane * 16 + i * 1024, 0, 16));
          q[2 * i][0] += __uint_as_float(y[0] << 16); q[2 * i][1] += __uint_as_float(y[0] & 0xFFFF0000u); q[2 * i][2] += __uint_as_float(y[1] << 16); q[2 * i][3] += __uint_as_float(y[1] & 0xFFFF0000u);
          q[2 * i + 1][0] += __uint_as_float(y[2] << 16); q[2 * i + 1][1] += __uint_as_float(y[2] & 0xFFFF0000u); q[2 * i + 1][2] += __uint_as_float(y[3] << 16); q[2 * i + 1][3] += __uint_as_float(y[3] & 0xFFFF0000u);
        }
        store_out(1152);
      }
    }
  }
}

__global__ __launch_bounds__(512, 2) void kb_main(K1P p) {
  extern __shared__ __attribute__((aligned(16))) char lds[];
  const int tid = threadIdx.x, lane = tid & 63, l32 = lane & 31, hi = lane >> 5;
  const int b = blockIdx.x, xcd = b & 7, ii = b >> 3, head = xcd * 4 + (ii & 3), j = ii >> 2;
  const int w = rfl(tid >> 6);
  const unsigned lds0 = (unsigned)(size_t)(lds_ptr)lds, voff = lane * 16;
  const int grp = w >> 2, wq = w & 3, pp = j;
  char* flagw = p.ws + WS_FLAG + (head * 8 + j) * 128;
  AttB a;
  if (grp == 0) {
    AttA A, B;
    float km = *(const float*)(p.ws + WS_KMAX + lane * 4);
#pragma unroll
    for (int o = 32; o >= 1; o >>= 1) km = fmaxf(km, __shfl_xor(km, o));
    const int aqrow = j * 256 + wq * 64 + l32;
    a_initq(A, p.ws, aqrow, head, lane, km); a_initq(B, p.ws, aqrow + 32, head, lane, km);
    PIN(A.qf0); PIN(A.qf1); PIN(B.qf0); PIN(B.qf1);
    unsigned wv[7] = {0, 0, 0, 0, 0, 0, 0};
    if (WARM) {
      const char* src[7] = {(const char*)p.cx, (const char*)p.wuk, (const char*)p.wuv, (const char*)p.bx, (const char*)p.wq, (const char*)p.wo, (const char*)p.awo};
      const unsigned nline[7] = {2048u * 1024 / 128, 512u * 8192 / 128, 512u * 8192 / 128, 2048u * 1152 / 128, 576u * 8192 / 128, 4096u * 1152 / 128, 576u * 1152 / 128};
#pragma unroll
      for (int a7 = 0; a7 < 7; ++a7) {
        const unsigned i = (unsigned)(b * 192 + tid);
        if (tid < 192 && i < nline[a7]) wv[a7] = *(const unsigned*)(src[a7] + (size_t)i * 128);
      }
    }
    a_loop(A, B, lds, lane, p.ws + WS_FLAG + head * 8 * 128, w == 0);
    if ((wv[0] ^ wv[1] ^ wv[2] ^ wv[3] ^ wv[4] ^ wv[5] ^ wv[6]) == 0x9E3779B9u) *(unsigned*)(p.ws + WS_FLAG + 256 * 128 + 64) = 1u;

    a_finish(A, p.ws, lane, aqrow, head); a_finish(B, p.ws, lane, aqrow + 32, head);
    asm volatile("s_waitcnt vmcnt(0) lgkmcnt(0)\n\ts_barrier" ::: "memory");
  } else {
    __builtin_amdgcn_s_setprio(PPRIO);
    PG c;
    const int gr = wq >> 1, gc = wq & 1;
    c.lds0 = lds0; c.aw = wq; c.voff = voff;
    { const int r = lane >> 3, chp = lane & 7;
#pragma unroll
      for (int par = 0; par < 2; ++par) { const int ch = chp ^ ((4 * par + (r >> 1)) & 7); c.xoC[par] = (unsigned)(r * 1024 + ch * 16); c.xoX[par] = (unsigned)(r * 1152 + ch * 16); }
      c.woff = (unsigned)(((lane & 31) >> 2) * 8192 + (32 * (lane >> 5) + 8 * (lane & 3)) * 2); }
    c.sC = mksrd((const char*)p.cx + (size_t)j * 256 * 1024, 256 * 1024);
    c.sX = mksrd((const char*)p.bx, 2048 * 1152);
    c.rowL = (unsigned)((15 - j) * 128 * 1152); c.rowS = (unsigned)(j * 128 * 1152);
    c.sUK = mksrd((const char*)p.wuk + head * 256, 512 * 8192 - head * 256);
    c.sUV = mksrd((const char*)p.wuv + head * 256, 512 * 8192 - head * 256);
    c.sWQ = mksrd((const char*)p.wq + head * 256, 576 * 8192 - head * 256);
    c.sA = mksrd(p.ws + WS_KA, 64 * ATB);
    {
      auto da = [&](int q) { if (q > 14) q = 14; dma16(rfl(lds0 + ARING + (q / 3) * ATB + (q % 3) * 1024), voff, c.sA, rfl((unsigned)((q / 3) * ATB + (q % 3) * 1024))); };
      auto dw = [&](int st) { const unsigned m0 = rfl(lds0 + WRING + st * WSLOT + wq * 2048), so = rfl((unsigned)((st * 32 + wq * 8) * 8192)); dma16(m0, c.woff, c.sUK, so); dma16(m0 + 1024, c.woff + 128, c.sUK, so); };
      auto dx = [&](int T) {
#pragma unroll
        for (int half = 0; half < 2; ++half)
#pragma unroll
          for (int i = 0; i < 4; ++i)
            dma16(rfl(lds0 + T * XSLOT + (16 * half + 4 * wq + i) * 1024), c.xoC[i & 1], c.sC, rfl((unsigned)((128 * half + 32 * wq + 8 * i) * 1024 + T * 128))); };
      { const int q0 = 3 * wq; da(q0 < 9 ? q0 : 0); da(q0 + 1 < 9 ? q0 + 1 : 1); da(q0 + 2 < 9 ? q0 + 2 : 2); }
      dw(0); dx(0); dw(1); dw(2); dx(1);
      da(9 + 2 * wq); da(10 + 2 * wq);
    }
    const int blk = (lane >> 4) & 1, q4 = (lane & 15) >> 2, p4 = lane & 3;
#pragma unroll
    for (int s = 0; s < 4; ++s) c.xb[s] = (lds_cptr)lds + (gr * 4) * 4096 + 128 * l32 + 16 * ((2 * s + hi) ^ ((l32 >> 1) & 7));
    c.wb = (lds_cptr)lds + WRING + (2 * gc) * 512 + 2048 * hi + 64 * q4 + 32 * blk + 8 * p4;
    f32x16 acc[4][2]; FragKV fa, fb; bf16x8 kvp[16]; KVSt ks; ks.voff = voff;
#define ZACC() do { _Pragma("unroll") for (int r = 0; r < 4; ++r) { acc[r][0] = f32x16{}; acc[r][1] = f32x16{}; } } while (0)
#define PACKKV() do { _Pragma("unroll") for (int r = 0; r < 4; ++r) _Pragma("unroll") for (int ct = 0; ct < 2; ++ct) _Pragma("unroll") for (int g8 = 0; g8 < 2; ++g8) kvp[r * 4 + ct * 2 + g8] = pack8n(acc[r][ct], g8); SBAR(); } while (0)
#pragma unroll
    for (int i = 0; i < 16; ++i) kvp[i] = bf16x8{};
    ks.srd = mksrd(p.ws + WS_KF + ((size_t)(head * 64 + 8 * j + 4 * gr) * 8 + 4 * gc) * 1024, 65536);
    ZACC();
    GLoop<0, 0, 2>::run(c, acc, fa, fb, kvp, ks, flagw);
    GLoop<0, 2, 16>::run(c, acc, fa, fb, kvp, ks, flagw);
    PACKKV(); ZACC();
    GLoop<1, 16, 32>::run(c, acc, fa, fb, kvp, ks, flagw);
    PACKKV(); ZACC();
    ks.srd = mksrd(p.ws + WS_VF + ((size_t)(head * 32 + 4 * j + 2 * gr) * 16 + 2 * gc) * 1024, 65536);
    GLoop<2, 32, 50>::run(c, acc, fa, fb, kvp, ks, flagw);
    __builtin_amdgcn_s_setprio(0);
    asm volatile("s_waitcnt vmcnt(0) lgkmcnt(0)\n\ts_barrier" ::: "memory");
#pragma unroll
    for (int r = 0; r < 4; ++r)
#pragma unroll
      for (int ct = 0; ct < 2; ++ct)
#pragma unroll
        for (int g8 = 0; g8 < 2; ++g8) {
          const int fi = (4 * gr + r) * 8 + 2 * (2 * gc + ct) + g8;
          *(bf16x8*)(lds + fi * 1024 + lane * 16) = pack8(acc[r][ct], g8, CB);
        }
  }
  {
    const __amdgpu_buffer_rsrc_t sQLw = mksrd(p.ws + WS_QF + (size_t)b * 65536, 65536);
    asm volatile("s_waitcnt lgkmcnt(0)\n\ts_barrier" ::: "memory");
#pragma unroll
    for (int ds = 0; ds < 8; ++ds) a.qf[ds] = lds_rd16((lds_cptr)lds + ((grp * 4 + wq) * 8 + ds) * 1024 + lane * 16);
    if (w == 0) asm volatile("s_waitcnt vmcnt(0)" ::: "memory");
    asm volatile("s_waitcnt lgkmcnt(0)\n\ts_barrier" ::: "memory");
#pragma unroll
    for (int ds = 0; ds < 8; ++ds) bst16(a.qf[ds], sQLw, voff, (unsigned)(((grp * 4 + wq) * 8 + ds) * 1024));
  }
  const int L = 15 - pp, n1 = 2 * pp + 2;
  {
  AttEnv e; e.grp = grp; e.wq = wq; e.lane = lane; e.hi = hi; e.l32 = l32; e.n1 = n1; e.L = L; e.head = head; e.lds = lds; e.lds0 = lds0; e.ws = p.ws;
  e.sK = mksrd(p.ws + WS_KF + (size_t)head * S * 256, S * 256);
  e.sV = mksrd(p.ws + WS_VF + (size_t)head * S * 256, S * 256);
  e.sQ = mksrd(p.ws + WS_QF + (size_t)b * 65536, 65536);
  e.sC = mksrd(p.ws + WS_CF + (size_t)head * 8192, 64 * 256 * 1024 - head * 8192);
  int qb = grp == 0 ? L : pp;
  {
    const unsigned gb = grp * 81920;
#pragma unroll
    for (int t = 0; t < 2; ++t)
#pragma unroll
      for (int i = 0; i < 4; ++i) dma16s(rfl(lds0 + gb + t * 16384 + wq * 4096 + i * 1024), voff, e.sK, rfl((unsigned)(t * 16384 + wq * 4096 + i * 1024)));
  }
  if (grp == BPRIO_G) __builtin_amdgcn_s_setprio(BPRIO);
  {
    const int send0 = grp == 0 ? 17 : n1;
    const int rmin0 = qb * 128 + wq * 32;
    int nt0 = ((rmin0 + 31) >> 6) + 1; if (nt0 > send0) nt0 = send0;
    int nt1 = 0;
    if (grp == 1) { const int rminL = L * 128 + wq * 32; nt1 = ((rminL + 31) >> 6) - 17 + 1; if (nt1 > 17 - n1) nt1 = 17 - n1; if (nt1 < 0) nt1 = 0; }
    a.l = 0.f; a.ps = 0.f; a.m = -1e30f;
#pragma unroll
    for (int db = 0; db < 4; ++db) a.o[db] = f32x16{};
    int s = 0; bool carry = false; int qprev = qb;
    const int nseg = grp == 0 ? 1 : 2;
#pragma unroll 1
    for (int seg = 0; seg < nseg; ++seg) {
      const int nt = seg == 0 ? nt0 : nt1;
      const int send = seg == 0 ? send0 : 17;
      const bool shortblk = grp == 1 && seg == 0;
      if (nt > 0) {
        const int slast = s + nt;
        if (carry) att_step<M_SWITCH>(a, e, s, qprev, qb); else att_step<M_FIRST>(a, e, s, qb, qb, shortblk && nt == 1);
        for (++s; s < slast; ++s) att_step<M_STEADY>(a, e, s, qb, qb, shortblk && s == slast - 1);
        carry = shortblk && s == send && nt1 > 0;
        if (!carry) {
          att_step<M_DRAIN>(a, e, s, qb, qb); ++s;
          if (shortblk) {
            store_block(a, e, qb);
#pragma unroll
            for (int db = 0; db < 4; ++db) a.o[db] = f32x16{};
            a.l = 0.f; a.ps = 0.f; a.m = -1e30f;
          }
        }
      }
      for (; s < send; ++s) { StepCtx sc; step_top(e, s, sc); dma_all(e, sc); }
      qprev = qb; qb = L;
    }
    for (; s < 18; ++s) { StepCtx sc; step_top(e, s, sc); dma_all(e, sc); }
  }
  asm volatile("s_waitcnt vmcnt(0) lgkmcnt(0)\n\ts_barrier" ::: "memory");
  {
    const float lt = hsum(a.l);
    char* xb = lds + w * 8192;
    float* st = (float*)(lds + 65536) + w * 128;
#pragma unroll
    for (int i = 0; i < 2; ++i)
#pragma unroll
      for (int q = 0; q < 4; ++q) {
        const f32x16& o = grp == 0 ? a.o[2 + i] : a.o[i];
        *(f32x4*)(xb + (i * 4 + q) * 1024 + lane * 16) = f32x4{o[4 * q], o[4 * q + 1], o[4 * q + 2], o[4 * q + 3]};
      }
    st[lane] = a.m; st[64 + lane] = lt;
    __syncthreads();
    const int ow = w ^ 4;
    const char* yb = lds + ow * 8192; const float* so = (const float*)(lds + 65536) + ow * 128;
    const float m2 = so[lane], l2 = so[64 + lane];
    const float mn = fmaxf(a.m, m2), f1 = EXP2(a.m - mn), f2 = EXP2(m2 - mn);
    const float inv = 1.f / (lt * f1 + l2 * f2);
    const float g1 = f1 * inv, g2 = f2 * inv;
#pragma unroll
    for (int i = 0; i < 2; ++i) {
      f32x16 o = grp == 0 ? a.o[i] : a.o[2 + i];
#pragma unroll
      for (int q = 0; q < 4; ++q) { const f32x4 y = *(const f32x4*)(yb + (i * 4 + q) * 1024 + lane * 16);
#pragma unroll
        for (int e = 0; e < 4; ++e) o[4 * q + e] = o[4 * q + e] * g1 + y[e] * g2; }
      const int db = (grp == 0 ? 0 : 2) + i;
#pragma unroll
      for (int g = 0; g < 2; ++g)
        bst16s(pack8n(o, g), e.sC, voff, rfl((unsigned)(((L * 4 + wq) * 256 + db * 2 + g) * 1024)));
    }
  }
  }
  __builtin_amdgcn_s_setprio(0);
  asm volatile("s_waitcnt vmcnt(0) lgkmcnt(0)\n\ts_barrier" ::: "memory");
  if (tid == 0) __hip_atomic_store((__attribute__((address_space(1))) unsigned*)(p.ws + WS_DONE + b * 128), 1u, __ATOMIC_RELAXED, __HIP_MEMORY_SCOPE_AGENT);
  k3_phase(p, lds);
}

extern "C" void kernel_launch(void* const* d_in, const int* in_sizes, int n_in, void* d_out, int out_size, void* d_ws, size_t ws_size, hipStream_t stream) {
  (void)hipFuncSetAttribute((const void*)k0_aproj, hipFuncAttributeMaxDynamicSharedMemorySize, 147456);
  (void)hipFuncSetAttribute((const void*)kb_main, hipFuncAttributeMaxDynamicSharedMemorySize, 163840);
  K1P p1; p1.cx = d_in[6]; p1.bx = d_in[5]; p1.wuk = d_in[8]; p1.wuv = d_in[9]; p1.wq = d_in[7];
  p1.ax = d_in[0]; p1.awq = d_in[1]; p1.awk = d_in[2]; p1.awv = d_in[3]; p1.wo = d_in[10]; p1.awo = d_in[4]; p1.out = d_out; p1.ws = (char*)d_ws;
  k0_aproj<<<256, 512, 147456, stream>>>(p1);
  kb_main<<<256, 512, 163840, stream>>>(p1);
}
```
